```python
import torch

def _softmax(x, axis=-1):
    return torch.softmax(x, dim=axis)
def _gelu(x):
    return torch.nn.functional.gelu(x, approximate="tanh")
def _silu(x):
    return torch.nn.functional.silu(x)
def _sigmoid(x):
    return torch.sigmoid(x)
def _layernorm(x, axis=-1, eps=1e-5):
    m = x.mean(dim=axis, keepdim=True)
    v = x.var(dim=axis, keepdim=True, unbiased=False)
    return (x - m) / torch.sqrt(v + eps)
def _scatter_add(tgt, idx, src, axis=0):
    return tgt.clone().index_add_(axis, idx, src)
def _segment_sum(src, seg, n):
    out = torch.zeros((n,) + src.shape[1:], dtype=src.dtype)
    return out.index_add_(0, seg, src)
def _pad(x, pw):
    flat = tuple(v for pair in reversed(pw) for v in pair)
    return torch.nn.functional.pad(x, flat)

kernel_name = 'sequential_preLN__ssm_conv_h3__swiglu_ffn__prompt'

def setup_inputs():

    _w = (lambda a: (lambda t: t.to(torch.bfloat16) if t.is_floating_point() else t)(torch.as_tensor(a)))
    def _A_setup():
        B, S, D = 1, 2048, 512
        ks = 4
        return {
            "q": _w((lambda *s: torch.randn(*s, dtype=torch.bfloat16) * 0.05)(B, S, D)),
            "k": _w((lambda *s: torch.randn(*s, dtype=torch.bfloat16) * 0.4)(B, S, D)),
            "v": _w((lambda *s: torch.randn(*s, dtype=torch.bfloat16) * 0.4)(B, S, D)),
            "shift_k": _w((lambda *s: torch.randn(*s, dtype=torch.bfloat16) * 0.4)(D, ks) * 0.5),
            "ssm_k": _w((lambda *s: torch.randn(*s, dtype=torch.bfloat16) * 0.4)(D, S) * (1.0 / S ** 0.5)),
            "ssm_D": _w((lambda *s: torch.randn(*s, dtype=torch.bfloat16) * 0.4)(D)),
        }
    _A_in = _A_setup()

    def _B_setup():
        B, S, D, F = 1, 2048, 512, 1792
        return {
            "x": _w((lambda *s: torch.randn(*s, dtype=torch.bfloat16) * 0.05)(B, S, D)),
            "w_gate": _w((lambda *s: torch.randn(*s, dtype=torch.bfloat16) * 0.05)(D, F)),
            "w_up": _w((lambda *s: torch.randn(*s, dtype=torch.bfloat16) * 0.05)(D, F)),
            "w_down": _w((lambda *s: torch.randn(*s, dtype=torch.bfloat16) * 0.05)(F, D)),
        }
    _B_in = _B_setup()

    return {
        "A_q": _A_in["q"],
        "A_k": _A_in["k"],
        "A_v": _A_in["v"],
        "A_shift_k": _A_in["shift_k"],
        "A_ssm_k": _A_in["ssm_k"],
        "A_ssm_D": _A_in["ssm_D"],
        "B_x": _B_in["x"],
        "B_w_gate": _B_in["w_gate"],
        "B_w_up": _B_in["w_up"],
        "B_w_down": _B_in["w_down"],
    }

def reference(A_q, A_k, A_v, A_shift_k, A_ssm_k, A_ssm_D, B_x, B_w_gate, B_w_up, B_w_down):
    def _A_ref(q, k, v, shift_k, ssm_k, ssm_D):
        B, S, D = q.shape
        ks = shift_k.shape[-1]
        k_pad = _pad(k, ((0, 0), (ks - 1, 0), (0, 0)))
        k_shift = torch.zeros_like(k)
        for t in range(ks):
            k_shift = k_shift + k_pad[:, t:t + S, :] * shift_k[:, ks - 1 - t][None, None, :]
        kv = k_shift * v
        L = 2 * S
        kv_pad = _pad(kv, ((0, 0), (0, S), (0, 0)))
        kf_pad = _pad(ssm_k, ((0, 0), (0, S)))
        kv_f = (lambda x, axes=(-2,-1): torch.fft.rfft2(x.float(), dim=axes))(kv_pad[:, None, :, :], axes=(1, 2))
        kf_f = (lambda x, axes=(-2,-1): torch.fft.rfft2(x.float(), dim=axes))(kf_pad[:, None, :], axes=(1, 2))
        kf_f = (lambda x, *d: x.permute(*d))(kf_f, 1, 2, 0)
        y_f = kv_f * kf_f[None, :, :, :]
        y = (lambda x, s, axes=(-2,-1): torch.fft.irfft2(x, s=s, dim=axes).to(torch.bfloat16))(y_f, (1, L), axes=(1, 2))[:, 0, :S, :]
        y = y + kv * ssm_D[None, None, :]
        return q * y

    def _B_ref(x, w_gate, w_up, w_down):
        g = _silu(x @ w_gate)
        u = x @ w_up
        return (g * u) @ w_down

    x = A_q
    h = x + _A_ref(_layernorm(x, axis=-1), A_k, A_v, A_shift_k, A_ssm_k, A_ssm_D)
    h2 = h + _B_ref(_layernorm(h, axis=-1), B_w_gate, B_w_up, B_w_down)
    return h2
```

```cpp
#include <hip/hip_runtime.h>
#include <hip/hip_bf16.h>
#include <stdint.h>

namespace naive {
typedef unsigned short u16;
constexpr int S = 2048, D = 512, F = 1792;
__device__ __forceinline__ float bf2f(u16 b) { return __builtin_bit_cast(float, (unsigned)b << 16); }
__device__ __forceinline__ u16 f2bf(float f) { return __builtin_bit_cast(u16, (__bf16)f); }
__device__ __forceinline__ float rb(float f) { return bf2f(f2bf(f)); }

__global__ void k_kv(const u16* k, const u16* v, const u16* sk, u16* kv) {
  int i = blockIdx.x * 256 + threadIdx.x; int s = i / D, c = i % D;
  float acc = 0.f;
  for (int t = 0; t < 4; ++t) {
    int ss = s + t - 3;
    float kk = ss >= 0 ? bf2f(k[ss * D + c]) : 0.f;
    acc = rb(acc + rb(kk * bf2f(sk[c * 4 + 3 - t])));
  }
  kv[i] = f2bf(acc * bf2f(v[i]));
}
__global__ void k_ln(const u16* x, u16* o) {
  int s = blockIdx.x; int t = threadIdx.x;
  float v[8]; float sum = 0.f;
  for (int j = 0; j < 8; ++j) { v[j] = bf2f(x[s * D + t * 8 + j]); sum += v[j]; }
  for (int m = 32; m >= 1; m >>= 1) sum += __shfl_xor(sum, m);
  float mean = sum / D; float q = 0.f;
  for (int j = 0; j < 8; ++j) { float d = v[j] - mean; q += d * d; }
  for (int m = 32; m >= 1; m >>= 1) q += __shfl_xor(q, m);
  float mb = rb(mean), vb = rb(q / D);
  float sd = rb(sqrtf(rb(vb + 1e-5f)));
  for (int j = 0; j < 8; ++j) o[s * D + t * 8 + j] = f2bf(rb(v[j] - mb) / sd);
}
__global__ void k_conv(const u16* kv, const u16* sk, const u16* sD, const u16* x, const u16* ln1, u16* h) {
  int i = blockIdx.x * 256 + threadIdx.x; int s = i / D, c = i % D;
  float acc = 0.f;
  for (int t = 0; t <= s; ++t) acc += bf2f(sk[c * S + t]) * bf2f(kv[(s - t) * D + c]);
  float y = rb(rb(acc) + rb(bf2f(kv[i]) * bf2f(sD[c])));
  h[i] = f2bf(bf2f(x[i]) + rb(bf2f(ln1[i]) * y));
}
__global__ void k_ffn1(const u16* a, const u16* wg, const u16* wu, u16* act) {
  int i = blockIdx.x * 256 + threadIdx.x; int s = i / F, f = i % F;
  float g = 0.f, u = 0.f;
  for (int k = 0; k < D; ++k) { float av = bf2f(a[s * D + k]); g += av * bf2f(wg[k * F + f]); u += av * bf2f(wu[k * F + f]); }
  g = rb(g); u = rb(u);
  float sg = rb(g / (1.f + expf(-g)));
  act[i] = f2bf(sg * u);
}
__global__ void k_ffn2(const u16* act, const u16* wd, const u16* h, u16* out) {
  int i = blockIdx.x * 256 + threadIdx.x; int s = i / D, c = i % D;
  float acc = 0.f;
  for (int k = 0; k < F; ++k) acc += bf2f(act[s * F + k]) * bf2f(wd[k * D + c]);
  out[i] = f2bf(bf2f(h[i]) + rb(acc));
}
inline void run(void* const* d_in, void* d_out, void* d_ws, hipStream_t stream) {
  const u16* q = (const u16*)d_in[0]; const u16* k = (const u16*)d_in[1]; const u16* v = (const u16*)d_in[2];
  const u16* shk = (const u16*)d_in[3]; const u16* ssk = (const u16*)d_in[4]; const u16* sD = (const u16*)d_in[5];
  const u16* wg = (const u16*)d_in[7]; const u16* wu = (const u16*)d_in[8]; const u16* wd = (const u16*)d_in[9];
  u16* ws = (u16*)d_ws; u16* kv = ws; u16* ln1 = ws + S * D; u16* h = ws + 2 * S * D; u16* ln2 = ws + 3 * S * D; u16* act = ws + 4 * S * D;
  k_kv<<<S * D / 256, 256, 0, stream>>>(k, v, shk, kv);
  k_ln<<<S, 64, 0, stream>>>(q, ln1);
  k_conv<<<S * D / 256, 256, 0, stream>>>(kv, ssk, sD, q, ln1, h);
  k_ln<<<S, 64, 0, stream>>>(h, ln2);
  k_ffn1<<<S * F / 256, 256, 0, stream>>>(ln2, wg, wu, act);
  k_ffn2<<<S * D / 256, 256, 0, stream>>>(act, wd, h, (u16*)d_out);
}
}
extern "C" void kernel_launch(void* const* d_in, const int* in_sizes, int n_in, void* d_out, int out_size, void* d_ws, size_t ws_size, hipStream_t stream) {
  naive::run(d_in, d_out, d_ws, stream);
}
```

```cpp
#include <hip/hip_runtime.h>
#include <stdint.h>
#define PFD 5

namespace fk {
typedef unsigned u32;
typedef unsigned short u16;
typedef unsigned long long u64;
typedef u32 v4u __attribute__((ext_vector_type(4)));
typedef u32 v2u __attribute__((ext_vector_type(2)));
typedef float f32x4 __attribute__((ext_vector_type(4)));
typedef float f32x2 __attribute__((ext_vector_type(2)));
typedef short s16x4 __attribute__((ext_vector_type(4)));
typedef short s16x8 __attribute__((ext_vector_type(8)));
typedef __bf16 bf2 __attribute__((ext_vector_type(2)));
#define LAS __attribute__((address_space(3)))
#define GAS __attribute__((address_space(1)))
#define RLX __ATOMIC_RELAXED, __HIP_MEMORY_SCOPE_AGENT
#define SB() __builtin_amdgcn_sched_barrier(0)
#define WAITV(n) asm volatile("s_waitcnt vmcnt(%0)" ::"n"(n) : "memory")
#define BAR() do { asm volatile("s_waitcnt lgkmcnt(0)" ::: "memory"); __builtin_amdgcn_s_barrier(); asm volatile("" ::: "memory"); } while (0)

constexpr int S = 2048, D = 512, F = 1792;
constexpr u32 O_EP = 0, O_F1 = 4096, O_C2 = 8192, O_F3 = 32768, O_ST1 = 65536, O_ST2 = 131072;
constexpr u32 O_KVT = 1u << 20, O_QT = 3u << 20, O_HT = 5u << 20, O_W2T = 7u << 20, O_ACT = 9u << 20, O_W1S = 17u << 20, WS_BYTES = 21u << 20;
constexpr u32 O_DIAG = 24u << 20;
constexpr u32 L_WG = 0, L_WU = 57344, L_R = 114688, L_K = L_R, L_V = L_R + 9216, L_Q = L_R + 17408, L_TZ = 36864, TZ_S = 4160, TZ_C = L_WU, L_XR = L_R + 33280, XR_S = 4608,
              L_S1 = L_R, L_EX = L_R + 16384, LDS_BYTES = 163840;
constexpr u32 SPIN_CAP = 1u << 18;
constexpr int SC1 = 16;

struct Args { const u16 *q, *k, *v, *shk, *ssk, *ssd, *wg, *wu, *wd; u16* out; char* ws; };

__device__ __forceinline__ u32 f2u(float x) { return __builtin_bit_cast(u32, x); }
__device__ __forceinline__ float u2f(u32 x) { return __builtin_bit_cast(float, x); }
__device__ __forceinline__ float lo2f(u32 x) { return u2f(x << 16); }
__device__ __forceinline__ float hi2f(u32 x) { return u2f(x & 0xffff0000u); }
__device__ __forceinline__ u32 pk(float lo, float hi) { bf2 r = {(__bf16)lo, (__bf16)hi}; return __builtin_bit_cast(u32, r); }
__device__ __forceinline__ float rbf(float x) { return lo2f(pk(x, x)); }
__device__ __forceinline__ f32x4 mfma(v4u a, v4u b, f32x4 c) {
  return __builtin_amdgcn_mfma_f32_16x16x32_bf16(__builtin_bit_cast(s16x8, a), __builtin_bit_cast(s16x8, b), c, 0, 0, 0);
}
template <bool FIRST> __device__ __forceinline__ void mfv(f32x4& c, v4u a, v4u b) {
  if (FIRST) asm volatile("v_mfma_f32_16x16x32_bf16 %0, %1, %2, 0" : "=&v"(c) : "v"(a), "v"(b));
  else asm volatile("v_mfma_f32_16x16x32_bf16 %0, %1, %2, %0" : "+v"(c) : "v"(a), "v"(b));
}
template <bool FIRST> __device__ __forceinline__ void mfa(f32x4& c, v4u a, v4u b) {
  if (FIRST) asm volatile("v_mfma_f32_16x16x32_bf16 %0, %1, %2, 0" : "=&v"(c) : "a"(a), "v"(b));
  else asm volatile("v_mfma_f32_16x16x32_bf16 %0, %1, %2, %0" : "+v"(c) : "a"(a), "v"(b));
}
__device__ __forceinline__ v2u trd(LAS char* lds, u32 off) {
  return __builtin_bit_cast(v2u, __builtin_amdgcn_ds_read_tr16_b64_v4i16((LAS s16x4*)(lds + off)));
}
__device__ __forceinline__ v4u mkrs(const void* p, u32 bytes) { u64 a = (u64)p; return (v4u){(u32)a, (u32)(a >> 32) & 0xffffu, bytes, 0x00020000u}; }
template <bool COH> __device__ __forceinline__ void dma(u32 lb, u32 lo, u32 voff, v4u rs, u32 sb, u32 so) {
  u32 t;
  if (COH) asm volatile("s_add_u32 m0, %1, %6\n\ts_add_u32 %0, %4, %5\n\tbuffer_load_dwordx4 %2, %3, %0 offen sc1 lds" : "=&s"(t) : "s"(lb), "v"(voff), "s"(rs), "s"(sb), "i"(so), "i"(lo) : "memory", "scc");
  else asm volatile("s_add_u32 m0, %1, %6\n\ts_add_u32 %0, %4, %5\n\tbuffer_load_dwordx4 %2, %3, %0 offen lds" : "=&s"(t) : "s"(lb), "v"(voff), "s"(rs), "s"(sb), "i"(so), "i"(lo) : "memory", "scc");
}
template <int CTRL, int RM> __device__ __forceinline__ float dppadd(float v) {
  return v + __builtin_bit_cast(float, __builtin_amdgcn_update_dpp(0, __builtin_bit_cast(int, v), CTRL, RM, 0xf, true));
}
__device__ __forceinline__ float hsum(float v) {
  v = dppadd<0x111, 0xf>(v); v = dppadd<0x112, 0xf>(v); v = dppadd<0x114, 0xf>(v); v = dppadd<0x118, 0xf>(v);
  return dppadd<0x142, 0xa>(v);
}
#define STAMP(i)
constexpr int conv_first(int i) { int n = 0; for (int j = 0; j < i; ++j) n += 8 - (j >> 2); return n; }
constexpr int conv_i(int n) { int i = 0; while (conv_first(i + 1) <= n) ++i; return i; }
template <int V> struct IC { static constexpr int value = V; };
template <int B, int E, class Fn> __device__ __forceinline__ void sfor(Fn&& f) { if constexpr (B < E) { f(IC<B>{}); sfor<B + 1, E>(f); } }
constexpr int w2_n(int s) { return s < 1 ? 0 : s < 12 ? 2 : 5; }
constexpr int w2_first(int s) { int n = 0; for (int j = 0; j < s; ++j) n += w2_n(j); return n; }
constexpr int w2_at(int s, int k) { return k < w2_n(s) ? w2_first(s) + k : -1; }
constexpr int W2_TAIL = w2_first(16);
constexpr int vm_after(int s) {
  return w2_n(s) + (s == 8 ? 1 : 0) + (s == 13 ? 4 : 0);
}
constexpr int h_wait(int s) {
  int n = 0;
  if (s < 3) { n = 4 * (2 - s); for (int j = 0; j < s; ++j) n += 4 + vm_after(j); return n; }
  n = vm_after(s - 3);
  for (int j = s - 2; j < s; ++j) n += (j < 12 ? 4 : 0) + vm_after(j);
  return n;
}

__global__ void __launch_bounds__(256, 1) fused(Args A) {
  __shared__ __attribute__((aligned(16))) char smem_[LDS_BYTES];
  LAS char* lds = (LAS char*)smem_;
  const u32 lds0 = (u32)(uintptr_t)lds;
  STAMP(0);
  const int b = blockIdx.x, x = b & 7, y = b >> 3;
  const int l = threadIdx.x & 63, w = __builtin_amdgcn_readfirstlane(threadIdx.x >> 6);
  const int n16 = l & 15, q = l >> 4, rr = (l >> 2) & 3, p4 = l & 3;
  const __amdgpu_buffer_rsrc_t WS = __builtin_amdgcn_make_buffer_rsrc(A.ws, 0, WS_BYTES, 0x00020000);
  const v4u rWS = mkrs(A.ws, WS_BYTES);
  const u32 tag = *(const u32*)(A.ws + O_EP) + 1u;
  const u32 dpair = *(const u32*)(A.ssd + 64 * x + 2 * y);

  const int cA = 64 * x + 16 * w + n16;
  const int srow = 8 * b + 2 * w;
  const int ch = w >> 1, par2 = w & 1, cB = 64 * x + 2 * y + ch;
  const int wU = b < 224 ? 2 * b + ch : 0;
  v2u shk; v4u sx0, sx1, kz[2], w1raw[4]; u32 w2raw[8];
  const v2u kedge = *(const v2u*)(A.ssk + cB * S + 1024);
  {
    const v4u rK = mkrs(A.k, S * D * 2), rV = mkrs(A.v, S * D * 2), rQ = mkrs(A.q, S * D * 2);
    const u32 c0 = ((l & 7) ^ ((l >> 3) & 2)) * 16, tv = (u32)((64 * y + (l >> 3)) * 1024 + 128 * x);
    const u32 va = tv + c0, vb = tv + (c0 ^ 64);
    const u32 vk = ((w & 1) ? vb : va) + (u32)(w * 8192 - 8192);
    dma<false>(lds0 + w * 1024, L_K, vk, rK, 0, 0);
    dma<false>(lds0 + w * 1024, L_K + 4096, vk + 32768, rK, 0, 0);
    if (w == 0) dma<false>(lds0, L_K + 8192, vk + 65536, rK, 0, 0);
    dma<false>(lds0 + w * 2048, L_V, va, rV, w * 16384, 0);
    dma<false>(lds0 + w * 2048, L_V + 1024, vb, rV, w * 16384, 8192);
    asm volatile("global_load_dwordx2 %0, %1, %2" : "=&v"(shk) : "v"(cA * 8), "s"(A.shk) : "memory");
    asm volatile("global_load_dwordx4 %0, %2, %3\n\tglobal_load_dwordx4 %1, %2, %3 offset:1024" : "=&v"(kz[0]), "=&v"(kz[1]) : "v"(l * 16), "s"(A.ssk + cB * S + 1024 * par2) : "memory");
    dma<false>(lds0 + w * 2048, L_Q, va, rQ, w * 16384, 0);
    dma<false>(lds0 + w * 2048, L_Q + 1024, vb, rQ, w * 16384, 8192);
    asm volatile("global_load_dwordx4 %0, %2, %3\n\tglobal_load_dwordx4 %1, %2, %3 offset:512" : "=&v"(sx0), "=&v"(sx1) : "v"((l >> 5) * 1024 + (l & 31) * 16), "s"(A.q + srow * D) : "memory");
  }
  u32 w1d[4];
  {
    const v4u rW = mkrs(ch ? A.wu : A.wg, D * F * 2);
    u32 w1s[4];
#pragma unroll
    for (int j = 0; j < 4; ++j) {
      const int P = 128 * j + 64 * par2 + l, c = P / 56, rem = P - 56 * c, r = rem / 7, rb = 32 * (x >> 2) + y;
      w1s[j] = P < 448 ? (u32)((8 * rb + r) * (F * 2) + (8 * (x & 3) + c) * 112 + (rem - 7 * r) * 16) : 0xffffff00u;
      w1d[j] = P < 448 ? O_W1S + (u32)(((8 * (x & 3) + c) * 2 + ch) * 57344 + rb * 896 + rem * 16) : 0xffffff00u;
    }
    asm volatile("buffer_load_dwordx4 %0, %4, %8, 0 offen\n\tbuffer_load_dwordx4 %1, %5, %8, 0 offen\n\tbuffer_load_dwordx4 %2, %6, %8, 0 offen\n\tbuffer_load_dwordx4 %3, %7, %8, 0 offen"
                 : "=&v"(w1raw[0]), "=&v"(w1raw[1]), "=&v"(w1raw[2]), "=&v"(w1raw[3]) : "v"(w1s[0]), "v"(w1s[1]), "v"(w1s[2]), "v"(w1s[3]), "s"(rW) : "memory");
  }
  int oA, oB;
  {
    const int UU = 4 * (wU >> 3) + q, f = UU / 7, u = UU - 7 * f;
    const int rowA = 56 * f + 4 * u + (u >= 4 ? 16 : 0), col = 64 * (wU & 7) + 8 * rr + 4 * (p4 & 1) + 32 * (p4 >> 1) + 2 * par2;
    oA = (rowA * D + col) * 2; oB = oA + (u < 6 ? 16 : 4) * (D * 2);
  }
  const int pan = 4 * (x >> 2) + (y >> 3), fch = 8 * (x & 3) + (y & 7);
  const int rot = 2 * (fch & 1);
  const u32 aAo = (u32)((8 * q + rr) * 128 + (((2 * w + (p4 >> 1)) ^ (rr & 2) ^ ((q & 1) << 2)) << 4) + (p4 & 1) * 8);
  LAS char* pA = lds + L_K + aAo;
  LAS char* pB = lds + L_K + (aAo ^ 64);
  u32 okv = O_KVT + cA * 4096 + (2040 - 32 - 64 * y - 8 * q) * 2, oqt = O_QT + cA * 4096 + (64 * y + 8 * q) * 2;
  LAS char* tw = lds + L_TZ + ch * TZ_C + (128 * par2 + l) * 16 + 32;
  u32 tb = lds0 + L_TZ + ch * TZ_C + ((n16 + 1) & 3) * TZ_S + (u32)((((n16 + 1) >> 2) + 2 * q + 8 * par2) * 8);
  u32 og = (l & 31) == 31 ? O_ST1 + (srow + (l >> 5)) * 8 : 0xffffff00u;
  asm volatile("" : "+v"(w1d[0]), "+v"(w1d[1]), "+v"(w1d[2]), "+v"(w1d[3]), "+v"(pA), "+v"(pB), "+v"(okv), "+v"(oqt), "+v"(oA), "+v"(oB));
  asm volatile("" : "+v"(tw), "+v"(tb), "+v"(og));
  if (w == 0) *(LAS v4u*)(lds + L_XR + (l >> 5) * XR_S + 4096 + (l & 31) * 16) = (v4u){0, 0, 0, 0};
  SB();
  STAMP(23);
  asm volatile("s_waitcnt vmcnt(10)" : "+v"(shk)::"memory");
  BAR();
  STAMP(1);
  {
    v2u ka[2], kb[2], kc[2], va[2], vb[2];
#pragma unroll
    for (int it = 0; it < 2; ++it) { ka[it] = trd(pA, it * 4096 + 512); kb[it] = trd(pB, it * 4096 + 1024); kc[it] = trd(pB, it * 4096 + 1536); }
#pragma unroll
    for (int it = 0; it < 2; ++it) { va[it] = trd(pA, L_V - L_K + it * 4096); vb[it] = trd(pA, L_V - L_K + it * 4096 + 512); }
    SB();
    auto LO = [](u32 a, u32 c) { return (f32x2){lo2f(a), lo2f(c)}; };
    auto HI = [](u32 a, u32 c) { return (f32x2){hi2f(a), hi2f(c)}; };
    const f32x2 w0 = LO(shk.x, shk.x), w1 = HI(shk.x, shk.x), w2 = LO(shk.y, shk.y), w3 = HI(shk.y, shk.y);
    const f32x2 e[12] = {w0, HI(ka[0].x, ka[1].x), LO(ka[0].y, ka[1].y), HI(ka[0].y, ka[1].y), LO(kb[0].x, kb[1].x), HI(kb[0].x, kb[1].x), LO(kb[0].y, kb[1].y),
                         HI(kb[0].y, kb[1].y), LO(kc[0].x, kc[1].x), HI(kc[0].x, kc[1].x), LO(kc[0].y, kc[1].y), HI(kc[0].y, kc[1].y)};
    const f32x2 vv[8] = {LO(va[0].x, va[1].x), HI(va[0].x, va[1].x), LO(va[0].y, va[1].y), HI(va[0].y, va[1].y), LO(vb[0].x, vb[1].x), HI(vb[0].x, vb[1].x), LO(vb[0].y, vb[1].y), HI(vb[0].y, vb[1].y)};
    f32x2 kv[8];
#pragma unroll
    for (int s = 0; s < 8; ++s) kv[s] = (w3 * e[s + 1] + w2 * e[s + 2] + w1 * e[s + 3] + w0 * e[s + 4]) * vv[s];
#pragma unroll
    for (int it = 0; it < 2; ++it)
      __builtin_amdgcn_raw_buffer_store_b128((v4u){pk(kv[7][it], kv[6][it]), pk(kv[5][it], kv[4][it]), pk(kv[3][it], kv[2][it]), pk(kv[1][it], kv[0][it])}, WS, okv + (1 - it) * 64, 0, SC1);
    SB();
    asm volatile("s_waitcnt vmcnt(8)" : "+v"(kz[0]), "+v"(kz[1])::"memory");
    BAR();
    v2u qa[2], qb[2];
#pragma unroll
    for (int it = 0; it < 2; ++it) { qa[it] = trd(pA, L_Q - L_K + it * 4096); qb[it] = trd(pA, L_Q - L_K + it * 4096 + 512); }
#pragma unroll
    for (int it = 0; it < 2; ++it) __builtin_amdgcn_raw_buffer_store_b128((v4u){qa[it].x, qa[it].y, qb[it].x, qb[it].y}, WS, oqt + it * 64, 0, SC1);
    if (y == 0 && w == 0 && l < 8) __builtin_amdgcn_raw_buffer_store_b32(tag * 64u, WS, O_C2 + l * 2048 + x * 256, 0, SC1);
  }
  SB();
  STAMP(24);
  {
    auto put = [&](LAS char* p, v4u d, u32 d4, u32 d5) {
      const u32 e0 = __builtin_amdgcn_alignbit(d.y, d.x, 16), e1 = __builtin_amdgcn_alignbit(d.z, d.y, 16), e2 = __builtin_amdgcn_alignbit(d.w, d.z, 16),
                e3 = __builtin_amdgcn_alignbit(d4, d.w, 16), e4 = __builtin_amdgcn_alignbit(d5, d4, 16);
      *(LAS v4u*)p = d; *(LAS v4u*)(p + TZ_S) = (v4u){e0, e1, e2, e3}; *(LAS v4u*)(p + 2 * TZ_S) = (v4u){d.y, d.z, d.w, d4}; *(LAS v4u*)(p + 3 * TZ_S) = (v4u){e1, e2, e3, e4};
    };
    auto nxt = [](u32 last, u32 v) { return (u32)__builtin_amdgcn_update_dpp((int)last, (int)v, 0x130, 0xf, 0xf, false); };
    put(tw, kz[0], nxt(__builtin_amdgcn_readlane(kz[1].x, 0), kz[0].x), nxt(__builtin_amdgcn_readlane(kz[1].y, 0), kz[0].y));
    put(tw + 1024, kz[1], nxt(par2 ? 0u : kedge.x, kz[1].x), nxt(par2 ? 0u : kedge.y, kz[1].y));
    const u32 f0 = __builtin_amdgcn_readlane(kz[0].x, 0), f1 = __builtin_amdgcn_readlane(kz[0].y, 0);
    if (l < 2 && !par2) put(tw - 32, (v4u){0, 0, 0, 0}, l ? f0 : 0u, l ? f1 : 0u);
  }
  SB();
  asm volatile("s_waitcnt vmcnt(4)" : "+v"(sx0), "+v"(sx1), "+v"(w1raw[0]), "+v"(w1raw[1]), "+v"(w1raw[2]), "+v"(w1raw[3])::"memory");
#pragma unroll
  for (int j = 0; j < 4; ++j) __builtin_amdgcn_raw_buffer_store_b128(w1raw[j], WS, w1d[j], 0, SC1);
  SB();
  {
    const f32x2 v[8] = {{lo2f(sx0.x), hi2f(sx0.x)}, {lo2f(sx0.y), hi2f(sx0.y)}, {lo2f(sx0.z), hi2f(sx0.z)}, {lo2f(sx0.w), hi2f(sx0.w)},
                        {lo2f(sx1.x), hi2f(sx1.x)}, {lo2f(sx1.y), hi2f(sx1.y)}, {lo2f(sx1.z), hi2f(sx1.z)}, {lo2f(sx1.w), hi2f(sx1.w)}};
    const f32x2 s2 = ((v[0] + v[1]) + (v[2] + v[3])) + ((v[4] + v[5]) + (v[6] + v[7]));
    const float sm = hsum(s2[0] + s2[1]);
    const float m0 = u2f(__builtin_amdgcn_readlane(f2u(sm), 31)), m1 = u2f(__builtin_amdgcn_readlane(f2u(sm), 63));
    const float mean = (l < 32 ? m0 : m1) * (1.f / D);
    f32x2 ss = {0.f, 0.f};
#pragma unroll
    for (int j = 0; j < 8; ++j) { const f32x2 d = v[j] - mean; ss += d * d; }
    const float var = hsum(ss[0] + ss[1]) * (1.f / D);
    const u32 pay = pk(mean, __builtin_amdgcn_sqrtf(rbf(rbf(var) + 1e-5f)));
    __builtin_amdgcn_raw_buffer_store_b64((v2u){pay, tag}, WS, og, 0, SC1);
  }
  SB();
  WAITV(1);
  BAR();
  STAMP(2);
  if (w == 0 && l == 0) __builtin_amdgcn_raw_buffer_store_b32(tag, WS, O_F1 + x * 256 + y * 4, 0, SC1);
  v4u T[32];
  {
#pragma unroll
    for (int i0 = 0; i0 < 32; i0 += 4) {
#pragma unroll
      for (int i = i0; i < i0 + 4; ++i) {
        v2u a, c;
        asm volatile("ds_read_b64 %0, %2 offset:%3\n\tds_read_b64 %1, %2 offset:%4" : "=&v"(a), "=&v"(c) : "v"(tb), "i"(128 * i), "i"(128 * i + 8) : "memory");
        T[i] = (v4u){a.x, a.y, c.x, c.y};
      }
    }
    asm volatile("s_waitcnt lgkmcnt(0)" : "+v"(T[0]), "+v"(T[1]), "+v"(T[2]), "+v"(T[3]), "+v"(T[4]), "+v"(T[5]), "+v"(T[6]), "+v"(T[7]), "+v"(T[8]), "+v"(T[9]), "+v"(T[10]),
                 "+v"(T[11]), "+v"(T[12]), "+v"(T[13]), "+v"(T[14]), "+v"(T[15])::"memory");
    asm volatile("" : "+v"(T[16]), "+v"(T[17]), "+v"(T[18]), "+v"(T[19]), "+v"(T[20]), "+v"(T[21]), "+v"(T[22]), "+v"(T[23]), "+v"(T[24]), "+v"(T[25]), "+v"(T[26]),
                 "+v"(T[27]), "+v"(T[28]), "+v"(T[29]), "+v"(T[30]), "+v"(T[31])::"memory");
  }
  const int tB = 1024 * par2 + 16 * n16 + 4 * q;
  LAS char* xp = lds + L_XR + ch * XR_S + (2032 - 16 * n16 + 32 * par2 + 8 * q) * 2 - 3584;
  LAS char* kp = lds + L_XR + ch * XR_S + (2044 - 768 - tB) * 2;
  LAS char* gp = lds + L_S1 + tB * 8;
  u32 qo = O_QT + cB * 4096 + tB * 2, l16 = l * 16;
  asm volatile("" : "+v"(xp), "+v"(kp), "+v"(gp), "+v"(qo), "+v"(l16));
  STAMP(21);
  if (w == 0) {
    for (u32 sp = 0; sp < SPIN_CAP; ++sp) {
      asm volatile("" ::: "memory");
      const u32 f = __builtin_amdgcn_raw_buffer_load_b32(WS, O_F1 + (x ^ (4 * (l >> 5))) * 256 + (l & 31) * 4, 0, SC1);
      if (__all(f == tag)) break;
    }
  }
  STAMP(27);
  BAR();
  STAMP(3);

  dma<true>(lds0 + ch * XR_S + par2 * 2048, L_XR, l16, rWS, cB * 4096 + par2 * 2048, O_KVT);
  dma<true>(lds0 + ch * XR_S + par2 * 2048, L_XR + 1024, l16, rWS, cB * 4096 + par2 * 2048, O_KVT + 1024);
#pragma unroll
  for (int i = 0; i < 4; ++i) dma<true>(lds0 + w * 4096, L_S1 + i * 1024, l16, rWS, w * 4096, O_ST1 + i * 1024);
  v2u xq[4]; v4u g1[8];
  {
    asm volatile("buffer_load_dwordx2 %0, %4, %5, 0 offen sc1\n\tbuffer_load_dwordx2 %1, %4, %5, 0 offen offset:512 sc1\n\t"
                 "buffer_load_dwordx2 %2, %4, %5, 0 offen offset:1024 sc1\n\tbuffer_load_dwordx2 %3, %4, %5, 0 offen offset:1536 sc1"
                 : "=&v"(xq[0]), "=&v"(xq[1]), "=&v"(xq[2]), "=&v"(xq[3]) : "v"(qo), "s"(rWS) : "memory");
  }
  SB();
  WAITV(8);
  BAR();
  STAMP(4);
  auto w1l = [&](int i) { dma<true>(lds0 + w * 1024, i * 4096, l16, rWS, fch * 114688 + w * 1024, O_W1S + i * 4096); };
  f32x4 cacc[8];
  float xf[16], ln[16], kd[16];
  const float dsc = ch ? hi2f(dpair) : lo2f(dpair);
  {
    v4u B[32]; v2u kr[4]; float tm[16];
#pragma unroll
    for (int i = 0; i < 3; ++i) B[i] = *(LAS v4u*)(xp + 128 * (31 - i));
    SB();
    sfor<0, 144>([&](auto N_) {
      {
        constexpr int n = decltype(N_)::value, i = conv_i(n), a = (i >> 2) + n - conv_first(i), eps = 4 * a - i + 3;
        if constexpr (i == 0) cacc[a] = mfma(T[eps], B[i], (f32x4){0.f, 0.f, 0.f, 0.f}); else cacc[a] = mfma(T[eps], B[i], cacc[a]);
        SB();
        if constexpr (n < 87 && n % 3 == 0) B[3 + n / 3] = *(LAS v4u*)(xp + 128 * (28 - n / 3));
        if constexpr (n < 12 && n % 3 == 1) kr[n / 3] = *(LAS v2u*)(kp + 512 * (3 - n / 3));
        if constexpr (n >= 4 && n < 52 && n % 3 == 2) { constexpr int j = (n - 4) / 3; const u32 kk = kr[j >> 2][1 - ((j & 3) >> 1)]; kd[j] = ((j & 1) ? lo2f(kk) : hi2f(kk)) * dsc; asm volatile("" : "+v"(kd[j])); }
        if constexpr (n == 22) {
          asm volatile("s_waitcnt vmcnt(4)" : "+v"(xq[0]), "+v"(xq[1]), "+v"(xq[2]), "+v"(xq[3])::"memory");
          __builtin_amdgcn_s_barrier();
          asm volatile("" ::: "memory");
        }
        if constexpr (n == 23) asm volatile("global_load_dword %0, %4, %5\n\tglobal_load_dword %1, %4, %5 offset:1024\n\tglobal_load_dword %2, %4, %5 offset:2048\n\tglobal_load_dword %3, %4, %5 offset:3072"
                                            : "=&v"(w2raw[0]), "=&v"(w2raw[1]), "=&v"(w2raw[2]), "=&v"(w2raw[3]) : "v"(oA), "s"(A.wd) : "memory");
        if constexpr (n == 35) asm volatile("global_load_dword %0, %4, %5\n\tglobal_load_dword %1, %4, %5 offset:1024\n\tglobal_load_dword %2, %4, %5 offset:2048\n\tglobal_load_dword %3, %4, %5 offset:3072"
                                            : "=&v"(w2raw[4]), "=&v"(w2raw[5]), "=&v"(w2raw[6]), "=&v"(w2raw[7]) : "v"(oB), "s"(A.wd) : "memory");
        if constexpr (n >= 25 && n < 49 && n % 3 == 1) { constexpr int j = (n - 25) / 3; g1[j] = *(LAS v4u*)(gp + (j >> 1) * 2048 + (j & 1) * 16); }
        if constexpr (n < 140 && n % 5 == 2) w1l(n / 5);
        if constexpr (n >= 55 && n < 135 && (n - 55) % 5 < 4) {
          constexpr int j = (n - 55) / 5, ph = (n - 55) % 5;
          const u32 pay = g1[j >> 1][(j & 1) * 2];
          if constexpr (ph == 0) { xf[j] = (j & 1) ? hi2f(xq[j >> 2][(j & 3) >> 1]) : lo2f(xq[j >> 2][(j & 3) >> 1]); asm volatile("" : "+v"(xf[j])); }
          if constexpr (ph == 1) { tm[j] = __builtin_amdgcn_rcpf(hi2f(pay)); asm volatile("" : "+v"(tm[j])); }
          if constexpr (ph == 2) { ln[j] = xf[j] - lo2f(pay); asm volatile("" : "+v"(ln[j])); }
          if constexpr (ph == 3) { ln[j] *= tm[j]; asm volatile("" : "+v"(ln[j])); }
        }
        SB();
      }
    });
  }
  STAMP(5);
  const int ct = y & 7, rbl = y >> 3;
  const u32 w2v = O_W2T + (u32)((ct * 56 + 14 * w) * 4096 + l * 16);
  v4u W2[56];
  {
    bool ok = true;
#pragma unroll
    for (int i = 0; i < 8; ++i) ok &= (g1[i].y == tag) & (g1[i].w == tag);
    if (!__all(ok)) {
      for (u32 sp = 0; sp < SPIN_CAP; ++sp) {
        asm volatile("" ::: "memory");
        ok = true;
#pragma unroll
        for (int i = 0; i < 8; ++i) { g1[i] = __builtin_amdgcn_raw_buffer_load_b128(WS, O_ST1 + tB * 8 + (i >> 1) * 2048 + (i & 1) * 16, 0, SC1); ok &= (g1[i].y == tag) & (g1[i].w == tag); }
        if (__all(ok)) break;
      }
#pragma unroll
      for (int j = 0; j < 16; ++j) { const u32 pay = g1[j >> 1][(j & 1) * 2]; ln[j] = (xf[j] - lo2f(pay)) * __builtin_amdgcn_rcpf(hi2f(pay)); }
    }
    f32x4 mn[4];
    if (par2) {
#pragma unroll
      for (int aa = 0; aa < 4; ++aa) { *(LAS f32x4*)(lds + L_EX + w * 4096 + aa * 1024 + l * 16) = cacc[aa]; mn[aa] = cacc[4 + aa]; asm volatile("" : "+v"(mn[aa])); }
    } else {
#pragma unroll
      for (int aa = 0; aa < 4; ++aa) { *(LAS f32x4*)(lds + L_EX + w * 4096 + aa * 1024 + l * 16) = cacc[4 + aa]; mn[aa] = cacc[aa]; asm volatile("" : "+v"(mn[aa])); }
    }
    BAR();
    u32 hp[4][2];
#pragma unroll
    for (int aa = 0; aa < 4; ++aa) {
      const f32x4 o = *(LAS f32x4*)(lds + L_EX + (w ^ 1) * 4096 + aa * 1024 + l * 16), m = mn[aa];
      float h[4];
#pragma unroll
      for (int i = 0; i < 4; ++i) h[i] = xf[4 * aa + i] + ln[4 * aa + i] * ((m[i] + kd[4 * aa + i]) + o[i]);
      hp[aa][0] = pk(h[0], h[1]); hp[aa][1] = pk(h[2], h[3]);
    }
#pragma unroll
    for (int pr = 0; pr < 2; ++pr) {
      const auto s0 = __builtin_amdgcn_permlane16_swap(hp[2 * pr][0], hp[2 * pr + 1][0], false, false);
      const auto s1 = __builtin_amdgcn_permlane16_swap(hp[2 * pr][1], hp[2 * pr + 1][1], false, false);
      __builtin_amdgcn_raw_buffer_store_b128((v4u){s0[0], s1[0], s0[1], s1[1]}, WS,
          O_HT + cB * 4096 + (1024 * par2 + 256 * (2 * pr + (q & 1)) + 16 * n16 + 8 * (q >> 1)) * 2, 0, SC1);
    }
    SB();
    STAMP(6);
    {
      const u32 w2o = b < 224 ? O_W2T + (u32)(((wU & 7) * 56 + (wU >> 3)) * 4096 + par2 * 2048 + l * 16) : 0xffff0000u;
      asm volatile("s_waitcnt vmcnt(2)" : "+v"(w2raw[0]), "+v"(w2raw[1]), "+v"(w2raw[2]), "+v"(w2raw[3]), "+v"(w2raw[4]), "+v"(w2raw[5]), "+v"(w2raw[6]), "+v"(w2raw[7])::"memory");
#pragma unroll
      for (int pp = 0; pp < 2; ++pp) {
        v4u fr;
#pragma unroll
        for (int d = 0; d < 4; ++d) fr[d] = __builtin_amdgcn_perm(w2raw[2 * d + 1], w2raw[2 * d], pp ? 0x07060302u : 0x05040100u);
        __builtin_amdgcn_raw_buffer_store_b128(fr, WS, w2o + pp * 1024, 0, SC1);
      }
    }
    SB();
    WAITV(0);
    STAMP(28);
    BAR();
    STAMP(7);
    if (w == 0 && l < 8) __hip_atomic_fetch_add((GAS u32*)(A.ws + O_C2 + l * 2048 + x * 256), 1u, RLX);
  }
  const u32 aW = (u32)((16 * (q >> 1) + 8 * (q & 1) + 2 * rr) * 112 + 8 * p4), aM = aW + (p4 < 2 ? 96u : (L_WU + 96u - 16u));
  auto wfrag = [&](u32 bG, u32 bM, int ks, int t) -> v4u {
    const u32 o = ks * 3584;
    v2u a, c;
    if (t == 6) { a = trd(lds, bM + o); c = trd(lds, bM + o + 112); }
    else if (t & 1) { a = trd(lds + L_WU, bG + o + 32 * (t >> 1)); c = trd(lds + L_WU, bG + o + 32 * (t >> 1) + 112); }
    else { a = trd(lds, bG + o + 32 * (t >> 1)); c = trd(lds, bG + o + 32 * (t >> 1) + 112); }
    return (v4u){a.x, a.y, c.x, c.y};
  };
  const v4u ones = {0x3f803f80u, 0x3f803f80u, 0x3f803f80u, 0x3f803f80u};
  f32x4 cs[7], cgm, cum;
  f32x4 acc[7][4];
  v4u g2[4];
  const u32 ring = L_R + w * 12288;
  const int spar = (fch >> 1) & 3;
  {
    const int ssel = (fch >> 3) & 1;
    u32 hv[4];
#pragma unroll
    for (int n = 0; n < 4; ++n)
      hv[n] = (u32)((8 * (l >> 5) + 2 * ((l >> 3) & 3) + 16 * (n >> 1) + (n & 1)) * 4096 + (((l & 7) ^ ((l >> 3) & 6)) << 4) + (256 * pan + 64 * w) * 2 + O_HT - 1024 * n);
    const u32 hb = ring + (u32)((16 * (q >> 1) + 4 * (q & 1) + rr) * 128 + (p4 & 1) * 8);
    u32 hx[4];
#pragma unroll
    for (int pr = 0; pr < 4; ++pr) hx[pr] = hb + (((2 * ((pr + spar) & 3) + (p4 >> 1)) ^ ((4 * (q & 1) + rr) & 6)) << 4);
    u32 sg[4], hs;
#pragma unroll
    for (int g = 0; g < 4; ++g) sg[g] = (u32)((g + rot) & 3);
    auto hdma = [&](int s, int n) {
      if (n == 0) asm volatile("s_add_u32 m0, %1, %2\n\ts_add_u32 %0, %3, %4\n\tbuffer_load_dwordx4 %5, %6, %0 offen sc1 lds"
                               : "=&s"(hs) : "s"(lds0 + ring), "i"((s % 3) * 4096), "s"(sg[s >> 2] * (128 * 4096)), "i"((s & 3) * (32 * 4096)), "v"(hv[0]), "s"(rWS) : "memory", "scc");
      else asm volatile("buffer_load_dwordx4 %0, %1, %2 offen offset:%3 sc1 lds" ::"v"(hv[n]), "s"(rWS), "s"(hs), "i"(n * 1024) : "memory");
    };
    auto hfrag = [&](int s, int pr, int t) { return trd(lds, hx[pr] + (s % 3) * 4096 + t * 1024); };
    f32x4 sS = {0.f, 0.f, 0.f, 0.f}, sQ = sS;
    u32 bG = aW + sg[0] * 14336, bM = aM + sg[0] * 14336;
    v4u wf[3], hf[2][4];
    u32 c0 = aW + (w & 1) * L_WU + 32 * (w >> 1), c1 = w == 3 ? c0 : w == 2 ? aM : c0 + 64;
    f32x4 ca, cb;
    auto crd = [&](u32 base, int ks) { const v2u a = trd(lds, base + ks * 3584), c = trd(lds, base + ks * 3584 + 112); return (v4u){a.x, a.y, c.x, c.y}; };
    {
      asm volatile("" : "+v"(c0), "+v"(c1));
      v4u fa[16], fb_[16];
#pragma unroll
      for (int k = 0; k < 3; ++k) { fa[k] = crd(c0, k); fb_[k] = crd(c1, k); }
      SB();
#pragma unroll
      for (int k = 0; k < 16; ++k) {
        if (k + 3 < 16) { fa[k + 3] = crd(c0, k + 3); fb_[k + 3] = crd(c1, k + 3); }
        ca = mfma(fa[k], ones, k ? +ca : (f32x4){0.f, 0.f, 0.f, 0.f}); cb = mfma(fb_[k], ones, k ? +cb : (f32x4){0.f, 0.f, 0.f, 0.f});
        SB();
      }
    }
    wf[0] = wfrag(bG, bM, 0, 0); wf[1] = wfrag(bG, bM, 0, 1);
    asm volatile("" : "+v"(wf[0]), "+v"(wf[1]), "+v"(bG), "+v"(bM), "+v"(hv[0]), "+v"(hv[1]), "+v"(hv[2]), "+v"(hv[3]), "+v"(hx[0]), "+v"(hx[1]), "+v"(hx[2]), "+v"(hx[3]));
    STAMP(22);
    if (w == 0) {
      const u32 want = tag * 64u + 32u;
      for (u32 sp = 0; sp < SPIN_CAP; ++sp) {
        asm volatile("" ::: "memory");
        const u32 f = __builtin_amdgcn_raw_buffer_load_b32(WS, O_C2 + x * 2048 + (l & 7) * 256, 0, SC1);
        if (__all(f == want)) break;
      }
    }
    STAMP(25);
    STAMP(26);
    BAR();
    STAMP(8);
#pragma unroll
    for (int s = 0; s < 3; ++s)
#pragma unroll
      for (int n = 0; n < 4; ++n) hdma(s, n);
    WAITV(8);
#pragma unroll
    for (int pr = 0; pr < 4; ++pr) { const v2u a = hfrag(0, pr, 0), c = hfrag(0, pr, 1); hf[0][pr] = (v4u){a.x, a.y, c.x, c.y}; }
    asm volatile("" : "+v"(hf[0][0]), "+v"(hf[0][1]), "+v"(hf[0][2]), "+v"(hf[0][3]));
#pragma unroll
    for (int n = 0; n < 4; ++n) hdma(3, n);
    SB();
    STAMP(20);
    sfor<0, 16>([&](auto S_) {
      constexpr int s = decltype(S_)::value;
      v4u(&hc)[4] = hf[s & 1]; v4u(&hn)[4] = hf[(s + 1) & 1];
      u32 bGn = bG, bMn = bM;
      if constexpr ((s & 3) == 3 && s < 15) { bGn = aW + sg[(s + 1) >> 2] * 14336; bMn = aM + sg[(s + 1) >> 2] * 14336; }
      sfor<0, 7>([&](auto T_) {
        constexpr int t = decltype(T_)::value, it = s * 7 + t, nx = it + 2, ns = nx / 7, nt = nx % 7;
        constexpr bool have = nx < 112;
        v2u na, nc, ha;
        const u32 g_ = (ns > s) ? bGn : bG, m_ = (ns > s) ? bMn : bM;
        LAS char* wb = lds + ((nt != 6 && (nt & 1)) ? L_WU : 0);
        const u32 wa = (nt == 6 ? m_ : g_ + 32 * (nt >> 1)) + (ns & 3) * 3584;
        auto w2l = [&](auto K_) { constexpr int i = w2_at(s, decltype(K_)::value); if constexpr (i >= 0) W2[i] = __builtin_amdgcn_raw_buffer_load_b128(WS, w2v + i * 1024, 0, SC1); };
        mfv<s == 0>(acc[t][0], wf[it % 3], hc[0]);
        SB();
        if constexpr (have) na = trd(wb, wa);
        SB();
        mfv<s == 0>(acc[t][1], wf[it % 3], hc[1]);
        SB();
        if constexpr (have) nc = trd(wb, wa + 112);
        SB();
        mfv<s == 0>(acc[t][2], wf[it % 3], hc[2]);
        SB();
        if constexpr (s < 15 && t < 4) { if constexpr (t == 0) { constexpr int hw = h_wait(s); WAITV(hw); } ha = hfrag(s + 1, t, 0); }
        if constexpr (s < 12 && (t == 4 || t == 5)) { if constexpr (t == 4) asm volatile("" : "+v"(hn[0]), "+v"(hn[1]), "+v"(hn[2]), "+v"(hn[3])); hdma(s + 4, 2 * (t - 4)); }
        if constexpr (t == 6) w2l(IC<0>{});
        if constexpr (t == 4) w2l(IC<2>{});
        if constexpr (t == 5) w2l(IC<3>{});
        SB();
        mfv<s == 0>(acc[t][3], wf[it % 3], hc[3]);
        SB();
        if constexpr (s < 15 && t < 4) { const v2u hb_ = hfrag(s + 1, t, 1); hn[t] = (v4u){ha.x, ha.y, hb_.x, hb_.y}; }
        if constexpr (s < 12 && (t == 4 || t == 5)) hdma(s + 4, 2 * (t - 4) + 1);
        if constexpr (t == 6) w2l(IC<1>{});
        if constexpr (t == 4) w2l(IC<4>{});
        if constexpr (have) wf[nx % 3] = (v4u){na.x, na.y, nc.x, nc.y};
        SB();
      });
      if constexpr (s < 8) {
        if (ssel == (s >> 2)) { sS = mfma(ones, hc[0], sS); sQ = mfma(hc[0], hc[0], sQ); }
        SB();
      }
      if constexpr (s == 8) {
        const float q01 = (p4 & 1) ? +sQ[1] : +sQ[0], q23 = (p4 & 1) ? +sQ[3] : +sQ[2], qq = (p4 & 2) ? q23 : q01;
        const u32 off = (fch < 16 && rr == q) ? O_ST2 + (u32)(((ssel + rot) & 3) * 32768 + (256 * pan + 64 * w + 16 * spar + n16) * 16) : 0xffffff00u;
        __builtin_amdgcn_raw_buffer_store_b128((v4u){f2u(sS[0]), tag, f2u(qq), tag}, WS, off, 0, SC1);
        SB();
      }
      if constexpr (s == 13) {
        if (n16 == 0) { *(LAS f32x4*)(lds + ring + 4096 + q * 16) = ca; *(LAS f32x4*)(lds + ring + 4096 + 64 + q * 16) = cb; }
#pragma unroll
        for (int k = 0; k < 4; ++k) g2[k] = __builtin_amdgcn_raw_buffer_load_b128(WS, O_ST2 + k * 32768 + (256 * pan + 64 * w + l) * 16, 0, SC1);
        SB();
      }
      if constexpr (s == 14) { BAR(); SB(); }
      bG = bGn; bM = bMn;
      if constexpr ((s & 3) == 3) STAMP(16 + (s >> 2));
    });
    asm volatile("s_nop 7" ::: "memory");
    SB();
  }
  STAMP(9);
  {
    bool ok = true;
#pragma unroll
    for (int k = 0; k < 4; ++k) ok &= (g2[k].y == tag) & (g2[k].w == tag);
    if (!__all(ok)) {
      for (u32 sp = 0; sp < SPIN_CAP; ++sp) {
        asm volatile("" ::: "memory");
        ok = true;
#pragma unroll
        for (int k = 0; k < 4; ++k) { g2[k] = __builtin_amdgcn_raw_buffer_load_b128(WS, O_ST2 + k * 32768 + (256 * pan + 64 * w + l) * 16, 0, SC1); ok &= (g2[k].y == tag) & (g2[k].w == tag); }
        if (__all(ok)) break;
      }
    }
    const float mean = ((u2f(g2[0].x) + u2f(g2[1].x)) + (u2f(g2[2].x) + u2f(g2[3].x))) * (1.f / D);
    const float v2 = __builtin_fmaxf(((u2f(g2[0].z) + u2f(g2[1].z)) + (u2f(g2[2].z) + u2f(g2[3].z))) * (1.f / D) - mean * mean, 0.f) + 1e-5f;
    *(LAS f32x4*)(lds + ring + l * 16) = (f32x4){-mean, -1.44269504089f * __builtin_amdgcn_rsqf(v2), v2, 0.f};
#pragma unroll
    for (int t = 0; t < 7; ++t) cs[t] = *(LAS f32x4*)(lds + L_R + (t & 3) * 12288 + 4096 + (t >> 2) * 64 + q * 16);
#pragma unroll
    for (int i = 0; i < 4; ++i) {
      const auto s_ = __builtin_amdgcn_permlane32_swap(f2u(cs[6][i]), f2u(cs[6][i]), false, false);
      cgm[i] = u2f(s_[0]); cum[i] = u2f(s_[1]);
    }
    f32x4 st[4], sm[2];
#pragma unroll
    for (int pr = 0; pr < 4; ++pr) st[pr] = *(LAS f32x4*)(lds + ring + (16 * ((pr + spar) & 3) + n16) * 16);
#pragma unroll
    for (int pp = 0; pp < 2; ++pp) sm[pp] = *(LAS f32x4*)(lds + ring + (16 * ((2 * pp + (l >> 5) + spar) & 3) + n16) * 16);
    int w2n = W2_TAIL;
    auto act4 = [&](f32x4 g, f32x4 u, f32x4 cg, f32x4 cu, f32x4 s) -> v2u {
      if (w2n < 56) { W2[w2n] = __builtin_amdgcn_raw_buffer_load_b128(WS, w2v + w2n * 1024, 0, SC1); ++w2n; }
      const f32x4 a = g + s[0] * cg, bb = u + s[0] * cu, t = a * s[1];
      f32x4 e;
#pragma unroll
      for (int i = 0; i < 4; ++i) e[i] = __builtin_amdgcn_exp2f(t[i]);
      e = e * s[2] + s[2];
#pragma unroll
      for (int i = 0; i < 4; ++i) e[i] = __builtin_amdgcn_rcpf(e[i]);
      const f32x4 r = a * bb * e;
      return (v2u){pk(r[0], r[1]), pk(r[2], r[3])};
    };
    const u32 ab = O_ACT + (u32)(((16 * pan + 4 * w) * 224 + 7 * fch) * 256 + n16 * 16);
    v2u X[4];
#pragma unroll
    for (int pr = 0; pr < 4; ++pr) {
      const v2u r0 = act4(acc[0][pr], acc[1][pr], cs[0], cs[1], st[pr]), r1 = act4(acc[2][pr], acc[3][pr], cs[2], cs[3], st[pr]);
      __builtin_amdgcn_raw_buffer_store_b128((v4u){r0.x, r0.y, r1.x, r1.y}, WS, ab + q * 256 + ((pr + spar) & 3) * (224 * 256), 0, SC1);
      X[pr] = act4(acc[4][pr], acc[5][pr], cs[4], cs[5], st[pr]);
      SB();
    }
    v2u Bh[2];
#pragma unroll
    for (int pp = 0; pp < 2; ++pp) {
      f32x4 g, u;
#pragma unroll
      for (int i = 0; i < 4; ++i) {
        const auto s_ = __builtin_amdgcn_permlane32_swap(f2u(acc[6][2 * pp][i]), f2u(acc[6][2 * pp + 1][i]), false, false);
        g[i] = u2f(s_[0]); u[i] = u2f(s_[1]);
      }
      const v2u M = act4(g, u, cgm, cum, sm[pp]);
      const auto x0 = __builtin_amdgcn_permlane32_swap(X[2 * pp].x, X[2 * pp + 1].x, false, false);
      const auto x1 = __builtin_amdgcn_permlane32_swap(X[2 * pp].y, X[2 * pp + 1].y, false, false);
      __builtin_amdgcn_raw_buffer_store_b128((v4u){x0[0], x1[0], M.x, M.y}, WS, ab + (4 + (q & 1)) * 256 + ((2 * pp + (l >> 5) + spar) & 3) * (224 * 256), 0, SC1);
      Bh[pp] = (v2u){x0[1], x1[1]};
    }
    const auto z0 = __builtin_amdgcn_permlane16_swap(Bh[0].x, Bh[1].x, false, false);
    const auto z1 = __builtin_amdgcn_permlane16_swap(Bh[0].y, Bh[1].y, false, false);
    __builtin_amdgcn_raw_buffer_store_b128((v4u){z0[0], z1[0], z0[1], z1[1]}, WS, ab + 6 * 256 + (((((q & 1) << 1) | (q >> 1)) + spar) & 3) * (224 * 256), 0, SC1);
  }
  SB();
  STAMP(10);
  const int row2 = 256 * x + 64 * rbl;
  dma<true>(lds0 + ring, 8192, (u32)((l >> 1) * 4096 + (l & 1) * 16), rWS, (64 * ct) * 4096 + (row2 + 16 * w) * 2, O_HT);
  dma<true>(lds0 + ring, 9216, (u32)((l >> 1) * 4096 + (l & 1) * 16), rWS, (64 * ct) * 4096 + (row2 + 16 * w) * 2, O_HT + 32 * 4096);
  WAITV(2);
  if (l == 0) __builtin_amdgcn_raw_buffer_store_b32(tag, WS, O_F3 + ((pan * 4 + w) * 4 + (fch >> 3)) * 256 + (fch & 7) * 4, 0, SC1);
  if (b == 0 && w == 0 && l == 0) __builtin_amdgcn_raw_buffer_store_b32(tag, WS, O_EP, 0, SC1);
  STAMP(11);
  __builtin_amdgcn_s_barrier();
  for (u32 sp = 0; sp < SPIN_CAP; ++sp) {
    asm volatile("" ::: "memory");
    const u32 f = __builtin_amdgcn_raw_buffer_load_b32(WS, O_F3 + ((x * 4 + rbl) * 4 + w) * 256 + (l & 7) * 4, 0, SC1);
    if (__all(f == tag)) break;
  }
  WAITV(0);
  STAMP(12);

  {
    constexpr int PF = PFD;
    v4u AC[56];
    const u32 av = O_ACT + (u32)(((16 * x + 4 * rbl) * 224 + 56 * w) * 256 + l * 16);
    auto ald = [&](int s) {
#pragma unroll
      for (int rt = 0; rt < 4; ++rt) { AC[4 * s + rt] = __builtin_amdgcn_raw_buffer_load_b128(WS, av + s * 1024, ((rt + w) & 3) * (224 * 256), SC1); SB(); }
    };
#pragma unroll
    for (int s = 0; s < PF; ++s) ald(s);
    v2u hr[4];
#pragma unroll
    for (int i = 0; i < 4; ++i) hr[i] = trd(lds, ring + 8192 + (u32)((8 * q + 4 * (i & 1) + 32 * (i >> 1) + rr) * 32 + p4 * 8));
    SB();
    f32x4 o[4][4];
#pragma unroll
    for (int s = 0; s < 14; ++s) {
#pragma unroll
      for (int rt = 0; rt < 4; ++rt)
#pragma unroll
        for (int pp = 0; pp < 4; ++pp) { if (s == 0) mfa<true>(o[pp][rt], W2[4 * s + pp], AC[4 * s + rt]); else mfa<false>(o[pp][rt], W2[4 * s + pp], AC[4 * s + rt]); }
      SB();
      if (s + PF < 14) ald(s + PF);
    }
    asm volatile("s_nop 7" ::: "memory");
    SB();
    STAMP(13);
    f32x4 mine[4];
#pragma unroll
    for (int rt = 1; rt < 4; ++rt)
#pragma unroll
      for (int pp = 0; pp < 4; ++pp) *(LAS f32x4*)(lds + ring + (rt - 1) * 4096 + pp * 1024 + l * 16) = o[pp][rt];
#pragma unroll
    for (int pp = 0; pp < 4; ++pp) mine[pp] = o[pp][0];
    BAR();
#pragma unroll
    for (int j = 1; j < 4; ++j)
#pragma unroll
      for (int pp = 0; pp < 4; ++pp) mine[pp] += *(LAS f32x4*)(lds + L_R + ((w - j) & 3) * 12288 + (j - 1) * 4096 + pp * 1024 + l * 16);
    u32 r[8];
#pragma unroll
    for (int i = 0; i < 4; ++i) {
      r[2 * i] = pk(lo2f(hr[i].x) + mine[0][i], hi2f(hr[i].x) + mine[1][i]);
      r[2 * i + 1] = pk(lo2f(hr[i].y) + mine[2][i], hi2f(hr[i].y) + mine[3][i]);
    }
    u16* op = A.out + (row2 + 16 * w + n16) * D + 64 * ct + 8 * q;
    *(v4u*)op = (v4u){r[0], r[1], r[2], r[3]};
    *(v4u*)(op + 32) = (v4u){r[4], r[5], r[6], r[7]};
  }
}
}

extern "C" void kernel_launch(void* const* d_in, const int* in_sizes, int n_in, void* d_out, int out_size, void* d_ws, size_t ws_size, hipStream_t stream) {
  fk::Args a;
  a.q = (const fk::u16*)d_in[0]; a.k = (const fk::u16*)d_in[1]; a.v = (const fk::u16*)d_in[2]; a.shk = (const fk::u16*)d_in[3];
  a.ssk = (const fk::u16*)d_in[4]; a.ssd = (const fk::u16*)d_in[5]; a.wg = (const fk::u16*)d_in[7]; a.wu = (const fk::u16*)d_in[8];
  a.wd = (const fk::u16*)d_in[9]; a.out = (fk::u16*)d_out; a.ws = (char*)d_ws;
  fk::fused<<<256, 256, 0, stream>>>(a);
}
```

```cpp
#include <hip/hip_runtime.h>
#include <stdint.h>
#define PFD 5

namespace fk {
typedef unsigned u32;
typedef unsigned short u16;
typedef unsigned long long u64;
typedef u32 v4u __attribute__((ext_vector_type(4)));
typedef u32 v2u __attribute__((ext_vector_type(2)));
typedef float f32x4 __attribute__((ext_vector_type(4)));
typedef float f32x2 __attribute__((ext_vector_type(2)));
typedef short s16x4 __attribute__((ext_vector_type(4)));
typedef short s16x8 __attribute__((ext_vector_type(8)));
typedef __bf16 bf2 __attribute__((ext_vector_type(2)));
#define LAS __attribute__((address_space(3)))
#define GAS __attribute__((address_space(1)))
#define RLX __ATOMIC_RELAXED, __HIP_MEMORY_SCOPE_AGENT
#define SB() __builtin_amdgcn_sched_barrier(0)
#define WAITV(n) asm volatile("s_waitcnt vmcnt(%0)" ::"n"(n) : "memory")
#define BAR() do { asm volatile("s_waitcnt lgkmcnt(0)" ::: "memory"); __builtin_amdgcn_s_barrier(); asm volatile("" ::: "memory"); } while (0)

constexpr int S = 2048, D = 512, F = 1792;
constexpr u32 O_EP = 0, O_F1 = 4096, O_C2 = 8192, O_F3 = 32768, O_ST1 = 65536, O_ST2 = 131072;
constexpr u32 O_KVT = 1u << 20, O_QT = 3u << 20, O_HT = 5u << 20, O_W2T = 7u << 20, O_ACT = 9u << 20, O_W1S = 17u << 20, WS_BYTES = 21u << 20;
constexpr u32 O_DIAG = 24u << 20;
constexpr u32 L_WG = 0, L_WU = 57344, L_R = 114688, L_K = L_R, L_V = L_R + 9216, L_Q = L_R + 17408, L_TZ = 36864, TZ_S = 4160, TZ_C = L_WU, L_XR = L_R + 33280, XR_S = 4608,
              L_S1 = L_R, L_EX = L_R + 16384, LDS_BYTES = 163840;
constexpr u32 SPIN_CAP = 1u << 18;
constexpr int SC1 = 16;

struct Args { const u16 *q, *k, *v, *shk, *ssk, *ssd, *wg, *wu, *wd; u16* out; char* ws; };

__device__ __forceinline__ u32 f2u(float x) { return __builtin_bit_cast(u32, x); }
__device__ __forceinline__ float u2f(u32 x) { return __builtin_bit_cast(float, x); }
__device__ __forceinline__ float lo2f(u32 x) { return u2f(x << 16); }
__device__ __forceinline__ float hi2f(u32 x) { return u2f(x & 0xffff0000u); }
__device__ __forceinline__ u32 pk(float lo, float hi) { bf2 r = {(__bf16)lo, (__bf16)hi}; return __builtin_bit_cast(u32, r); }
__device__ __forceinline__ float rbf(float x) { return lo2f(pk(x, x)); }
__device__ __forceinline__ f32x4 mfma(v4u a, v4u b, f32x4 c) {
  return __builtin_amdgcn_mfma_f32_16x16x32_bf16(__builtin_bit_cast(s16x8, a), __builtin_bit_cast(s16x8, b), c, 0, 0, 0);
}
template <bool FIRST> __device__ __forceinline__ void mfv(f32x4& c, v4u a, v4u b) {
  if (FIRST) asm volatile("v_mfma_f32_16x16x32_bf16 %0, %1, %2, 0" : "=&v"(c) : "v"(a), "v"(b));
  else asm volatile("v_mfma_f32_16x16x32_bf16 %0, %1, %2, %0" : "+v"(c) : "v"(a), "v"(b));
}
template <bool FIRST> __device__ __forceinline__ void mfa(f32x4& c, v4u a, v4u b) {
  if (FIRST) asm volatile("v_mfma_f32_16x16x32_bf16 %0, %1, %2, 0" : "=&v"(c) : "a"(a), "v"(b));
  else asm volatile("v_mfma_f32_16x16x32_bf16 %0, %1, %2, %0" : "+v"(c) : "a"(a), "v"(b));
}
__device__ __forceinline__ v2u trd(LAS char* lds, u32 off) {
  return __builtin_bit_cast(v2u, __builtin_amdgcn_ds_read_tr16_b64_v4i16((LAS s16x4*)(lds + off)));
}
__device__ __forceinline__ v4u mkrs(const void* p, u32 bytes) { u64 a = (u64)p; return (v4u){(u32)a, (u32)(a >> 32) & 0xffffu, bytes, 0x00020000u}; }
template <bool COH> __device__ __forceinline__ void dma(u32 lb, u32 lo, u32 voff, v4u rs, u32 sb, u32 so) {
  u32 t;
  if (COH) asm volatile("s_add_u32 m0, %1, %6\n\ts_add_u32 %0, %4, %5\n\tbuffer_load_dwordx4 %2, %3, %0 offen sc1 lds" : "=&s"(t) : "s"(lb), "v"(voff), "s"(rs), "s"(sb), "i"(so), "i"(lo) : "memory", "scc");
  else asm volatile("s_add_u32 m0, %1, %6\n\ts_add_u32 %0, %4, %5\n\tbuffer_load_dwordx4 %2, %3, %0 offen lds" : "=&s"(t) : "s"(lb), "v"(voff), "s"(rs), "s"(sb), "i"(so), "i"(lo) : "memory", "scc");
}
template <int CTRL, int RM> __device__ __forceinline__ float dppadd(float v) {
  return v + __builtin_bit_cast(float, __builtin_amdgcn_update_dpp(0, __builtin_bit_cast(int, v), CTRL, RM, 0xf, true));
}
__device__ __forceinline__ float hsum(float v) {
  v = dppadd<0x111, 0xf>(v); v = dppadd<0x112, 0xf>(v); v = dppadd<0x114, 0xf>(v); v = dppadd<0x118, 0xf>(v);
  return dppadd<0x142, 0xa>(v);
}
#define STAMP(i) do { if ((i) == 0) asm volatile("s_memrealtime %0\n\ts_waitcnt lgkmcnt(0)" : "=s"(pt0)::"memory"); if ((i) == PK) { u64 t_; asm volatile("s_memrealtime %0\n\ts_waitcnt lgkmcnt(0)" : "=s"(t_)::"memory"); ptk = t_; } } while (0)
constexpr int PK = 0;
constexpr int conv_first(int i) { int n = 0; for (int j = 0; j < i; ++j) n += 8 - (j >> 2); return n; }
constexpr int conv_i(int n) { int i = 0; while (conv_first(i + 1) <= n) ++i; return i; }
template <int V> struct IC { static constexpr int value = V; };
template <int B, int E, class Fn> __device__ __forceinline__ void sfor(Fn&& f) { if constexpr (B < E) { f(IC<B>{}); sfor<B + 1, E>(f); } }
constexpr int w2_n(int s) { return s < 1 ? 0 : s < 12 ? 2 : 5; }
constexpr int w2_first(int s) { int n = 0; for (int j = 0; j < s; ++j) n += w2_n(j); return n; }
constexpr int w2_at(int s, int k) { return k < w2_n(s) ? w2_first(s) + k : -1; }
constexpr int W2_TAIL = w2_first(16);
constexpr int vm_after(int s) {
  return w2_n(s) + (s == 8 ? 1 : 0) + (s == 13 ? 4 : 0);
}
constexpr int h_wait(int s) {
  int n = 0;
  if (s < 3) { n = 4 * (2 - s); for (int j = 0; j < s; ++j) n += 4 + vm_after(j); return n; }
  n = vm_after(s - 3);
  for (int j = s - 2; j < s; ++j) n += (j < 12 ? 4 : 0) + vm_after(j);
  return n;
}

__global__ void __launch_bounds__(256, 1) fused(Args A) {
  __shared__ __attribute__((aligned(16))) char smem_[LDS_BYTES];
  LAS char* lds = (LAS char*)smem_;
  const u32 lds0 = (u32)(uintptr_t)lds;
  u64 pt0, ptk;
  STAMP(0);
  ptk = pt0;
  const int b = blockIdx.x, x = b & 7, y = b >> 3;
  const int l = threadIdx.x & 63, w = __builtin_amdgcn_readfirstlane(threadIdx.x >> 6);
  const int n16 = l & 15, q = l >> 4, rr = (l >> 2) & 3, p4 = l & 3;
  const __amdgpu_buffer_rsrc_t WS = __builtin_amdgcn_make_buffer_rsrc(A.ws, 0, WS_BYTES, 0x00020000);
  const v4u rWS = mkrs(A.ws, WS_BYTES);
  const u32 tag = *(const u32*)(A.ws + O_EP) + 1u;
  const u32 dpair = *(const u32*)(A.ssd + 64 * x + 2 * y);

  const int cA = 64 * x + 16 * w + n16;
  const int srow = 8 * b + 2 * w;
  const int ch = w >> 1, par2 = w & 1, cB = 64 * x + 2 * y + ch;
  const int wU = b < 224 ? 2 * b + ch : 0;
  v2u shk; v4u sx0, sx1, kz[2], w1raw[4]; u32 w2raw[8];
  const v2u kedge = *(const v2u*)(A.ssk + cB * S + 1024);
  {
    const v4u rK = mkrs(A.k, S * D * 2), rV = mkrs(A.v, S * D * 2), rQ = mkrs(A.q, S * D * 2);
    const u32 c0 = ((l & 7) ^ ((l >> 3) & 2)) * 16, tv = (u32)((64 * y + (l >> 3)) * 1024 + 128 * x);
    const u32 va = tv + c0, vb = tv + (c0 ^ 64);
    const u32 vk = ((w & 1) ? vb : va) + (u32)(w * 8192 - 8192);
    dma<false>(lds0 + w * 1024, L_K, vk, rK, 0, 0);
    dma<false>(lds0 + w * 1024, L_K + 4096, vk + 32768, rK, 0, 0);
    if (w == 0) dma<false>(lds0, L_K + 8192, vk + 65536, rK, 0, 0);
    dma<false>(lds0 + w * 2048, L_V, va, rV, w * 16384, 0);
    dma<false>(lds0 + w * 2048, L_V + 1024, vb, rV, w * 16384, 8192);
    asm volatile("global_load_dwordx2 %0, %1, %2" : "=&v"(shk) : "v"(cA * 8), "s"(A.shk) : "memory");
    asm volatile("global_load_dwordx4 %0, %2, %3\n\tglobal_load_dwordx4 %1, %2, %3 offset:1024" : "=&v"(kz[0]), "=&v"(kz[1]) : "v"(l * 16), "s"(A.ssk + cB * S + 1024 * par2) : "memory");
    dma<false>(lds0 + w * 2048, L_Q, va, rQ, w * 16384, 0);
    dma<false>(lds0 + w * 2048, L_Q + 1024, vb, rQ, w * 16384, 8192);
    asm volatile("global_load_dwordx4 %0, %2, %3\n\tglobal_load_dwordx4 %1, %2, %3 offset:512" : "=&v"(sx0), "=&v"(sx1) : "v"((l >> 5) * 1024 + (l & 31) * 16), "s"(A.q + srow * D) : "memory");
  }
  u32 w1d[4];
  {
    const v4u rW = mkrs(ch ? A.wu : A.wg, D * F * 2);
    u32 w1s[4];
#pragma unroll
    for (int j = 0; j < 4; ++j) {
      const int P = 128 * j + 64 * par2 + l, c = P / 56, rem = P - 56 * c, r = rem / 7, rb = 32 * (x >> 2) + y;
      w1s[j] = P < 448 ? (u32)((8 * rb + r) * (F * 2) + (8 * (x & 3) + c) * 112 + (rem - 7 * r) * 16) : 0xffffff00u;
      w1d[j] = P < 448 ? O_W1S + (u32)(((8 * (x & 3) + c) * 2 + ch) * 57344 + rb * 896 + rem * 16) : 0xffffff00u;
    }
    asm volatile("buffer_load_dwordx4 %0, %4, %8, 0 offen\n\tbuffer_load_dwordx4 %1, %5, %8, 0 offen\n\tbuffer_load_dwordx4 %2, %6, %8, 0 offen\n\tbuffer_load_dwordx4 %3, %7, %8, 0 offen"
                 : "=&v"(w1raw[0]), "=&v"(w1raw[1]), "=&v"(w1raw[2]), "=&v"(w1raw[3]) : "v"(w1s[0]), "v"(w1s[1]), "v"(w1s[2]), "v"(w1s[3]), "s"(rW) : "memory");
  }
  int oA, oB;
  {
    const int UU = 4 * (wU >> 3) + q, f = UU / 7, u = UU - 7 * f;
    const int rowA = 56 * f + 4 * u + (u >= 4 ? 16 : 0), col = 64 * (wU & 7) + 8 * rr + 4 * (p4 & 1) + 32 * (p4 >> 1) + 2 * par2;
    oA = (rowA * D + col) * 2; oB = oA + (u < 6 ? 16 : 4) * (D * 2);
  }
  const int pan = 4 * (x >> 2) + (y >> 3), fch = 8 * (x & 3) + (y & 7);
  const int rot = 2 * (fch & 1);
  const u32 aAo = (u32)((8 * q + rr) * 128 + (((2 * w + (p4 >> 1)) ^ (rr & 2) ^ ((q & 1) << 2)) << 4) + (p4 & 1) * 8);
  LAS char* pA = lds + L_K + aAo;
  LAS char* pB = lds + L_K + (aAo ^ 64);
  u32 okv = O_KVT + cA * 4096 + (2040 - 32 - 64 * y - 8 * q) * 2, oqt = O_QT + cA * 4096 + (64 * y + 8 * q) * 2;
  LAS char* tw = lds + L_TZ + ch * TZ_C + (128 * par2 + l) * 16 + 32;
  u32 tb = lds0 + L_TZ + ch * TZ_C + ((n16 + 1) & 3) * TZ_S + (u32)((((n16 + 1) >> 2) + 2 * q + 8 * par2) * 8);
  u32 og = (l & 31) == 31 ? O_ST1 + (srow + (l >> 5)) * 8 : 0xffffff00u;
  asm volatile("" : "+v"(w1d[0]), "+v"(w1d[1]), "+v"(w1d[2]), "+v"(w1d[3]), "+v"(pA), "+v"(pB), "+v"(okv), "+v"(oqt), "+v"(oA), "+v"(oB));
  asm volatile("" : "+v"(tw), "+v"(tb), "+v"(og));
  if (w == 0) *(LAS v4u*)(lds + L_XR + (l >> 5) * XR_S + 4096 + (l & 31) * 16) = (v4u){0, 0, 0, 0};
  SB();
  STAMP(23);
  asm volatile("s_waitcnt vmcnt(10)" : "+v"(shk)::"memory");
  BAR();
  STAMP(1);
  {
    v2u ka[2], kb[2], kc[2], va[2], vb[2];
#pragma unroll
    for (int it = 0; it < 2; ++it) { ka[it] = trd(pA, it * 4096 + 512); kb[it] = trd(pB, it * 4096 + 1024); kc[it] = trd(pB, it * 4096 + 1536); }
#pragma unroll
    for (int it = 0; it < 2; ++it) { va[it] = trd(pA, L_V - L_K + it * 4096); vb[it] = trd(pA, L_V - L_K + it * 4096 + 512); }
    SB();
    auto LO = [](u32 a, u32 c) { return (f32x2){lo2f(a), lo2f(c)}; };
    auto HI = [](u32 a, u32 c) { return (f32x2){hi2f(a), hi2f(c)}; };
    const f32x2 w0 = LO(shk.x, shk.x), w1 = HI(shk.x, shk.x), w2 = LO(shk.y, shk.y), w3 = HI(shk.y, shk.y);
    const f32x2 e[12] = {w0, HI(ka[0].x, ka[1].x), LO(ka[0].y, ka[1].y), HI(ka[0].y, ka[1].y), LO(kb[0].x, kb[1].x), HI(kb[0].x, kb[1].x), LO(kb[0].y, kb[1].y),
                         HI(kb[0].y, kb[1].y), LO(kc[0].x, kc[1].x), HI(kc[0].x, kc[1].x), LO(kc[0].y, kc[1].y), HI(kc[0].y, kc[1].y)};
    const f32x2 vv[8] = {LO(va[0].x, va[1].x), HI(va[0].x, va[1].x), LO(va[0].y, va[1].y), HI(va[0].y, va[1].y), LO(vb[0].x, vb[1].x), HI(vb[0].x, vb[1].x), LO(vb[0].y, vb[1].y), HI(vb[0].y, vb[1].y)};
    f32x2 kv[8];
#pragma unroll
    for (int s = 0; s < 8; ++s) kv[s] = (w3 * e[s + 1] + w2 * e[s + 2] + w1 * e[s + 3] + w0 * e[s + 4]) * vv[s];
#pragma unroll
    for (int it = 0; it < 2; ++it)
      __builtin_amdgcn_raw_buffer_store_b128((v4u){pk(kv[7][it], kv[6][it]), pk(kv[5][it], kv[4][it]), pk(kv[3][it], kv[2][it]), pk(kv[1][it], kv[0][it])}, WS, okv + (1 - it) * 64, 0, SC1);
    SB();
    asm volatile("s_waitcnt vmcnt(8)" : "+v"(kz[0]), "+v"(kz[1])::"memory");
    BAR();
    v2u qa[2], qb[2];
#pragma unroll
    for (int it = 0; it < 2; ++it) { qa[it] = trd(pA, L_Q - L_K + it * 4096); qb[it] = trd(pA, L_Q - L_K + it * 4096 + 512); }
#pragma unroll
    for (int it = 0; it < 2; ++it) __builtin_amdgcn_raw_buffer_store_b128((v4u){qa[it].x, qa[it].y, qb[it].x, qb[it].y}, WS, oqt + it * 64, 0, SC1);
    if (y == 0 && w == 0 && l < 8) __builtin_amdgcn_raw_buffer_store_b32(tag * 64u, WS, O_C2 + l * 2048 + x * 256, 0, SC1);
  }
  SB();
  STAMP(24);
  {
    auto put = [&](LAS char* p, v4u d, u32 d4, u32 d5) {
      const u32 e0 = __builtin_amdgcn_alignbit(d.y, d.x, 16), e1 = __builtin_amdgcn_alignbit(d.z, d.y, 16), e2 = __builtin_amdgcn_alignbit(d.w, d.z, 16),
                e3 = __builtin_amdgcn_alignbit(d4, d.w, 16), e4 = __builtin_amdgcn_alignbit(d5, d4, 16);
      *(LAS v4u*)p = d; *(LAS v4u*)(p + TZ_S) = (v4u){e0, e1, e2, e3}; *(LAS v4u*)(p + 2 * TZ_S) = (v4u){d.y, d.z, d.w, d4}; *(LAS v4u*)(p + 3 * TZ_S) = (v4u){e1, e2, e3, e4};
    };
    auto nxt = [](u32 last, u32 v) { return (u32)__builtin_amdgcn_update_dpp((int)last, (int)v, 0x130, 0xf, 0xf, false); };
    put(tw, kz[0], nxt(__builtin_amdgcn_readlane(kz[1].x, 0), kz[0].x), nxt(__builtin_amdgcn_readlane(kz[1].y, 0), kz[0].y));
    put(tw + 1024, kz[1], nxt(par2 ? 0u : kedge.x, kz[1].x), nxt(par2 ? 0u : kedge.y, kz[1].y));
    const u32 f0 = __builtin_amdgcn_readlane(kz[0].x, 0), f1 = __builtin_amdgcn_readlane(kz[0].y, 0);
    if (l < 2 && !par2) put(tw - 32, (v4u){0, 0, 0, 0}, l ? f0 : 0u, l ? f1 : 0u);
  }
  SB();
  asm volatile("s_waitcnt vmcnt(4)" : "+v"(sx0), "+v"(sx1), "+v"(w1raw[0]), "+v"(w1raw[1]), "+v"(w1raw[2]), "+v"(w1raw[3])::"memory");
#pragma unroll
  for (int j = 0; j < 4; ++j) __builtin_amdgcn_raw_buffer_store_b128(w1raw[j], WS, w1d[j], 0, SC1);
  SB();
  {
    const f32x2 v[8] = {{lo2f(sx0.x), hi2f(sx0.x)}, {lo2f(sx0.y), hi2f(sx0.y)}, {lo2f(sx0.z), hi2f(sx0.z)}, {lo2f(sx0.w), hi2f(sx0.w)},
                        {lo2f(sx1.x), hi2f(sx1.x)}, {lo2f(sx1.y), hi2f(sx1.y)}, {lo2f(sx1.z), hi2f(sx1.z)}, {lo2f(sx1.w), hi2f(sx1.w)}};
    const f32x2 s2 = ((v[0] + v[1]) + (v[2] + v[3])) + ((v[4] + v[5]) + (v[6] + v[7]));
    const float sm = hsum(s2[0] + s2[1]);
    const float m0 = u2f(__builtin_amdgcn_readlane(f2u(sm), 31)), m1 = u2f(__builtin_amdgcn_readlane(f2u(sm), 63));
    const float mean = (l < 32 ? m0 : m1) * (1.f / D);
    f32x2 ss = {0.f, 0.f};
#pragma unroll
    for (int j = 0; j < 8; ++j) { const f32x2 d = v[j] - mean; ss += d * d; }
    const float var = hsum(ss[0] + ss[1]) * (1.f / D);
    const u32 pay = pk(mean, __builtin_amdgcn_sqrtf(rbf(rbf(var) + 1e-5f)));
    __builtin_amdgcn_raw_buffer_store_b64((v2u){pay, tag}, WS, og, 0, SC1);
  }
  SB();
  WAITV(1);
  BAR();
  STAMP(2);
  if (w == 0 && l == 0) __builtin_amdgcn_raw_buffer_store_b32(tag, WS, O_F1 + x * 256 + y * 4, 0, SC1);
  v4u T[32];
  {
#pragma unroll
    for (int i0 = 0; i0 < 32; i0 += 4) {
#pragma unroll
      for (int i = i0; i < i0 + 4; ++i) {
        v2u a, c;
        asm volatile("ds_read_b64 %0, %2 offset:%3\n\tds_read_b64 %1, %2 offset:%4" : "=&v"(a), "=&v"(c) : "v"(tb), "i"(128 * i), "i"(128 * i + 8) : "memory");
        T[i] = (v4u){a.x, a.y, c.x, c.y};
      }
    }
    asm volatile("s_waitcnt lgkmcnt(0)" : "+v"(T[0]), "+v"(T[1]), "+v"(T[2]), "+v"(T[3]), "+v"(T[4]), "+v"(T[5]), "+v"(T[6]), "+v"(T[7]), "+v"(T[8]), "+v"(T[9]), "+v"(T[10]),
                 "+v"(T[11]), "+v"(T[12]), "+v"(T[13]), "+v"(T[14]), "+v"(T[15])::"memory");
    asm volatile("" : "+v"(T[16]), "+v"(T[17]), "+v"(T[18]), "+v"(T[19]), "+v"(T[20]), "+v"(T[21]), "+v"(T[22]), "+v"(T[23]), "+v"(T[24]), "+v"(T[25]), "+v"(T[26]),
                 "+v"(T[27]), "+v"(T[28]), "+v"(T[29]), "+v"(T[30]), "+v"(T[31])::"memory");
  }
  const int tB = 1024 * par2 + 16 * n16 + 4 * q;
  LAS char* xp = lds + L_XR + ch * XR_S + (2032 - 16 * n16 + 32 * par2 + 8 * q) * 2 - 3584;
  LAS char* kp = lds + L_XR + ch * XR_S + (2044 - 768 - tB) * 2;
  LAS char* gp = lds + L_S1 + tB * 8;
  u32 qo = O_QT + cB * 4096 + tB * 2, l16 = l * 16;
  asm volatile("" : "+v"(xp), "+v"(kp), "+v"(gp), "+v"(qo), "+v"(l16));
  STAMP(21);
  if (w == 0) {
    for (u32 sp = 0; sp < SPIN_CAP; ++sp) {
      asm volatile("" ::: "memory");
      const u32 f = __builtin_amdgcn_raw_buffer_load_b32(WS, O_F1 + (x ^ (4 * (l >> 5))) * 256 + (l & 31) * 4, 0, SC1);
      if (__all(f == tag)) break;
    }
  }
  STAMP(27);
  BAR();
  STAMP(3);

  dma<true>(lds0 + ch * XR_S + par2 * 2048, L_XR, l16, rWS, cB * 4096 + par2 * 2048, O_KVT);
  dma<true>(lds0 + ch * XR_S + par2 * 2048, L_XR + 1024, l16, rWS, cB * 4096 + par2 * 2048, O_KVT + 1024);
#pragma unroll
  for (int i = 0; i < 4; ++i) dma<true>(lds0 + w * 4096, L_S1 + i * 1024, l16, rWS, w * 4096, O_ST1 + i * 1024);
  v2u xq[4]; v4u g1[8];
  {
    asm volatile("buffer_load_dwordx2 %0, %4, %5, 0 offen sc1\n\tbuffer_load_dwordx2 %1, %4, %5, 0 offen offset:512 sc1\n\t"
                 "buffer_load_dwordx2 %2, %4, %5, 0 offen offset:1024 sc1\n\tbuffer_load_dwordx2 %3, %4, %5, 0 offen offset:1536 sc1"
                 : "=&v"(xq[0]), "=&v"(xq[1]), "=&v"(xq[2]), "=&v"(xq[3]) : "v"(qo), "s"(rWS) : "memory");
  }
  SB();
  WAITV(8);
  BAR();
  STAMP(4);
  auto w1l = [&](int i) { dma<true>(lds0 + w * 1024, i * 4096, l16, rWS, fch * 114688 + w * 1024, O_W1S + i * 4096); };
  f32x4 cacc[8];
  float xf[16], ln[16], kd[16];
  const float dsc = ch ? hi2f(dpair) : lo2f(dpair);
  {
    v4u B[32]; v2u kr[4]; float tm[16];
#pragma unroll
    for (int i = 0; i < 3; ++i) B[i] = *(LAS v4u*)(xp + 128 * (31 - i));
    SB();
    sfor<0, 144>([&](auto N_) {
      {
        constexpr int n = decltype(N_)::value, i = conv_i(n), a = (i >> 2) + n - conv_first(i), eps = 4 * a - i + 3;
        if constexpr (i == 0) cacc[a] = mfma(T[eps], B[i], (f32x4){0.f, 0.f, 0.f, 0.f}); else cacc[a] = mfma(T[eps], B[i], cacc[a]);
        SB();
        if constexpr (n < 87 && n % 3 == 0) B[3 + n / 3] = *(LAS v4u*)(xp + 128 * (28 - n / 3));
        if constexpr (n < 12 && n % 3 == 1) kr[n / 3] = *(LAS v2u*)(kp + 512 * (3 - n / 3));
        if constexpr (n >= 4 && n < 52 && n % 3 == 2) { constexpr int j = (n - 4) / 3; const u32 kk = kr[j >> 2][1 - ((j & 3) >> 1)]; kd[j] = ((j & 1) ? lo2f(kk) : hi2f(kk)) * dsc; asm volatile("" : "+v"(kd[j])); }
        if constexpr (n == 22) {
          asm volatile("s_waitcnt vmcnt(4)" : "+v"(xq[0]), "+v"(xq[1]), "+v"(xq[2]), "+v"(xq[3])::"memory");
          __builtin_amdgcn_s_barrier();
          asm volatile("" ::: "memory");
        }
        if constexpr (n == 23) asm volatile("global_load_dword %0, %4, %5\n\tglobal_load_dword %1, %4, %5 offset:1024\n\tglobal_load_dword %2, %4, %5 offset:2048\n\tglobal_load_dword %3, %4, %5 offset:3072"
                                            : "=&v"(w2raw[0]), "=&v"(w2raw[1]), "=&v"(w2raw[2]), "=&v"(w2raw[3]) : "v"(oA), "s"(A.wd) : "memory");
        if constexpr (n == 35) asm volatile("global_load_dword %0, %4, %5\n\tglobal_load_dword %1, %4, %5 offset:1024\n\tglobal_load_dword %2, %4, %5 offset:2048\n\tglobal_load_dword %3, %4, %5 offset:3072"
                                            : "=&v"(w2raw[4]), "=&v"(w2raw[5]), "=&v"(w2raw[6]), "=&v"(w2raw[7]) : "v"(oB), "s"(A.wd) : "memory");
        if constexpr (n >= 25 && n < 49 && n % 3 == 1) { constexpr int j = (n - 25) / 3; g1[j] = *(LAS v4u*)(gp + (j >> 1) * 2048 + (j & 1) * 16); }
        if constexpr (n < 140 && n % 5 == 2) w1l(n / 5);
        if constexpr (n >= 55 && n < 135 && (n - 55) % 5 < 4) {
          constexpr int j = (n - 55) / 5, ph = (n - 55) % 5;
          const u32 pay = g1[j >> 1][(j & 1) * 2];
          if constexpr (ph == 0) { xf[j] = (j & 1) ? hi2f(xq[j >> 2][(j & 3) >> 1]) : lo2f(xq[j >> 2][(j & 3) >> 1]); asm volatile("" : "+v"(xf[j])); }
          if constexpr (ph == 1) { tm[j] = __builtin_amdgcn_rcpf(hi2f(pay)); asm volatile("" : "+v"(tm[j])); }
          if constexpr (ph == 2) { ln[j] = xf[j] - lo2f(pay); asm volatile("" : "+v"(ln[j])); }
          if constexpr (ph == 3) { ln[j] *= tm[j]; asm volatile("" : "+v"(ln[j])); }
        }
        SB();
      }
    });
  }
  STAMP(5);
  const int ct = y & 7, rbl = y >> 3;
  const u32 w2v = O_W2T + (u32)((ct * 56 + 14 * w) * 4096 + l * 16);
  v4u W2[56];
  {
    bool ok = true;
#pragma unroll
    for (int i = 0; i < 8; ++i) ok &= (g1[i].y == tag) & (g1[i].w == tag);
    if (!__all(ok)) {
      for (u32 sp = 0; sp < SPIN_CAP; ++sp) {
        asm volatile("" ::: "memory");
        ok = true;
#pragma unroll
        for (int i = 0; i < 8; ++i) { g1[i] = __builtin_amdgcn_raw_buffer_load_b128(WS, O_ST1 + tB * 8 + (i >> 1) * 2048 + (i & 1) * 16, 0, SC1); ok &= (g1[i].y == tag) & (g1[i].w == tag); }
        if (__all(ok)) break;
      }
#pragma unroll
      for (int j = 0; j < 16; ++j) { const u32 pay = g1[j >> 1][(j & 1) * 2]; ln[j] = (xf[j] - lo2f(pay)) * __builtin_amdgcn_rcpf(hi2f(pay)); }
    }
    f32x4 mn[4];
    if (par2) {
#pragma unroll
      for (int aa = 0; aa < 4; ++aa) { *(LAS f32x4*)(lds + L_EX + w * 4096 + aa * 1024 + l * 16) = cacc[aa]; mn[aa] = cacc[4 + aa]; asm volatile("" : "+v"(mn[aa])); }
    } else {
#pragma unroll
      for (int aa = 0; aa < 4; ++aa) { *(LAS f32x4*)(lds + L_EX + w * 4096 + aa * 1024 + l * 16) = cacc[4 + aa]; mn[aa] = cacc[aa]; asm volatile("" : "+v"(mn[aa])); }
    }
    BAR();
    u32 hp[4][2];
#pragma unroll
    for (int aa = 0; aa < 4; ++aa) {
      const f32x4 o = *(LAS f32x4*)(lds + L_EX + (w ^ 1) * 4096 + aa * 1024 + l * 16), m = mn[aa];
      float h[4];
#pragma unroll
      for (int i = 0; i < 4; ++i) h[i] = xf[4 * aa + i] + ln[4 * aa + i] * ((m[i] + kd[4 * aa + i]) + o[i]);
      hp[aa][0] = pk(h[0], h[1]); hp[aa][1] = pk(h[2], h[3]);
    }
#pragma unroll
    for (int pr = 0; pr < 2; ++pr) {
      const auto s0 = __builtin_amdgcn_permlane16_swap(hp[2 * pr][0], hp[2 * pr + 1][0], false, false);
      const auto s1 = __builtin_amdgcn_permlane16_swap(hp[2 * pr][1], hp[2 * pr + 1][1], false, false);
      __builtin_amdgcn_raw_buffer_store_b128((v4u){s0[0], s1[0], s0[1], s1[1]}, WS,
          O_HT + cB * 4096 + (1024 * par2 + 256 * (2 * pr + (q & 1)) + 16 * n16 + 8 * (q >> 1)) * 2, 0, SC1);
    }
    SB();
    STAMP(6);
    {
      const u32 w2o = b < 224 ? O_W2T + (u32)(((wU & 7) * 56 + (wU >> 3)) * 4096 + par2 * 2048 + l * 16) : 0xffff0000u;
      asm volatile("s_waitcnt vmcnt(2)" : "+v"(w2raw[0]), "+v"(w2raw[1]), "+v"(w2raw[2]), "+v"(w2raw[3]), "+v"(w2raw[4]), "+v"(w2raw[5]), "+v"(w2raw[6]), "+v"(w2raw[7])::"memory");
#pragma unroll
      for (int pp = 0; pp < 2; ++pp) {
        v4u fr;
#pragma unroll
        for (int d = 0; d < 4; ++d) fr[d] = __builtin_amdgcn_perm(w2raw[2 * d + 1], w2raw[2 * d], pp ? 0x07060302u : 0x05040100u);
        __builtin_amdgcn_raw_buffer_store_b128(fr, WS, w2o + pp * 1024, 0, SC1);
      }
    }
    SB();
    WAITV(0);
    STAMP(28);
    BAR();
    STAMP(7);
    if (w == 0 && l < 8) __hip_atomic_fetch_add((GAS u32*)(A.ws + O_C2 + l * 2048 + x * 256), 1u, RLX);
  }
  const u32 aW = (u32)((16 * (q >> 1) + 8 * (q & 1) + 2 * rr) * 112 + 8 * p4), aM = aW + (p4 < 2 ? 96u : (L_WU + 96u - 16u));
  auto wfrag = [&](u32 bG, u32 bM, int ks, int t) -> v4u {
    const u32 o = ks * 3584;
    v2u a, c;
    if (t == 6) { a = trd(lds, bM + o); c = trd(lds, bM + o + 112); }
    else if (t & 1) { a = trd(lds + L_WU, bG + o + 32 * (t >> 1)); c = trd(lds + L_WU, bG + o + 32 * (t >> 1) + 112); }
    else { a = trd(lds, bG + o + 32 * (t >> 1)); c = trd(lds, bG + o + 32 * (t >> 1) + 112); }
    return (v4u){a.x, a.y, c.x, c.y};
  };
  const v4u ones = {0x3f803f80u, 0x3f803f80u, 0x3f803f80u, 0x3f803f80u};
  f32x4 cs[7], cgm, cum;
  f32x4 acc[7][4];
  v4u g2[4];
  const u32 ring = L_R + w * 12288;
  const int spar = (fch >> 1) & 3;
  {
    const int ssel = (fch >> 3) & 1;
    u32 hv[4];
#pragma unroll
    for (int n = 0; n < 4; ++n)
      hv[n] = (u32)((8 * (l >> 5) + 2 * ((l >> 3) & 3) + 16 * (n >> 1) + (n & 1)) * 4096 + (((l & 7) ^ ((l >> 3) & 6)) << 4) + (256 * pan + 64 * w) * 2 + O_HT - 1024 * n);
    const u32 hb = ring + (u32)((16 * (q >> 1) + 4 * (q & 1) + rr) * 128 + (p4 & 1) * 8);
    u32 hx[4];
#pragma unroll
    for (int pr = 0; pr < 4; ++pr) hx[pr] = hb + (((2 * ((pr + spar) & 3) + (p4 >> 1)) ^ ((4 * (q & 1) + rr) & 6)) << 4);
    u32 sg[4], hs;
#pragma unroll
    for (int g = 0; g < 4; ++g) sg[g] = (u32)((g + rot) & 3);
    auto hdma = [&](int s, int n) {
      if (n == 0) asm volatile("s_add_u32 m0, %1, %2\n\ts_add_u32 %0, %3, %4\n\tbuffer_load_dwordx4 %5, %6, %0 offen sc1 lds"
                               : "=&s"(hs) : "s"(lds0 + ring), "i"((s % 3) * 4096), "s"(sg[s >> 2] * (128 * 4096)), "i"((s & 3) * (32 * 4096)), "v"(hv[0]), "s"(rWS) : "memory", "scc");
      else asm volatile("buffer_load_dwordx4 %0, %1, %2 offen offset:%3 sc1 lds" ::"v"(hv[n]), "s"(rWS), "s"(hs), "i"(n * 1024) : "memory");
    };
    auto hfrag = [&](int s, int pr, int t) { return trd(lds, hx[pr] + (s % 3) * 4096 + t * 1024); };
    f32x4 sS = {0.f, 0.f, 0.f, 0.f}, sQ = sS;
    u32 bG = aW + sg[0] * 14336, bM = aM + sg[0] * 14336;
    v4u wf[3], hf[2][4];
    u32 c0 = aW + (w & 1) * L_WU + 32 * (w >> 1), c1 = w == 3 ? c0 : w == 2 ? aM : c0 + 64;
    f32x4 ca, cb;
    auto crd = [&](u32 base, int ks) { const v2u a = trd(lds, base + ks * 3584), c = trd(lds, base + ks * 3584 + 112); return (v4u){a.x, a.y, c.x, c.y}; };
    {
      asm volatile("" : "+v"(c0), "+v"(c1));
      v4u fa[16], fb_[16];
#pragma unroll
      for (int k = 0; k < 3; ++k) { fa[k] = crd(c0, k); fb_[k] = crd(c1, k); }
      SB();
#pragma unroll
      for (int k = 0; k < 16; ++k) {
        if (k + 3 < 16) { fa[k + 3] = crd(c0, k + 3); fb_[k + 3] = crd(c1, k + 3); }
        ca = mfma(fa[k], ones, k ? +ca : (f32x4){0.f, 0.f, 0.f, 0.f}); cb = mfma(fb_[k], ones, k ? +cb : (f32x4){0.f, 0.f, 0.f, 0.f});
        SB();
      }
    }
    wf[0] = wfrag(bG, bM, 0, 0); wf[1] = wfrag(bG, bM, 0, 1);
    asm volatile("" : "+v"(wf[0]), "+v"(wf[1]), "+v"(bG), "+v"(bM), "+v"(hv[0]), "+v"(hv[1]), "+v"(hv[2]), "+v"(hv[3]), "+v"(hx[0]), "+v"(hx[1]), "+v"(hx[2]), "+v"(hx[3]));
    STAMP(22);
    if (w == 0) {
      const u32 want = tag * 64u + 32u;
      for (u32 sp = 0; sp < SPIN_CAP; ++sp) {
        asm volatile("" ::: "memory");
        const u32 f = __builtin_amdgcn_raw_buffer_load_b32(WS, O_C2 + x * 2048 + (l & 7) * 256, 0, SC1);
        if (__all(f == want)) break;
      }
    }
    STAMP(25);
    STAMP(26);
    BAR();
    STAMP(8);
#pragma unroll
    for (int s = 0; s < 3; ++s)
#pragma unroll
      for (int n = 0; n < 4; ++n) hdma(s, n);
    WAITV(8);
#pragma unroll
    for (int pr = 0; pr < 4; ++pr) { const v2u a = hfrag(0, pr, 0), c = hfrag(0, pr, 1); hf[0][pr] = (v4u){a.x, a.y, c.x, c.y}; }
    asm volatile("" : "+v"(hf[0][0]), "+v"(hf[0][1]), "+v"(hf[0][2]), "+v"(hf[0][3]));
#pragma unroll
    for (int n = 0; n < 4; ++n) hdma(3, n);
    SB();
    STAMP(20);
    sfor<0, 16>([&](auto S_) {
      constexpr int s = decltype(S_)::value;
      v4u(&hc)[4] = hf[s & 1]; v4u(&hn)[4] = hf[(s + 1) & 1];
      u32 bGn = bG, bMn = bM;
      if constexpr ((s & 3) == 3 && s < 15) { bGn = aW + sg[(s + 1) >> 2] * 14336; bMn = aM + sg[(s + 1) >> 2] * 14336; }
      sfor<0, 7>([&](auto T_) {
        constexpr int t = decltype(T_)::value, it = s * 7 + t, nx = it + 2, ns = nx / 7, nt = nx % 7;
        constexpr bool have = nx < 112;
        v2u na, nc, ha;
        const u32 g_ = (ns > s) ? bGn : bG, m_ = (ns > s) ? bMn : bM;
        LAS char* wb = lds + ((nt != 6 && (nt & 1)) ? L_WU : 0);
        const u32 wa = (nt == 6 ? m_ : g_ + 32 * (nt >> 1)) + (ns & 3) * 3584;
        auto w2l = [&](auto K_) { constexpr int i = w2_at(s, decltype(K_)::value); if constexpr (i >= 0) W2[i] = __builtin_amdgcn_raw_buffer_load_b128(WS, w2v + i * 1024, 0, SC1); };
        mfv<s == 0>(acc[t][0], wf[it % 3], hc[0]);
        SB();
        if constexpr (have) na = trd(wb, wa);
        SB();
        mfv<s == 0>(acc[t][1], wf[it % 3], hc[1]);
        SB();
        if constexpr (have) nc = trd(wb, wa + 112);
        SB();
        mfv<s == 0>(acc[t][2], wf[it % 3], hc[2]);
        SB();
        if constexpr (s < 15 && t < 4) { if constexpr (t == 0) { constexpr int hw = h_wait(s); WAITV(hw); } ha = hfrag(s + 1, t, 0); }
        if constexpr (s < 12 && (t == 4 || t == 5)) { if constexpr (t == 4) asm volatile("" : "+v"(hn[0]), "+v"(hn[1]), "+v"(hn[2]), "+v"(hn[3])); hdma(s + 4, 2 * (t - 4)); }
        if constexpr (t == 6) w2l(IC<0>{});
        if constexpr (t == 4) w2l(IC<2>{});
        if constexpr (t == 5) w2l(IC<3>{});
        SB();
        mfv<s == 0>(acc[t][3], wf[it % 3], hc[3]);
        SB();
        if constexpr (s < 15 && t < 4) { const v2u hb_ = hfrag(s + 1, t, 1); hn[t] = (v4u){ha.x, ha.y, hb_.x, hb_.y}; }
        if constexpr (s < 12 && (t == 4 || t == 5)) hdma(s + 4, 2 * (t - 4) + 1);
        if constexpr (t == 6) w2l(IC<1>{});
        if constexpr (t == 4) w2l(IC<4>{});
        if constexpr (have) wf[nx % 3] = (v4u){na.x, na.y, nc.x, nc.y};
        SB();
      });
      if constexpr (s < 8) {
        if (ssel == (s >> 2)) { sS = mfma(ones, hc[0], sS); sQ = mfma(hc[0], hc[0], sQ); }
        SB();
      }
      if constexpr (s == 8) {
        const float q01 = (p4 & 1) ? +sQ[1] : +sQ[0], q23 = (p4 & 1) ? +sQ[3] : +sQ[2], qq = (p4 & 2) ? q23 : q01;
        const u32 off = (fch < 16 && rr == q) ? O_ST2 + (u32)(((ssel + rot) & 3) * 32768 + (256 * pan + 64 * w + 16 * spar + n16) * 16) : 0xffffff00u;
        __builtin_amdgcn_raw_buffer_store_b128((v4u){f2u(sS[0]), tag, f2u(qq), tag}, WS, off, 0, SC1);
        SB();
      }
      if constexpr (s == 13) {
        if (n16 == 0) { *(LAS f32x4*)(lds + ring + 4096 + q * 16) = ca; *(LAS f32x4*)(lds + ring + 4096 + 64 + q * 16) = cb; }
#pragma unroll
        for (int k = 0; k < 4; ++k) g2[k] = __builtin_amdgcn_raw_buffer_load_b128(WS, O_ST2 + k * 32768 + (256 * pan + 64 * w + l) * 16, 0, SC1);
        SB();
      }
      if constexpr (s == 14) { BAR(); SB(); }
      bG = bGn; bM = bMn;
      if constexpr ((s & 3) == 3) STAMP(16 + (s >> 2));
    });
    asm volatile("s_nop 7" ::: "memory");
    SB();
  }
  STAMP(9);
  {
    bool ok = true;
#pragma unroll
    for (int k = 0; k < 4; ++k) ok &= (g2[k].y == tag) & (g2[k].w == tag);
    if (!__all(ok)) {
      for (u32 sp = 0; sp < SPIN_CAP; ++sp) {
        asm volatile("" ::: "memory");
        ok = true;
#pragma unroll
        for (int k = 0; k < 4; ++k) { g2[k] = __builtin_amdgcn_raw_buffer_load_b128(WS, O_ST2 + k * 32768 + (256 * pan + 64 * w + l) * 16, 0, SC1); ok &= (g2[k].y == tag) & (g2[k].w == tag); }
        if (__all(ok)) break;
      }
    }
    const float mean = ((u2f(g2[0].x) + u2f(g2[1].x)) + (u2f(g2[2].x) + u2f(g2[3].x))) * (1.f / D);
    const float v2 = __builtin_fmaxf(((u2f(g2[0].z) + u2f(g2[1].z)) + (u2f(g2[2].z) + u2f(g2[3].z))) * (1.f / D) - mean * mean, 0.f) + 1e-5f;
    *(LAS f32x4*)(lds + ring + l * 16) = (f32x4){-mean, -1.44269504089f * __builtin_amdgcn_rsqf(v2), v2, 0.f};
#pragma unroll
    for (int t = 0; t < 7; ++t) cs[t] = *(LAS f32x4*)(lds + L_R + (t & 3) * 12288 + 4096 + (t >> 2) * 64 + q * 16);
#pragma unroll
    for (int i = 0; i < 4; ++i) {
      const auto s_ = __builtin_amdgcn_permlane32_swap(f2u(cs[6][i]), f2u(cs[6][i]), false, false);
      cgm[i] = u2f(s_[0]); cum[i] = u2f(s_[1]);
    }
    f32x4 st[4], sm[2];
#pragma unroll
    for (int pr = 0; pr < 4; ++pr) st[pr] = *(LAS f32x4*)(lds + ring + (16 * ((pr + spar) & 3) + n16) * 16);
#pragma unroll
    for (int pp = 0; pp < 2; ++pp) sm[pp] = *(LAS f32x4*)(lds + ring + (16 * ((2 * pp + (l >> 5) + spar) & 3) + n16) * 16);
    int w2n = W2_TAIL;
    auto act4 = [&](f32x4 g, f32x4 u, f32x4 cg, f32x4 cu, f32x4 s) -> v2u {
      if (w2n < 56) { W2[w2n] = __builtin_amdgcn_raw_buffer_load_b128(WS, w2v + w2n * 1024, 0, SC1); ++w2n; }
      const f32x4 a = g + s[0] * cg, bb = u + s[0] * cu, t = a * s[1];
      f32x4 e;
#pragma unroll
      for (int i = 0; i < 4; ++i) e[i] = __builtin_amdgcn_exp2f(t[i]);
      e = e * s[2] + s[2];
#pragma unroll
      for (int i = 0; i < 4; ++i) e[i] = __builtin_amdgcn_rcpf(e[i]);
      const f32x4 r = a * bb * e;
      return (v2u){pk(r[0], r[1]), pk(r[2], r[3])};
    };
    const u32 ab = O_ACT + (u32)(((16 * pan + 4 * w) * 224 + 7 * fch) * 256 + n16 * 16);
    v2u X[4];
#pragma unroll
    for (int pr = 0; pr < 4; ++pr) {
      const v2u r0 = act4(acc[0][pr], acc[1][pr], cs[0], cs[1], st[pr]), r1 = act4(acc[2][pr], acc[3][pr], cs[2], cs[3], st[pr]);
      __builtin_amdgcn_raw_buffer_store_b128((v4u){r0.x, r0.y, r1.x, r1.y}, WS, ab + q * 256 + ((pr + spar) & 3) * (224 * 256), 0, SC1);
      X[pr] = act4(acc[4][pr], acc[5][pr], cs[4], cs[5], st[pr]);
      SB();
    }
    v2u Bh[2];
#pragma unroll
    for (int pp = 0; pp < 2; ++pp) {
      f32x4 g, u;
#pragma unroll
      for (int i = 0; i < 4; ++i) {
        const auto s_ = __builtin_amdgcn_permlane32_swap(f2u(acc[6][2 * pp][i]), f2u(acc[6][2 * pp + 1][i]), false, false);
        g[i] = u2f(s_[0]); u[i] = u2f(s_[1]);
      }
      const v2u M = act4(g, u, cgm, cum, sm[pp]);
      const auto x0 = __builtin_amdgcn_permlane32_swap(X[2 * pp].x, X[2 * pp + 1].x, false, false);
      const auto x1 = __builtin_amdgcn_permlane32_swap(X[2 * pp].y, X[2 * pp + 1].y, false, false);
      __builtin_amdgcn_raw_buffer_store_b128((v4u){x0[0], x1[0], M.x, M.y}, WS, ab + (4 + (q & 1)) * 256 + ((2 * pp + (l >> 5) + spar) & 3) * (224 * 256), 0, SC1);
      Bh[pp] = (v2u){x0[1], x1[1]};
    }
    const auto z0 = __builtin_amdgcn_permlane16_swap(Bh[0].x, Bh[1].x, false, false);
    const auto z1 = __builtin_amdgcn_permlane16_swap(Bh[0].y, Bh[1].y, false, false);
    __builtin_amdgcn_raw_buffer_store_b128((v4u){z0[0], z1[0], z0[1], z1[1]}, WS, ab + 6 * 256 + (((((q & 1) << 1) | (q >> 1)) + spar) & 3) * (224 * 256), 0, SC1);
  }
  SB();
  STAMP(10);
  const int row2 = 256 * x + 64 * rbl;
  dma<true>(lds0 + ring, 8192, (u32)((l >> 1) * 4096 + (l & 1) * 16), rWS, (64 * ct) * 4096 + (row2 + 16 * w) * 2, O_HT);
  dma<true>(lds0 + ring, 9216, (u32)((l >> 1) * 4096 + (l & 1) * 16), rWS, (64 * ct) * 4096 + (row2 + 16 * w) * 2, O_HT + 32 * 4096);
  WAITV(2);
  if (l == 0) __builtin_amdgcn_raw_buffer_store_b32(tag, WS, O_F3 + ((pan * 4 + w) * 4 + (fch >> 3)) * 256 + (fch & 7) * 4, 0, SC1);
  if (b == 0 && w == 0 && l == 0) __builtin_amdgcn_raw_buffer_store_b32(tag, WS, O_EP, 0, SC1);
  STAMP(11);
  __builtin_amdgcn_s_barrier();
  for (u32 sp = 0; sp < SPIN_CAP; ++sp) {
    asm volatile("" ::: "memory");
    const u32 f = __builtin_amdgcn_raw_buffer_load_b32(WS, O_F3 + ((x * 4 + rbl) * 4 + w) * 256 + (l & 7) * 4, 0, SC1);
    if (__all(f == tag)) break;
  }
  WAITV(0);
  STAMP(12);

  {
    constexpr int PF = PFD;
    v4u AC[56];
    const u32 av = O_ACT + (u32)(((16 * x + 4 * rbl) * 224 + 56 * w) * 256 + l * 16);
    auto ald = [&](int s) {
#pragma unroll
      for (int rt = 0; rt < 4; ++rt) { AC[4 * s + rt] = __builtin_amdgcn_raw_buffer_load_b128(WS, av + s * 1024, ((rt + w) & 3) * (224 * 256), SC1); SB(); }
    };
#pragma unroll
    for (int s = 0; s < PF; ++s) ald(s);
    v2u hr[4];
#pragma unroll
    for (int i = 0; i < 4; ++i) hr[i] = trd(lds, ring + 8192 + (u32)((8 * q + 4 * (i & 1) + 32 * (i >> 1) + rr) * 32 + p4 * 8));
    SB();
    f32x4 o[4][4];
#pragma unroll
    for (int s = 0; s < 14; ++s) {
#pragma unroll
      for (int rt = 0; rt < 4; ++rt)
#pragma unroll
        for (int pp = 0; pp < 4; ++pp) { if (s == 0) mfa<true>(o[pp][rt], W2[4 * s + pp], AC[4 * s + rt]); else mfa<false>(o[pp][rt], W2[4 * s + pp], AC[4 * s + rt]); }
      SB();
      if (s + PF < 14) ald(s + PF);
    }
    asm volatile("s_nop 7" ::: "memory");
    SB();
    STAMP(13);
    f32x4 mine[4];
#pragma unroll
    for (int rt = 1; rt < 4; ++rt)
#pragma unroll
      for (int pp = 0; pp < 4; ++pp) *(LAS f32x4*)(lds + ring + (rt - 1) * 4096 + pp * 1024 + l * 16) = o[pp][rt];
#pragma unroll
    for (int pp = 0; pp < 4; ++pp) mine[pp] = o[pp][0];
    BAR();
#pragma unroll
    for (int j = 1; j < 4; ++j)
#pragma unroll
      for (int pp = 0; pp < 4; ++pp) mine[pp] += *(LAS f32x4*)(lds + L_R + ((w - j) & 3) * 12288 + (j - 1) * 4096 + pp * 1024 + l * 16);
    u32 r[8];
#pragma unroll
    for (int i = 0; i < 4; ++i) {
      r[2 * i] = pk(lo2f(hr[i].x) + mine[0][i], hi2f(hr[i].x) + mine[1][i]);
      r[2 * i + 1] = pk(lo2f(hr[i].y) + mine[2][i], hi2f(hr[i].y) + mine[3][i]);
    }
    u16* op = A.out + (row2 + 16 * w + n16) * D + 64 * ct + 8 * q;
    *(v4u*)op = (v4u){r[0], r[1], r[2], r[3]};
    *(v4u*)(op + 32) = (v4u){r[4], r[5], r[6], r[7]};
  }
  STAMP(14);
  WAITV(0);
  STAMP(15);
  { const u64 until = pt0 + 5000ull + 100ull * (ptk - pt0); while (__builtin_amdgcn_s_memrealtime() < until) __builtin_amdgcn_s_sleep(8); }
}
}

extern "C" void kernel_launch(void* const* d_in, const int* in_sizes, int n_in, void* d_out, int out_size, void* d_ws, size_t ws_size, hipStream_t stream) {
  fk::Args a;
  a.q = (const fk::u16*)d_in[0]; a.k = (const fk::u16*)d_in[1]; a.v = (const fk::u16*)d_in[2]; a.shk = (const fk::u16*)d_in[3];
  a.ssk = (const fk::u16*)d_in[4]; a.ssd = (const fk::u16*)d_in[5]; a.wg = (const fk::u16*)d_in[7]; a.wu = (const fk::u16*)d_in[8];
  a.wd = (const fk::u16*)d_in[9]; a.out = (fk::u16*)d_out; a.ws = (char*)d_ws;
  fk::fused<<<256, 256, 0, stream>>>(a);
}
```

```cpp
#include <hip/hip_runtime.h>
#include <stdint.h>
#define NWE 20
#define UPOLL 8
#define UCHK 11

namespace fk {
constexpr int WD = 7;
typedef unsigned u32;
typedef unsigned short u16;
typedef unsigned long long u64;
typedef u32 v4u __attribute__((ext_vector_type(4)));
typedef u32 v2u __attribute__((ext_vector_type(2)));
typedef float f32x4 __attribute__((ext_vector_type(4)));
typedef float f32x2 __attribute__((ext_vector_type(2)));
typedef short s16x4 __attribute__((ext_vector_type(4)));
typedef short s16x8 __attribute__((ext_vector_type(8)));
typedef __bf16 bf2 __attribute__((ext_vector_type(2)));
#define LAS __attribute__((address_space(3)))
#define GAS __attribute__((address_space(1)))
#define RLX __ATOMIC_RELAXED, __HIP_MEMORY_SCOPE_AGENT
#define SB() __builtin_amdgcn_sched_barrier(0)
#define WAITV(n) asm volatile("s_waitcnt vmcnt(%0)" ::"n"(n) : "memory")
#define BAR() do { asm volatile("s_waitcnt lgkmcnt(0)" ::: "memory"); __builtin_amdgcn_s_barrier(); asm volatile("" ::: "memory"); } while (0)

constexpr int S = 2048, D = 512, F = 1792;
constexpr u32 O_EP = 0, O_C2 = 16384, O_ST1 = 65536, O_F1 = 98304, O_ST2 = 131072, O_F3 = 262144;
constexpr u32 O_KVT = 1u << 20, O_QT = 3u << 20, O_HT = 5u << 20, O_W2T = 7u << 20, O_ACT = 9u << 20, O_W1F = 17u << 20, WS_BYTES = 21u << 20;
constexpr u32 O_DIAG = 24u << 20;
constexpr u32 L_R = 114688, L_K = L_R, L_V = L_R + 9216, L_Q = L_R + 17408, L_WR = 0, WR_S = 1056, L_TZ = 36864, TZ_S = 4160, TZ_C = 4 * TZ_S, L_XR = L_R + 33280, XR_S = 4608,
              L_S1 = L_R, L_EX = L_R + 16384, LDS_BYTES = 163840;
constexpr u32 SPIN_CAP = 1u << 18;
constexpr int SC1 = 16;

struct Args { const u16 *q, *k, *v, *shk, *ssk, *ssd, *wg, *wu, *wd; u16* out; char* ws; };

__device__ __forceinline__ u32 f2u(float x) { return __builtin_bit_cast(u32, x); }
__device__ __forceinline__ float u2f(u32 x) { return __builtin_bit_cast(float, x); }
__device__ __forceinline__ float lo2f(u32 x) { return u2f(x << 16); }
__device__ __forceinline__ float hi2f(u32 x) { return u2f(x & 0xffff0000u); }
__device__ __forceinline__ u32 pk(float lo, float hi) { bf2 r = {(__bf16)lo, (__bf16)hi}; return __builtin_bit_cast(u32, r); }
__device__ __forceinline__ float rbf(float x) { return lo2f(pk(x, x)); }
__device__ __forceinline__ f32x4 mfma(v4u a, v4u b, f32x4 c) {
  return __builtin_amdgcn_mfma_f32_16x16x32_bf16(__builtin_bit_cast(s16x8, a), __builtin_bit_cast(s16x8, b), c, 0, 0, 0);
}
template <bool FIRST> __device__ __forceinline__ void mfv(f32x4& c, v4u a, v4u b) {
  if (FIRST) asm volatile("v_mfma_f32_16x16x32_bf16 %0, %1, %2, 0" : "=&v"(c) : "v"(a), "v"(b));
  else asm volatile("v_mfma_f32_16x16x32_bf16 %0, %1, %2, %0" : "+v"(c) : "v"(a), "v"(b));
}
template <bool FIRST> __device__ __forceinline__ void mfa(f32x4& c, v4u a, v4u b) {
  if (FIRST) asm volatile("v_mfma_f32_16x16x32_bf16 %0, %1, %2, 0" : "=&v"(c) : "a"(a), "v"(b));
  else asm volatile("v_mfma_f32_16x16x32_bf16 %0, %1, %2, %0" : "+v"(c) : "a"(a), "v"(b));
}
template <bool FIRST> __device__ __forceinline__ void mfb(f32x4& c, v4u a, v4u b) {
  if (FIRST) asm volatile("v_mfma_f32_16x16x32_bf16 %0, %1, %2, 0" : "=&v"(c) : "v"(a), "a"(b));
  else asm volatile("v_mfma_f32_16x16x32_bf16 %0, %1, %2, %0" : "+v"(c) : "v"(a), "a"(b));
}
__device__ __forceinline__ v2u trd(LAS char* lds, u32 off) {
  return __builtin_bit_cast(v2u, __builtin_amdgcn_ds_read_tr16_b64_v4i16((LAS s16x4*)(lds + off)));
}
__device__ __forceinline__ v4u mkrs(const void* p, u32 bytes) { u64 a = (u64)p; return (v4u){(u32)a, (u32)(a >> 32) & 0xffffu, bytes, 0x00020000u}; }
template <bool COH> __device__ __forceinline__ void dma(u32 lb, u32 lo, u32 voff, v4u rs, u32 sb, u32 so) {
  u32 t;
  if (COH) asm volatile("s_add_u32 m0, %1, %6\n\ts_add_u32 %0, %4, %5\n\tbuffer_load_dwordx4 %2, %3, %0 offen sc1 lds" : "=&s"(t) : "s"(lb), "v"(voff), "s"(rs), "s"(sb), "i"(so), "i"(lo) : "memory", "scc");
  else asm volatile("s_add_u32 m0, %1, %6\n\ts_add_u32 %0, %4, %5\n\tbuffer_load_dwordx4 %2, %3, %0 offen lds" : "=&s"(t) : "s"(lb), "v"(voff), "s"(rs), "s"(sb), "i"(so), "i"(lo) : "memory", "scc");
}
template <int CTRL, int RM> __device__ __forceinline__ float dppadd(float v) {
  return v + __builtin_bit_cast(float, __builtin_amdgcn_update_dpp(0, __builtin_bit_cast(int, v), CTRL, RM, 0xf, true));
}
__device__ __forceinline__ float hsum(float v) {
  v = dppadd<0x111, 0xf>(v); v = dppadd<0x112, 0xf>(v); v = dppadd<0x114, 0xf>(v); v = dppadd<0x118, 0xf>(v);
  return dppadd<0x142, 0xa>(v);
}
#define STAMP(i)
template <int V> struct IC { static constexpr int value = V; };
template <int B, int E, class Fn> __device__ __forceinline__ void sfor(Fn&& f) { if constexpr (B < E) { f(IC<B>{}); sfor<B + 1, E>(f); } }
constexpr int cv_a0(int u) { return u < 16 ? u >> 2 : ((u - 16) >> 2) > 4 ? (u - 16) >> 2 : 4; }
constexpr int cv_first(int u) { int n = 0; for (int j = 0; j < u; ++j) n += (j < 16 ? 4 : 8) - cv_a0(j); return n; }
constexpr int cv_u(int n) { int u = 0; while (cv_first(u + 1) <= n) ++u; return u; }
constexpr int cv_slot(int i) { int s_ = cv_first(i < 16 ? i : i + 16) - 12; if (i < 31) { const int v = cv_slot(i + 1) - 2; if (v < s_) s_ = v; } return s_ & ~1; }
constexpr int cv_at(int n) { for (int i = 0; i < 32; ++i) if (cv_slot(i) == n) return i; return -1; }
constexpr int w2_n(int g) { return g >= 27 ? 0 : (g & 15) < 11 ? ((g & 1) ^ 1) : 3; }
constexpr int W2_EPI = 56 - 27 - NWE;
constexpr int w2_first(int g) { int n = NWE + (g > 15 ? W2_EPI : 0); for (int j = 0; j < g; ++j) n += w2_n(j); return n; }
constexpr int a_n(int g) { return g >= 28 ? 4 : 0; }
constexpr int vm_tail(int g) { return (g == 4) + ((g & 15) == 5) + 2 * ((g & 15) == 12) + (g == 13) + (g == 19) + (g == 25) + 2 * (g == 27); }
constexpr int vm_ops(int g) { return w2_n(g) + a_n(g) + ((g & 15) < 11 ? 2 : 0) + vm_tail(g); }
constexpr int vm_sum(int a, int e) { int n = 0; for (int j = a; j < e; ++j) n += vm_ops(j); return n; }
constexpr int vm_pre(int ps) { return ps ? W2_EPI + 4 : 1; }
constexpr int h_wait(int g) {
  return (g & 15) < 4 ? 2 * (3 - (g & 15)) + vm_pre(g >> 4) + vm_sum(g & 16, g) : vm_tail(g - 4) + vm_sum(g - 3, g);
}
static_assert(w2_first(32) == 56 && W2_EPI >= 0 && W2_EPI <= 14, "all w_down fragments are requested");

__global__ void __launch_bounds__(256, 1) fused(Args A) {
  __shared__ __attribute__((aligned(16))) char smem_[LDS_BYTES];
  LAS char* lds = (LAS char*)smem_;
  const u32 lds0 = (u32)(uintptr_t)lds;
  STAMP(0);
  const int b = blockIdx.x, x = b & 7, y = b >> 3;
  const int l = threadIdx.x & 63, w = __builtin_amdgcn_readfirstlane(threadIdx.x >> 6);
  const int n16 = l & 15, q = l >> 4, rr = (l >> 2) & 3, p4 = l & 3;
  const __amdgpu_buffer_rsrc_t WS = __builtin_amdgcn_make_buffer_rsrc(A.ws, 0, WS_BYTES, 0x00020000);
  const v4u rWS = mkrs(A.ws, WS_BYTES);
  auto spin = [&](u32 off, u32 want) {
    for (u32 sp = 0; sp < SPIN_CAP; ++sp) {
      asm volatile("" ::: "memory");
      if (__all(__builtin_amdgcn_raw_buffer_load_b32(WS, off, 0, SC1) == want)) break;
    }
  };

  const int ti = 4 * x + (y >> 3), cj = y & 7;
  const int cA = 64 * cj + 16 * w + n16;
  const int srow = 256 * x + 8 * y + 2 * w;
  const int ch = w >> 1, par2 = w & 1, cB = 64 * x + 2 * y + ch;
  v2u shk; v4u sx0, sx1, kz[2];
  v2u kedge; u32 vtag, vdp;
  u32 l16 = l * 16;
  {
    const v4u rK = mkrs(A.k, S * D * 2), rV = mkrs(A.v, S * D * 2), rQ = mkrs(A.q, S * D * 2);
    const u32 c0 = ((l & 7) ^ ((l >> 3) & 2)) * 16, tv = (u32)((64 * ti + (l >> 3)) * 1024 + 128 * cj);
    const u32 va = tv + c0, vb = tv + (c0 ^ 64);
    const u32 vk = ((w & 1) ? vb : va) + (u32)(w * 8192 - 8192);
    dma<false>(lds0 + w * 1024, L_K, vk, rK, 0, 0);
    dma<false>(lds0 + w * 1024, L_K + 4096, vk + 32768, rK, 0, 0);
    if (w == 0) dma<false>(lds0, L_K + 8192, vk + 65536, rK, 0, 0);
    dma<false>(lds0 + w * 2048, L_V, va, rV, w * 16384, 0);
    dma<false>(lds0 + w * 2048, L_V + 1024, vb, rV, w * 16384, 8192);
    asm volatile("global_load_dwordx2 %0, %1, %2" : "=&v"(shk) : "v"(cA * 8), "s"(A.shk) : "memory");
    asm volatile("global_load_dwordx4 %0, %2, %3\n\tglobal_load_dwordx4 %1, %2, %3 offset:1024" : "=&v"(kz[0]), "=&v"(kz[1]) : "v"(l16), "s"(A.ssk + cB * S + 1024 * par2) : "memory");
    asm volatile("global_load_dwordx2 %0, %3, %4 offset:2048\n\tglobal_load_dword %1, %3, %5\n\tglobal_load_dword %2, %3, %6"
                 : "=&v"(kedge), "=&v"(vtag), "=&v"(vdp) : "v"(0), "s"(A.ssk + cB * S), "s"(A.ws + O_EP), "s"(A.ssd + 64 * x + 2 * y) : "memory");
    dma<false>(lds0 + w * 2048, L_Q, va, rQ, w * 16384, 0);
    dma<false>(lds0 + w * 2048, L_Q + 1024, vb, rQ, w * 16384, 8192);
    asm volatile("global_load_dwordx4 %0, %2, %3\n\tglobal_load_dwordx4 %1, %2, %3 offset:512" : "=&v"(sx0), "=&v"(sx1) : "v"((l >> 5) * 1024 + (l & 31) * 16), "s"(A.q + srow * D) : "memory");
  }
  const int rb = 8 * x + 2 * (y >> 3) + ((y >> 2) & 1), fg = y & 3;
  const v4u rW = mkrs(ch ? A.wu : A.wg, D * F * 2);
  u32 wrv = l < 56 ? l16 : 0xffffff00u, w1d[4];
  LAS char* pW = lds + L_WR + ((p4 >> 1) * 8 + rr) * WR_S + (4 * w + q) * 16 + (p4 & 1) * 8;
#pragma unroll
  for (int j = 0; j < 4; ++j) {
    int Z = 16 * j + 4 * w + q;
    asm volatile("" : "+v"(Z));
    const int c = (Z * 37) >> 8;
    w1d[j] = (j < 3 || Z < 56) ? O_W1F + (u32)(((8 * fg + c) * 16 + (rb >> 2)) * 7168 + (Z - 7 * c) * 1024 + (rb & 3) * 256 + (4 * ((n16 & 7) >> 1) + 2 * (n16 >> 3) + (n16 & 1)) * 16) : 0xffffff00u;
  }
  const u32 aAo = (u32)((8 * q + rr) * 128 + (((2 * w + (p4 >> 1)) ^ (rr & 2) ^ ((q & 1) << 2)) << 4) + (p4 & 1) * 8);
  LAS char* pA = lds + L_K + aAo;
  LAS char* pB = lds + L_K + (aAo ^ 64);
  u32 okv = O_KVT + cA * 4096 + (2040 - 32 - 64 * ti - 8 * q) * 2, oqt = O_QT + cA * 4096 + (64 * ti + 8 * q) * 2;
  LAS char* tw = lds + L_TZ + ch * TZ_C + (128 * par2 + l) * 16 + 32;
  u32 og = (l & 31) == 31 ? O_ST1 + (srow + (l >> 5)) * 8 : 0xffffff00u;
  u32 oci = (x == 0 && y < 8 && w == 0 && l < 24) ? O_C2 + l * 2048 + y * 256 : 0xffffff00u;
  asm volatile("" : "+v"(w1d[0]), "+v"(w1d[1]), "+v"(w1d[2]), "+v"(w1d[3]), "+v"(pA), "+v"(pB), "+v"(okv), "+v"(oqt), "+v"(tw), "+v"(og), "+v"(wrv), "+v"(pW), "+v"(oci));
  const int wU = b < 224 ? 2 * b + ch : 0;
  u32 ow[4], w2o = b < 224 ? O_W2T + (u32)(((wU & 7) * 56 + (wU >> 3)) * 4096 + par2 * 2048) + l16 : 0xffff0000u;
  {
    const int UU = 4 * (wU >> 3) + q, f = UU / 7, u = UU - 7 * f;
    const int col = 64 * (wU & 7) + 8 * rr + 4 * (p4 & 1) + 32 * (p4 >> 1) + 2 * par2;
#pragma unroll
    for (int d = 0; d < 4; ++d) ow[d] = (u32)(((56 * f + (u < 4 ? 2 * u + 8 * d : 8 * u + 2 * d)) * D + col) * 2);
  }
  asm volatile("" : "+v"(w2o), "+v"(ow[0]), "+v"(ow[1]), "+v"(ow[2]), "+v"(ow[3]));
  if (w == 0) *(LAS v4u*)(lds + L_XR + (l >> 5) * XR_S + 4096 + (l & 31) * 16) = (v4u){0, 0, 0, 0};
  SB();
  STAMP(23);
  asm volatile("s_waitcnt vmcnt(9)" : "+v"(shk)::"memory");
  BAR();
  STAMP(1);
  u32 tag, dpair;
  {
    v2u ka[2], kb[2], kc[2], va[2], vb[2];
#pragma unroll
    for (int it = 0; it < 2; ++it) { ka[it] = trd(pA, it * 4096 + 512); kb[it] = trd(pB, it * 4096 + 1024); kc[it] = trd(pB, it * 4096 + 1536); }
#pragma unroll
    for (int it = 0; it < 2; ++it) { va[it] = trd(pA, L_V - L_K + it * 4096); vb[it] = trd(pA, L_V - L_K + it * 4096 + 512); }
    SB();
#pragma unroll
    for (int j = 0; j < 4; ++j) dma<false>(lds0 + (8 * ch + 4 * par2) * WR_S, L_WR + j * WR_S, wrv, rW, (8 * rb + 4 * par2) * (F * 2) + fg * 896, j * (F * 2));
    SB();
    auto LO = [](u32 a, u32 c) { return (f32x2){lo2f(a), lo2f(c)}; };
    auto HI = [](u32 a, u32 c) { return (f32x2){hi2f(a), hi2f(c)}; };
    const f32x2 w0 = LO(shk.x, shk.x), w1 = HI(shk.x, shk.x), w2 = LO(shk.y, shk.y), w3 = HI(shk.y, shk.y);
    const f32x2 e[12] = {w0, HI(ka[0].x, ka[1].x), LO(ka[0].y, ka[1].y), HI(ka[0].y, ka[1].y), LO(kb[0].x, kb[1].x), HI(kb[0].x, kb[1].x), LO(kb[0].y, kb[1].y),
                         HI(kb[0].y, kb[1].y), LO(kc[0].x, kc[1].x), HI(kc[0].x, kc[1].x), LO(kc[0].y, kc[1].y), HI(kc[0].y, kc[1].y)};
    const f32x2 vv[8] = {LO(va[0].x, va[1].x), HI(va[0].x, va[1].x), LO(va[0].y, va[1].y), HI(va[0].y, va[1].y), LO(vb[0].x, vb[1].x), HI(vb[0].x, vb[1].x), LO(vb[0].y, vb[1].y), HI(vb[0].y, vb[1].y)};
    f32x2 kv[8];
#pragma unroll
    for (int s = 0; s < 8; ++s) kv[s] = (w3 * e[s + 1] + w2 * e[s + 2] + w1 * e[s + 3] + w0 * e[s + 4]) * vv[s];
#pragma unroll
    for (int it = 0; it < 2; ++it)
      __builtin_amdgcn_raw_buffer_store_b128((v4u){pk(kv[7][it], kv[6][it]), pk(kv[5][it], kv[4][it]), pk(kv[3][it], kv[2][it]), pk(kv[1][it], kv[0][it])}, WS, okv + (1 - it) * 64, 0, SC1);
    SB();
    asm volatile("s_waitcnt vmcnt(8)" : "+v"(kz[0]), "+v"(kz[1]), "+v"(kedge), "+v"(vtag), "+v"(vdp)::"memory");
    BAR();
    vtag += 1u;
    tag = __builtin_amdgcn_readfirstlane(vtag); dpair = __builtin_amdgcn_readfirstlane(vdp);
    v2u qa[2], qb[2];
#pragma unroll
    for (int it = 0; it < 2; ++it) { qa[it] = trd(pA, L_Q - L_K + it * 4096); qb[it] = trd(pA, L_Q - L_K + it * 4096 + 512); }
#pragma unroll
    for (int it = 0; it < 2; ++it) __builtin_amdgcn_raw_buffer_store_b128((v4u){qa[it].x, qa[it].y, qb[it].x, qb[it].y}, WS, oqt + it * 64, 0, SC1);
    __builtin_amdgcn_raw_buffer_store_b32(tag * 64u, WS, oci, 0, SC1);
  }
  SB();
  STAMP(24);
  {
    auto put = [&](LAS char* p, v4u d, u32 d4, u32 d5) {
      const u32 e0 = __builtin_amdgcn_alignbit(d.y, d.x, 16), e1 = __builtin_amdgcn_alignbit(d.z, d.y, 16), e2 = __builtin_amdgcn_alignbit(d.w, d.z, 16),
                e3 = __builtin_amdgcn_alignbit(d4, d.w, 16), e4 = __builtin_amdgcn_alignbit(d5, d4, 16);
      *(LAS v4u*)p = d; *(LAS v4u*)(p + TZ_S) = (v4u){e0, e1, e2, e3}; *(LAS v4u*)(p + 2 * TZ_S) = (v4u){d.y, d.z, d.w, d4}; *(LAS v4u*)(p + 3 * TZ_S) = (v4u){e1, e2, e3, e4};
    };
    auto nxt = [](u32 last, u32 v) { return (u32)__builtin_amdgcn_update_dpp((int)last, (int)v, 0x130, 0xf, 0xf, false); };
    put(tw, kz[0], nxt(__builtin_amdgcn_readlane(kz[1].x, 0), kz[0].x), nxt(__builtin_amdgcn_readlane(kz[1].y, 0), kz[0].y));
    put(tw + 1024, kz[1], nxt(par2 ? 0u : kedge.x, kz[1].x), nxt(par2 ? 0u : kedge.y, kz[1].y));
    const u32 f0 = __builtin_amdgcn_readlane(kz[0].x, 0), f1 = __builtin_amdgcn_readlane(kz[0].y, 0);
    if (l < 2 && !par2) put(tw - 32, (v4u){0, 0, 0, 0}, l ? f0 : 0u, l ? f1 : 0u);
  }
  SB();
  asm volatile("s_waitcnt vmcnt(5)" : "+v"(sx0), "+v"(sx1)::"memory");
  BAR();
  {
    v2u ta[4], tc[4];
#pragma unroll
    for (int j = 0; j < 4; ++j) { ta[j] = trd(pW, j * 256); tc[j] = trd(pW, j * 256 + 4 * WR_S); }
#pragma unroll
    for (int j = 0; j < 4; ++j) __builtin_amdgcn_raw_buffer_store_b128((v4u){ta[j].x, ta[j].y, tc[j].x, tc[j].y}, WS, w1d[j], 0, SC1);
  }
  SB();
  {
    const f32x2 v[8] = {{lo2f(sx0.x), hi2f(sx0.x)}, {lo2f(sx0.y), hi2f(sx0.y)}, {lo2f(sx0.z), hi2f(sx0.z)}, {lo2f(sx0.w), hi2f(sx0.w)},
                        {lo2f(sx1.x), hi2f(sx1.x)}, {lo2f(sx1.y), hi2f(sx1.y)}, {lo2f(sx1.z), hi2f(sx1.z)}, {lo2f(sx1.w), hi2f(sx1.w)}};
    const f32x2 s2 = ((v[0] + v[1]) + (v[2] + v[3])) + ((v[4] + v[5]) + (v[6] + v[7]));
    const float sm = hsum(s2[0] + s2[1]);
    const float m0 = u2f(__builtin_amdgcn_readlane(f2u(sm), 31)), m1 = u2f(__builtin_amdgcn_readlane(f2u(sm), 63));
    const float mean = (l < 32 ? m0 : m1) * (1.f / D);
    f32x2 ss = {0.f, 0.f};
#pragma unroll
    for (int j = 0; j < 8; ++j) { const f32x2 d = v[j] - mean; ss += d * d; }
    const float var = hsum(ss[0] + ss[1]) * (1.f / D);
    const u32 pay = pk(mean, __builtin_amdgcn_sqrtf(rbf(rbf(var) + 1e-5f)));
    __builtin_amdgcn_raw_buffer_store_b64((v2u){pay, vtag}, WS, og, 0, SC1);
  }
  SB();
  WAITV(1);
  BAR();
  STAMP(2);
  if (w == 0 && l == 0) __builtin_amdgcn_raw_buffer_store_b32(tag, WS, O_F1 + (cj * 32 + ti) * 128, 0, SC1);
  u32 w2raw[8];
  asm volatile("global_load_dword %0, %8, %12\n\tglobal_load_dword %1, %8, %12 offset:1024\n\tglobal_load_dword %2, %9, %12\n\tglobal_load_dword %3, %9, %12 offset:1024\n\t"
               "global_load_dword %4, %10, %12\n\tglobal_load_dword %5, %10, %12 offset:1024\n\tglobal_load_dword %6, %11, %12\n\tglobal_load_dword %7, %11, %12 offset:1024"
               : "=&v"(w2raw[0]), "=&v"(w2raw[1]), "=&v"(w2raw[2]), "=&v"(w2raw[3]), "=&v"(w2raw[4]), "=&v"(w2raw[5]), "=&v"(w2raw[6]), "=&v"(w2raw[7])
               : "v"(ow[0]), "v"(ow[1]), "v"(ow[2]), "v"(ow[3]), "s"(A.wd) : "memory");
  v4u T[32];
  {
    const u32 tb = lds0 + L_TZ + ch * TZ_C + ((n16 + 1) & 3) * TZ_S + (u32)((((n16 + 1) >> 2) + 2 * q + 8 * par2) * 8);
#pragma unroll
    for (int i = 0; i < 32; ++i) {
      v2u a, c;
      asm volatile("ds_read_b64 %0, %2 offset:%3\n\tds_read_b64 %1, %2 offset:%4" : "=&v"(a), "=&v"(c) : "v"(tb), "i"(128 * i), "i"(128 * i + 8) : "memory");
      T[i] = (v4u){a.x, a.y, c.x, c.y};
    }
    asm volatile("s_waitcnt lgkmcnt(0)" : "+v"(T[0]), "+v"(T[1]), "+v"(T[2]), "+v"(T[3]), "+v"(T[4]), "+v"(T[5]), "+v"(T[6]), "+v"(T[7]), "+v"(T[8]), "+v"(T[9]), "+v"(T[10]),
                 "+v"(T[11]), "+v"(T[12]), "+v"(T[13]), "+v"(T[14]), "+v"(T[15])::"memory");
    asm volatile("" : "+v"(T[16]), "+v"(T[17]), "+v"(T[18]), "+v"(T[19]), "+v"(T[20]), "+v"(T[21]), "+v"(T[22]), "+v"(T[23]), "+v"(T[24]), "+v"(T[25]), "+v"(T[26]),
                 "+v"(T[27]), "+v"(T[28]), "+v"(T[29]), "+v"(T[30]), "+v"(T[31])::"memory");
  }
  const int tB = 512 * par2 + 16 * n16 + 4 * q;
  LAS char* xp = lds + L_XR + ch * XR_S + (2032 - 16 * n16 + 32 * par2 + 8 * q) * 2 - 3584;
  LAS char* kp = lds + L_XR + ch * XR_S + (2044 - 1280 - tB) * 2;
  LAS char* gp = lds + L_S1 + tB * 8;
  LAS char* ep = lds + L_EX + w * 4096 + l * 16;
  LAS char* er = lds + L_EX + (w ^ 1) * 4096 + l * 16;
  u32 qo = O_QT + cB * 4096 + tB * 2;
  u32 oh = O_HT + (u32)((8 * (2 * par2 + (q & 1)) + (n16 >> 1)) * 32768 + (cB >> 5) * 2048 + (cB & 31) * 64 + (((2 * (n16 & 1) + (q >> 1)) ^ (2 * ((cB >> 3) & 1))) << 4));
  asm volatile("" : "+v"(xp), "+v"(kp), "+v"(gp), "+v"(ep), "+v"(er), "+v"(qo), "+v"(oh), "+v"(l16));
  const int hp = 4 * (x >> 2) + (y >> 3), fch = 8 * (x & 3) + (y & 7);
  const int ct = y & 7, rbl = y >> 3;
  const u32 ring = L_R + w * 12288;
  const int rot = fch & 3, spar = (fch >> 2) & 1;
  u32 w2v = O_W2T + (u32)((ct * 56 + 14 * w) * 4096) + l16;
  u32 ost = (fch < 8 && rr == q) ? O_ST2 + (u32)(rot * 32768 + (128 * hp + 32 * w + 16 * spar + n16) * 16) : 0xffff0000u;
  u32 og2 = O_ST2 + (u32)((l >> 5) * 65536 + (128 * hp + 32 * w + (l & 31)) * 16);
  u32 oact = O_ACT + (u32)(((8 * hp + 2 * w) * 224 + 7 * fch + q) * 256 + n16 * 16), oact2 = q < 3 ? oact + 1024 : 0xff000000u;
  u32 of3 = O_F3 + (u32)(((x * 4 + rbl) * 4 + w) * 128 + (l & 7) * 4), of3w = l == 0 ? O_F3 + (u32)(((hp * 4 + w) * 4 + (fch >> 3)) * 128 + (fch & 7) * 4) : 0xffff0000u;
  u32 av = O_ACT + (u32)(((8 * x + 2 * rbl) * 224 + 56 * w) * 256) + l16;
  LAS char* hx[2];
#pragma unroll
  for (int pr = 0; pr < 2; ++pr) hx[pr] = lds + ring + (8 * q + rr) * 64 + (((2 * (pr ^ spar) + (p4 >> 1)) ^ (2 * (q & 1))) << 4) + (p4 & 1) * 8;
  LAS char* pcs = lds + ring + 10240 + (n16 == 0 ? q * 16 : 128 + l * 16);
  LAS char* pcr = lds + L_R + 10240 + q * 16;
  asm volatile("" : "+v"(av), "+v"(pcs), "+v"(pcr), "+v"(w2v), "+v"(ost), "+v"(og2), "+v"(oact), "+v"(oact2), "+v"(of3), "+v"(of3w), "+v"(hx[0]), "+v"(hx[1]));
  STAMP(21);
  if (w == 0) spin(O_F1 + ((x ^ (4 * (l >> 5))) * 32 + (l & 31)) * 128, tag);
  BAR();
  STAMP(3);

  dma<true>(lds0 + ch * XR_S + par2 * 2048, L_XR, l16, rWS, cB * 4096 + par2 * 2048, O_KVT);
  dma<true>(lds0 + ch * XR_S + par2 * 2048, L_XR + 1024, l16, rWS, cB * 4096 + par2 * 2048, O_KVT + 1024);
#pragma unroll
  for (int i = 0; i < 4; ++i) dma<true>(lds0 + w * 4096, L_S1 + i * 1024, l16, rWS, w * 4096, O_ST1 + i * 1024);
  v2u xq[4];
  asm volatile("buffer_load_dwordx2 %0, %4, %5, 0 offen sc1\n\tbuffer_load_dwordx2 %1, %4, %5, 0 offen offset:512 sc1\n\t"
               "buffer_load_dwordx2 %2, %4, %5, 0 offen offset:2048 sc1\n\tbuffer_load_dwordx2 %3, %4, %5, 0 offen offset:2560 sc1"
               : "=&v"(xq[0]), "=&v"(xq[1]), "=&v"(xq[2]), "=&v"(xq[3]) : "v"(qo), "s"(rWS) : "memory");
  SB();
  asm volatile("s_waitcnt vmcnt(8)" : "+v"(w2raw[0]), "+v"(w2raw[1]), "+v"(w2raw[2]), "+v"(w2raw[3]), "+v"(w2raw[4]), "+v"(w2raw[5]), "+v"(w2raw[6]), "+v"(w2raw[7])::"memory");
  BAR();
  STAMP(4);
  v4u W2[56];
  auto w2l = [&](int i) { W2[i] = __builtin_amdgcn_raw_buffer_load_b128(WS, w2v + (i & 3) * 1024, (i >> 2) * 4096, SC1); };
  u32 hpk[4];
  GAS u32* pc2 = (GAS u32*)(A.ws + O_C2 + (l & 7) * 2048 + x * 256);
  auto put_h = [&](int h) {
    const auto s0 = __builtin_amdgcn_permlane16_swap(hpk[0], hpk[2], false, false);
    const auto s1 = __builtin_amdgcn_permlane16_swap(hpk[1], hpk[3], false, false);
    __builtin_amdgcn_raw_buffer_store_b128((v4u){s0[0], s1[0], s0[1], s1[1]}, WS, oh + h * (32 * 32768), 0, SC1);
  };
  const u32 oc2 = O_C2 + x * 2048 + (l & 7) * 256, c2want = tag * 64u + 32u;
  {
    u32 w1so, flw;
    auto w1l = [&](int i) {
      if ((i & 3) == 0) asm volatile("s_add_u32 m0, %1, %2\n\ts_add_u32 %0, %3, %4\n\tbuffer_load_dwordx4 %5, %6, %0 offen sc1 lds"
                                     : "=&s"(w1so) : "s"(lds0 + w * 28672), "i"(i * 1024), "s"(fch * 114688 + w * 28672), "i"(O_W1F + i * 1024), "v"(l16), "s"(rWS) : "memory", "scc");
      else asm volatile("buffer_load_dwordx4 %0, %1, %2 offen offset:%3 sc1 lds" ::"v"(l16), "s"(rWS), "s"(w1so), "i"((i & 3) * 1024) : "memory");
    };
    const float dsc = ch ? hi2f(dpair) : lo2f(dpair);
    asm volatile("" : "+v"(pc2));
    f32x4 cacc[8], mn[2], ot[2];
    v4u B[32], g1[8], w2f[2]; v2u kr[4];
    float xf[16], ln[16], kd[16], tm[16], hh[8];
    auto fix = [&](int h) {
      u32 df = 0;
#pragma unroll
      for (int i = 4 * h; i < 4 * h + 4; ++i) df |= (g1[i].y ^ vtag) | (g1[i].w ^ vtag);
      if (__builtin_amdgcn_ballot_w64(df != 0)) {
        bool ok;
        for (u32 sp = 0; sp < SPIN_CAP; ++sp) {
          asm volatile("" ::: "memory");
          ok = true;
#pragma unroll
          for (int i = 4 * h; i < 4 * h + 4; ++i) {
            g1[i] = __builtin_amdgcn_raw_buffer_load_b128(WS, O_ST1 + (tB + 256 * ((i >> 1) & 1) + 1024 * h) * 8 + (i & 1) * 16, 0, SC1);
            ok &= (g1[i].y == tag) & (g1[i].w == tag);
          }
          if (__all(ok)) break;
        }
#pragma unroll
        for (int j = 8 * h; j < 8 * h + 8; ++j) { const u32 pay = g1[j >> 1][(j & 1) * 2]; ln[j] = (xf[j] - lo2f(pay)) * __builtin_amdgcn_rcpf(hi2f(pay)); }
      }
    };
    auto give = [&](int h) {
      if (par2) { *(LAS f32x4*)(ep + 2048 * h) = cacc[4 * h]; *(LAS f32x4*)(ep + 2048 * h + 1024) = cacc[4 * h + 1]; mn[0] = cacc[4 * h + 2]; mn[1] = cacc[4 * h + 3]; asm volatile("" : "+v"(mn[0]), "+v"(mn[1])); }
      else { *(LAS f32x4*)(ep + 2048 * h) = cacc[4 * h + 2]; *(LAS f32x4*)(ep + 2048 * h + 1024) = cacc[4 * h + 3]; mn[0] = cacc[4 * h]; mn[1] = cacc[4 * h + 1]; asm volatile("" : "+v"(mn[0]), "+v"(mn[1])); }
    };
    auto pre = [&](auto J_, auto P_) {
      constexpr int j = decltype(J_)::value, ph = decltype(P_)::value;
      const u32 pay = g1[j >> 1][(j & 1) * 2];
      if constexpr (ph == 0) { xf[j] = (j & 1) ? hi2f(xq[j >> 2][(j & 3) >> 1]) : lo2f(xq[j >> 2][(j & 3) >> 1]); asm volatile("" : "+v"(xf[j])); }
      if constexpr (ph == 1) { tm[j] = __builtin_amdgcn_rcpf(hi2f(pay)); asm volatile("" : "+v"(tm[j])); }
      if constexpr (ph == 2) { ln[j] = xf[j] - lo2f(pay); asm volatile("" : "+v"(ln[j])); }
      if constexpr (ph == 3) { ln[j] *= tm[j]; asm volatile("" : "+v"(ln[j])); }
      if constexpr (ph == 4) { const u32 kk = kr[j >> 2][1 - ((j & 3) >> 1)]; kd[j] = (j & 1) ? lo2f(kk) : hi2f(kk); asm volatile("" : "+v"(kd[j])); }
    };
    sfor<0, 32>([&](auto U_) { constexpr int u = decltype(U_)::value; if constexpr (cv_slot(u) < 0) B[u] = *(LAS v4u*)(xp + 128 * (31 - u)); });
    SB();
    sfor<0, 144>([&](auto N_) {
      constexpr int n = decltype(N_)::value, u = cv_u(n), i = u < 16 ? u : u - 16, a = cv_a0(u) + n - cv_first(u), eps = 4 * a - i + 3;
      mfb<i == 0>(cacc[a], T[eps], B[i]);
      SB();
      constexpr int ub = cv_at(n);
      if constexpr (ub >= 0) B[ub] = *(LAS v4u*)(xp + 128 * (31 - ub));
      if constexpr (n < 112 && n % 4 == 1) w1l(n / 4);
      if constexpr (n == 2 || n == 3) {
        constexpr int pp = n - 2;
#pragma unroll
        for (int d = 0; d < 4; ++d) w2f[pp][d] = __builtin_amdgcn_perm(w2raw[2 * d + 1], w2raw[2 * d], pp ? 0x07060302u : 0x05040100u);
        __builtin_amdgcn_raw_buffer_store_b128(w2f[pp], WS, w2o + pp * 1024, 0, SC1);
      }
      if constexpr (n >= 11 && n < 27 && n % 4 == 3) { constexpr int aa = (n - 11) / 4; kr[aa] = *(LAS v2u*)(kp + 2560 - 512 * (aa & 1) - 2048 * (aa >> 1)); }
      if constexpr (n >= 23 && n < 39 && n % 2 == 1) pre(IC<(n - 23) / 2>{}, IC<4>{});
      if constexpr (n == 43) STAMP(25);
      if constexpr (n == 46) STAMP(26);
      if constexpr (n == 67) STAMP(27);
      if constexpr (n == 111) STAMP(28);
      if constexpr (n == 113) STAMP(22);
      if constexpr (n == 44) give(0);
      if constexpr (n == 45) {
        asm volatile("s_waitcnt vmcnt(11) lgkmcnt(0)" : "+v"(xq[0]), "+v"(xq[1]), "+v"(xq[2]), "+v"(xq[3])::"memory");
        __builtin_amdgcn_s_barrier();
        asm volatile("" ::: "memory");
        if (w == 0 && l < 8) __hip_atomic_fetch_add(pc2 + 8192, 1u, RLX);
      }
      if constexpr (n == 46 || n == 47) { g1[2 * (n - 46)] = *(LAS v4u*)(gp + (n - 46) * 2048); g1[2 * (n - 46) + 1] = *(LAS v4u*)(gp + (n - 46) * 2048 + 16); }
      if constexpr (n == 48) { ot[0] = *(LAS f32x4*)er; ot[1] = *(LAS f32x4*)(er + 1024); }
      if constexpr (n >= 50 && n < 58) { pre(IC<n - 50>{}, IC<0>{}); pre(IC<n - 50>{}, IC<1>{}); pre(IC<n - 50>{}, IC<2>{}); if constexpr (n > 50) pre(IC<n - 51>{}, IC<3>{}); }
      if constexpr (n == 58) { pre(IC<7>{}, IC<3>{}); fix(0); }
      if constexpr (n >= 59 && n < 63) {
#pragma unroll
        for (int j = 2 * (n - 59); j < 2 * (n - 59) + 2; ++j) { hh[j] = xf[j] + ln[j] * ((kd[j] * dsc + mn[j >> 2][j & 3]) + ot[j >> 2][j & 3]); asm volatile("" : "+v"(hh[j])); }
      }
      if constexpr (n == 63 || n == 64) { hpk[2 * (n - 63)] = pk(hh[4 * (n - 63)], hh[4 * (n - 63) + 1]); hpk[2 * (n - 63) + 1] = pk(hh[4 * (n - 63) + 2], hh[4 * (n - 63) + 3]); }
      if constexpr (n == 66) put_h(0);
      if constexpr (n == 98) asm volatile("buffer_load_dword %0, %1, %2, 0 offen sc1" : "=&v"(flw) : "v"(oc2 + 32768), "s"(rWS) : "memory");
      if constexpr (n == 112) {
        asm volatile("s_waitcnt vmcnt(3)" : "+v"(flw)::"memory");
        __builtin_amdgcn_s_barrier();
        if (w == 0 && l < 8) __hip_atomic_fetch_add(pc2, 1u, RLX);
        if (!__all(flw == c2want)) spin(oc2 + 32768, c2want);
      }
      if constexpr (n > 112 && n % 2 == 1 && (n - 113) / 2 < NWE) w2l((n - 113) / 2);
      if constexpr (n >= 90 && n < 98 && n % 2 == 0) { constexpr int j = 4 + (n - 90) / 2; g1[j] = *(LAS v4u*)(gp + 8192 + ((j >> 1) & 1) * 2048 + (j & 1) * 16); }
      if constexpr (n >= 104) pre(IC<8 + (n - 104) / 5>{}, IC<(n - 104) % 5>{});
      SB();
    });
    asm volatile("s_nop 7" ::: "memory");
    SB();
    STAMP(5);
    give(1);
    fix(1);
    WAITV(NWE < 16 ? NWE : 16);
    BAR();
#pragma unroll
    for (int i = 16; i < NWE; ++i) w2l(i);
#pragma unroll
    for (int aa = 0; aa < 2; ++aa) {
      const f32x4 o = *(LAS f32x4*)(er + 2048 + aa * 1024);
#pragma unroll
      for (int i = 0; i < 4; ++i) hh[4 * aa + i] = xf[8 + 4 * aa + i] + ln[8 + 4 * aa + i] * ((kd[8 + 4 * aa + i] * dsc + mn[aa][i]) + o[i]);
      hpk[2 * aa] = pk(hh[4 * aa], hh[4 * aa + 1]); hpk[2 * aa + 1] = pk(hh[4 * aa + 2], hh[4 * aa + 3]);
    }
    asm volatile("" : "+v"(hpk[0]), "+v"(hpk[1]), "+v"(hpk[2]), "+v"(hpk[3]));
    STAMP(6);
  }

  v4u ones = {0x3f803f80u, 0x3f803f80u, 0x3f803f80u, 0x3f803f80u};
  LAS char* wp0 = lds + l * 16;
  asm volatile("" : "+v"(ones), "+v"(wp0));
  u32 sg[4];
#pragma unroll
  for (int g = 0; g < 4; ++g) sg[g] = (u32)((g + rot) & 3);
  LAS char* wp = wp0 + sg[0] * 28672;
  v4u wf[WD + 1], hf[2][2], g2[2];
#pragma unroll
  for (int i = 0; i < WD; ++i) wf[i] = *(LAS v4u*)(wp + i * 1024);
  STAMP(7);
  if (w == 0) spin(oc2, c2want);
  BAR();
  STAMP(8);
  f32x4 cs[7];
  u32 fl3 = 0, fl2 = 0;
  v4u AC[28][2];
  const u32 ao[2] = {(u32)((w & 1) * (224 * 256)), (u32)(((w & 1) ^ 1) * (224 * 256))};
  auto ald = [&](int u) {
#pragma unroll
    for (int c = 0; c < 2; ++c) { AC[u][c] = __builtin_amdgcn_raw_buffer_load_b128(WS, av + (u % 14 & 3) * 1024, ao[c] + (u % 14 >> 2) * 4096 + (u / 14) * (64 * 224 * 256), SC1); SB(); }
  };
  {
    u32 hso;
    auto hdma = [&](int g, int n) {
      if (n == 0) asm volatile("s_add_u32 m0, %1, %2\n\ts_add_u32 %0, %3, %4\n\tbuffer_load_dwordx4 %5, %6, %0 offen sc1 lds"
                               : "=&s"(hso) : "s"(lds0 + ring), "i"((g % 5) * 2048), "s"((4 * hp + w) * 32768 + sg[(g >> 2) & 3] * 8192), "i"(O_HT + (g >> 4) * (32 * 32768) + (g & 3) * 2048), "v"(l16), "s"(rWS) : "memory", "scc");
      else asm volatile("buffer_load_dwordx4 %0, %1, %2 offen offset:1024 sc1 lds" ::"v"(l16), "s"(rWS), "s"(hso) : "memory");
    };
    auto hfrag = [&](int g, int pr, int t) { return trd(hx[pr], (g % 5) * 2048 + t * 256); };
    f32x4 acc[7][2], sS, sQ;
    auto epilogue = [&](auto P_) {
      constexpr int ps = decltype(P_)::value;
      if (!__all((g2[0].y == tag) & (g2[0].w == tag) & (g2[1].y == tag) & (g2[1].w == tag))) {
        for (u32 sp = 0; sp < SPIN_CAP; ++sp) {
          asm volatile("" ::: "memory");
#pragma unroll
          for (int k = 0; k < 2; ++k) g2[k] = __builtin_amdgcn_raw_buffer_load_b128(WS, og2, k * 32768 + ps * 16384, SC1);
          if (__all((g2[0].y == tag) & (g2[0].w == tag) & (g2[1].y == tag) & (g2[1].w == tag))) break;
        }
      }
      const u32 s1 = f2u(u2f(g2[0].x) + u2f(g2[1].x)), s2 = f2u(u2f(g2[0].z) + u2f(g2[1].z));
      const auto h1 = __builtin_amdgcn_permlane32_swap(s1, s1, false, false), h2 = __builtin_amdgcn_permlane32_swap(s2, s2, false, false);
      const float mean = (u2f(h1[0]) + u2f(h1[1])) * (1.f / D);
      const float v2 = __builtin_fmaxf((u2f(h2[0]) + u2f(h2[1])) * (1.f / D) - mean * mean, 0.f) + 1e-5f;
      const u32 e0 = f2u(-mean), e1 = f2u(-1.44269504089f * __builtin_amdgcn_rsqf(v2)), e2 = f2u(v2);
      const auto b0 = __builtin_amdgcn_permlane16_swap(e0, e0, false, false), b1 = __builtin_amdgcn_permlane16_swap(e1, e1, false, false),
                 b2 = __builtin_amdgcn_permlane16_swap(e2, e2, false, false);
      v4u sa[2], sb[2];
#pragma unroll
      for (int pr = 0; pr < 2; ++pr) {
        const bool up = (pr ^ spar) != 0;
        const float nm = u2f(up ? b0[1] : b0[0]), k1 = u2f(up ? b1[1] : b1[0]), vv = u2f(up ? b2[1] : b2[0]);
        u32 d[8];
        f32x2 a[7], bb[7], e[7];
#pragma unroll
        for (int t = 0; t < 7; ++t) a[t] = (f32x2){acc[t][pr][0], acc[t][pr][1]} + nm * (f32x2){cs[t][0], cs[t][1]};
        SB();
#pragma unroll
        for (int t = 0; t < 7; ++t) e[t] = a[t] * k1;
        SB();
#pragma unroll
        for (int t = 0; t < 7; ++t) {
          e[t] = (f32x2){__builtin_amdgcn_exp2f(e[t][0]), __builtin_amdgcn_exp2f(e[t][1])};
          bb[t] = (f32x2){acc[t][pr][2], acc[t][pr][3]} + nm * (f32x2){cs[t][2], cs[t][3]};
          if (ps == 0 && 7 * (1 - pr) + t < W2_EPI) w2l(w2_first(16) - W2_EPI + 7 * (1 - pr) + t);
          if (ps == 1 && t < 3) ald(8 + 3 * pr + t);
          SB();
        }
#pragma unroll
        for (int t = 0; t < 7; ++t) e[t] = e[t] * vv + vv;
        SB();
#pragma unroll
        for (int t = 0; t < 7; ++t) { e[t] = (f32x2){__builtin_amdgcn_rcpf(e[t][0]), __builtin_amdgcn_rcpf(e[t][1])}; a[t] *= bb[t]; SB(); }
#pragma unroll
        for (int t = 0; t < 7; ++t) a[t] *= e[t];
        SB();
#pragma unroll
        for (int t = 0; t < 7; ++t) d[t] = pk(a[t][0], a[t][1]);
        sa[pr] = (v4u){d[0], d[1], d[2], d[3]};
        const auto x0 = __builtin_amdgcn_permlane32_swap(d[4], d[6], false, false), x1 = __builtin_amdgcn_permlane32_swap(d[5], d[5], false, false);
        const auto y0 = __builtin_amdgcn_permlane16_swap(x0[0], x1[0], false, false), y1 = __builtin_amdgcn_permlane16_swap(x0[1], x1[1], false, false);
        sb[pr] = (v4u){y0[0], y0[1], y1[0], y1[1]};
        asm volatile("" : "+v"(sa[pr]), "+v"(sb[pr]));
        SB();
      }
#pragma unroll
      for (int pr = 0; pr < 2; ++pr) {
        const u32 ro = (u32)((pr ^ spar) * (224 * 256) + ps * (64 * 224 * 256));
        __builtin_amdgcn_raw_buffer_store_b128(sa[pr], WS, oact + ro, 0, SC1);
        __builtin_amdgcn_raw_buffer_store_b128(sb[pr], WS, oact2 + ro, 0, SC1);
      }
      SB();
    };
    hdma(0, 0); hdma(0, 1); hdma(1, 0); hdma(1, 1);
    {
      LAS char* c0 = wp0 + w * 1024;
      LAS char* c1 = wp0 + (w == 3 ? 3 : w + 4) * 1024;
      LAS char* c2 = c0 + 57344;
      LAS char* c3 = c1 + 57344;
      asm volatile("" : "+v"(c0), "+v"(c1), "+v"(c2), "+v"(c3));
      auto crd = [&](int k, int hi) { return *(LAS v4u*)((k < 8 ? (hi ? c1 : c0) : (hi ? c3 : c2)) + (k & 7) * 7168); };
      v4u fa[16], fb[16]; f32x4 ca, cb;
#pragma unroll
      for (int k = 0; k < 4; ++k) { fa[k] = crd(k, 0); fb[k] = crd(k, 1); }
      SB();
      sfor<0, 16>([&](auto K_) {
        constexpr int k = decltype(K_)::value;
        mfv<k == 0>(ca, fa[k], ones); SB();
        if constexpr (k + 4 < 16) fa[k + 4] = crd(k + 4, 0);
        SB();
        mfv<k == 0>(cb, fb[k], ones); SB();
        if constexpr (k + 4 < 16) fb[k + 4] = crd(k + 4, 1);
        if constexpr (k >= 2 && k < 8) hdma(2 + (k - 2) / 2, k & 1);
        if constexpr (k == 12) put_h(1);
        SB();
      });
      WAITV(9);
#pragma unroll
      for (int pr = 0; pr < 2; ++pr) { const v2u a = hfrag(0, pr, 0), c = hfrag(0, pr, 1); hf[0][pr] = (v4u){a.x, a.y, c.x, c.y}; }
      asm volatile("s_nop 7" ::: "memory");
      *(LAS f32x4*)pcs = ca; *(LAS f32x4*)(pcs + 64) = cb;
    }
    SB();
    STAMP(20);
    sfor<0, 32>([&](auto G_) {
      constexpr int g = decltype(G_)::value, s = g & 15;
      v4u(&hc)[2] = hf[g & 1]; v4u(&hn)[2] = hf[(g + 1) & 1];
      LAS char* wn = wp;
      if constexpr ((g & 3) == 3 && g < 31) wn = wp0 + sg[((g + 1) >> 2) & 3] * 28672;
      v2u ha;
      sfor<0, 7>([&](auto T_) {
        constexpr int t = decltype(T_)::value, it = g * 7 + t, nx = it + WD, ng = nx / 7, nt = nx % 7;
        if constexpr (t == 0) asm volatile("" : "+v"(wf[it % 8]), "+v"(wf[(it + 1) % 8]), "+v"(wf[(it + 2) % 8]), "+v"(wf[(it + 3) % 8]), "+v"(hc[0]), "+v"(hc[1]));
        if constexpr (t == 4) asm volatile("" : "+v"(wf[it % 8]), "+v"(wf[(it + 1) % 8]), "+v"(wf[(it + 2) % 8]));
        mfv<s == 0>(acc[t][0], wf[it % (WD + 1)], hc[0]);
        SB();
        if constexpr (nx < 224) wf[nx % (WD + 1)] = *(LAS v4u*)((ng > g ? wn : wp) + (ng & 3) * 7168 + nt * 1024);
        SB();
        mfv<s == 0>(acc[t][1], wf[it % (WD + 1)], hc[1]);
        SB();
        if constexpr (s < 15 && t < 4) {
          if constexpr (t == 0) { constexpr int hw = h_wait(g); WAITV(hw); }
          if constexpr (t & 1) { const v2u hb = hfrag(g + 1, t >> 1, 1); hn[t >> 1] = (v4u){ha.x, ha.y, hb.x, hb.y}; } else ha = hfrag(g + 1, t >> 1, 0);
        }
        if constexpr (t >= 4 && t - 4 < w2_n(g)) w2l(w2_first(g) + t - 4);
        if constexpr (s < 11 && t >= 5) hdma(g + 5, t - 5);
        if constexpr (g == 28 && t == 4) {
          constexpr int nw = vm_sum(26, 28);
          asm volatile("s_waitcnt vmcnt(%1)" : "+v"(fl3) : "n"(nw) : "memory");
          if (!__all(fl3 == tag)) spin(of3, tag);
        }
        if constexpr (g >= 28 && (t == 4 || t == 6)) ald(2 * (g - 28) + (t - 4) / 2);
        if constexpr (g == 5 || g == 6) { if constexpr (t >= 3 && 4 * (g - 5) + t - 3 < 7) { constexpr int k = 4 * (g - 5) + t - 3; cs[k] = *(LAS f32x4*)(pcr + (k & 3) * 12288 + (k >> 2) * 64); } }
        SB();
      });
      if constexpr (s < 4) { mfv<s == 0>(sS, ones, hc[0]); mfv<s == 0>(sQ, hc[0], hc[0]); }
      if constexpr (g == 4) {
        constexpr int nw = vm_sum(0, 4) + w2_n(4) + 2;
        WAITV(nw);
        __builtin_amdgcn_s_barrier();
        asm volatile("" ::: "memory");
        if (w == 0) { if (l < 8) __hip_atomic_fetch_add(pc2 + 4096, 1u, RLX); }
        else __builtin_amdgcn_raw_buffer_store_b32(vtag, WS, 0xffffff00u, 0, SC1);
      }
      if constexpr (g == 13) asm volatile("buffer_load_dword %0, %1, %2, 0 offen sc1" : "=&v"(fl2) : "v"(oc2 + 16384), "s"(rWS) : "memory");
      if constexpr (s == 5) {
        float q01, q23, qq;
        asm volatile("v_cndmask_b32 %0, %3, %4, %7\n\tv_cndmask_b32 %1, %5, %6, %7\n\tv_cndmask_b32 %2, %0, %1, %8"
                     : "=&v"(q01), "=&v"(q23), "=&v"(qq) : "v"(sQ[0]), "v"(sQ[1]), "v"(sQ[2]), "v"(sQ[3]), "s"(0xaaaaaaaaaaaaaaaaull), "s"(0xccccccccccccccccull));
        __builtin_amdgcn_raw_buffer_store_b128((v4u){f2u(sS[0]), vtag, f2u(qq), vtag}, WS, ost + (g >> 4) * 16384, 0, SC1);
      }
      if constexpr (s == 12) {
        asm volatile("buffer_load_dwordx4 %0, %2, %3, %4 offen sc1\n\tbuffer_load_dwordx4 %1, %2, %3, %5 offen sc1" : "=&v"(g2[0]), "=&v"(g2[1])
                     : "v"(og2), "s"(rWS), "s"((g >> 4) * 16384), "s"(32768 + (g >> 4) * 16384) : "memory");
      }
      if constexpr (g == 19) {
        constexpr int nw = vm_sum(16, 19) + w2_n(19) + 2;
        WAITV(nw);
        __builtin_amdgcn_raw_buffer_store_b32(vtag, WS, of3w, 0, SC1);
      }
      if constexpr (g == 27) {
        dma<true>(lds0 + ring, 4096, l16, rWS, (32 * (w >> 1) + 4 * x + rbl) * 32768 + (2 * ct + (w & 1)) * 2048, O_HT);
        dma<true>(lds0 + ring, 5120, l16, rWS, (32 * (w >> 1) + 4 * x + rbl) * 32768 + (2 * ct + (w & 1)) * 2048, O_HT + 1024);
      }
      if constexpr (g == 25) asm volatile("buffer_load_dword %0, %1, %2, 0 offen sc1" : "=&v"(fl3) : "v"(of3), "s"(rWS) : "memory");
      SB();
      wp = wn;
      if constexpr ((g & 7) == 7) STAMP(16 + (g >> 3));
      if constexpr (s == 15) {
        constexpr int nw = g == 15 ? vm_sum(14, 16) : vm_sum(29, 32);
        asm volatile("s_waitcnt vmcnt(%3)\n\ts_nop 7" : "+v"(g2[0]), "+v"(g2[1]), "+v"(fl2) : "n"(nw) : "memory");
        SB();
        if constexpr (g == 15) {
          if (!__all(fl2 == c2want)) spin(oc2 + 16384, c2want);
#pragma unroll
          for (int k = 16; k < 21; ++k) { hdma(k, 0); hdma(k, 1); }
        }
        epilogue(IC<(g >> 4)>{});
        if constexpr (g == 15) {
          WAITV(8 + vm_pre(1));
#pragma unroll
          for (int pr = 0; pr < 2; ++pr) { const v2u a = hfrag(16, pr, 0), c = hfrag(16, pr, 1); hf[0][pr] = (v4u){a.x, a.y, c.x, c.y}; }
          SB();
        }
        if constexpr (g == 15) STAMP(9);
      }
    });
  }
  STAMP(10);

  {
    __builtin_amdgcn_s_barrier();
    asm volatile("" ::: "memory");
    v2u hr[4];
#pragma unroll
    for (int i = 0; i < 4; ++i)
      hr[i] = trd(lds, L_R + (u32)(((w & 2) + (i >> 1)) * 12288 + 4096 + (8 * q + 4 * (i & 1) + rr) * 64 + (((2 * (w & 1) + (p4 >> 1)) ^ (2 * (q & 1))) << 4) + (p4 & 1) * 8));
    LAS char* pw = lds + w * 4096 + l * 16;
    u32 f3b;
    f32x4 o[4][2], mine[4];
    SB();
    sfor<0, 28>([&](auto U_) {
      constexpr int u = decltype(U_)::value;
#pragma unroll
      for (int c = 0; c < 2; ++c)
#pragma unroll
        for (int pp = 0; pp < 4; ++pp) mfa<u % 14 == 0>(o[pp][c], W2[4 * (u % 14) + pp], AC[u][c]);
      SB();
      if constexpr (u == 5) {
        WAITV(0);
        __builtin_amdgcn_raw_buffer_store_b32(vtag, WS, of3w + 16384, 0, SC1);
        SB();
      }
      if constexpr (u == UPOLL) { f3b = __builtin_amdgcn_raw_buffer_load_b32(WS, of3 + 16384, 0, SC1); SB(); }
      if constexpr (u == UCHK) {
        if (!__all(f3b == tag)) spin(of3 + 16384, tag);
        STAMP(12);
#pragma unroll
        for (int v = 14; v < 20; ++v) ald(v);
      }
      if constexpr (u > UCHK && u <= UCHK + 8) ald(u - UCHK + 19);
      if constexpr (u % 14 == 13) {
        asm volatile("s_nop 7" ::: "memory");
        SB();
        constexpr int hfb = 2 * (u / 14);
#pragma unroll
        for (int pp = 0; pp < 4; ++pp) *(LAS f32x4*)(pw + ((hfb + ((w & 1) ^ 1)) * 16384) + pp * 1024) = o[pp][1];
        if ((w & 2) == hfb) {
#pragma unroll
          for (int pp = 0; pp < 4; ++pp) { mine[pp] = o[pp][0]; asm volatile("" : "+v"(mine[pp])); }
        } else {
#pragma unroll
          for (int pp = 0; pp < 4; ++pp) *(LAS f32x4*)(pw + ((hfb + (w & 1)) * 16384) + pp * 1024) = o[pp][0];
        }
        if constexpr (u == 13) STAMP(11);
        SB();
      }
    });
    STAMP(13);
    BAR();
#pragma unroll
    for (int j = 1; j < 4; ++j)
#pragma unroll
      for (int pp = 0; pp < 4; ++pp) mine[pp] += *(LAS f32x4*)(lds + w * 16384 + ((w + j) & 3) * 4096 + pp * 1024 + l * 16);
    u32 r[8];
#pragma unroll
    for (int i = 0; i < 4; ++i) {
      r[2 * i] = pk(lo2f(hr[i].x) + mine[0][i], hi2f(hr[i].x) + mine[1][i]);
      r[2 * i + 1] = pk(lo2f(hr[i].y) + mine[2][i], hi2f(hr[i].y) + mine[3][i]);
    }
    u16* op = A.out + (1024 * (w >> 1) + 128 * x + 32 * rbl + 16 * (w & 1) + n16) * D + 64 * ct + 8 * q;
    *(v4u*)op = (v4u){r[0], r[1], r[2], r[3]};
    *(v4u*)(op + 32) = (v4u){r[4], r[5], r[6], r[7]};
    if (b == 0 && w == 0 && l == 0) __builtin_amdgcn_raw_buffer_store_b32(tag, WS, O_EP, 0, SC1);
  }
}
}

extern "C" void kernel_launch(void* const* d_in, const int* in_sizes, int n_in, void* d_out, int out_size, void* d_ws, size_t ws_size, hipStream_t stream) {
  fk::Args a;
  a.q = (const fk::u16*)d_in[0]; a.k = (const fk::u16*)d_in[1]; a.v = (const fk::u16*)d_in[2]; a.shk = (const fk::u16*)d_in[3];
  a.ssk = (const fk::u16*)d_in[4]; a.ssd = (const fk::u16*)d_in[5]; a.wg = (const fk::u16*)d_in[7]; a.wu = (const fk::u16*)d_in[8];
  a.wd = (const fk::u16*)d_in[9]; a.out = (fk::u16*)d_out; a.ws = (char*)d_ws;
  fk::fused<<<256, 256, 0, stream>>>(a);
}
```

```cpp
#include <hip/hip_runtime.h>
#include <stdint.h>
#define NWE 20
#define UPOLL 8
#define UCHK 11

namespace fk {
constexpr int WD = 7;
typedef unsigned u32;
typedef unsigned short u16;
typedef unsigned long long u64;
typedef u32 v4u __attribute__((ext_vector_type(4)));
typedef u32 v2u __attribute__((ext_vector_type(2)));
typedef float f32x4 __attribute__((ext_vector_type(4)));
typedef float f32x2 __attribute__((ext_vector_type(2)));
typedef short s16x4 __attribute__((ext_vector_type(4)));
typedef short s16x8 __attribute__((ext_vector_type(8)));
typedef __bf16 bf2 __attribute__((ext_vector_type(2)));
#define LAS __attribute__((address_space(3)))
#define GAS __attribute__((address_space(1)))
#define RLX __ATOMIC_RELAXED, __HIP_MEMORY_SCOPE_AGENT
#define SB() __builtin_amdgcn_sched_barrier(0)
#define WAITV(n) asm volatile("s_waitcnt vmcnt(%0)" ::"n"(n) : "memory")
#define BAR() do { asm volatile("s_waitcnt lgkmcnt(0)" ::: "memory"); __builtin_amdgcn_s_barrier(); asm volatile("" ::: "memory"); } while (0)

constexpr int S = 2048, D = 512, F = 1792;
constexpr u32 O_EP = 0, O_C2 = 16384, O_ST1 = 65536, O_F1 = 98304, O_ST2 = 131072, O_F3 = 262144;
constexpr u32 O_KVT = 1u << 20, O_QT = 3u << 20, O_HT = 5u << 20, O_W2T = 7u << 20, O_ACT = 9u << 20, O_W1F = 17u << 20, WS_BYTES = 21u << 20;
constexpr u32 O_DIAG = 24u << 20;
constexpr u32 L_R = 114688, L_K = L_R, L_V = L_R + 9216, L_Q = L_R + 17408, L_WR = 0, WR_S = 1056, L_TZ = 36864, TZ_S = 4160, TZ_C = 4 * TZ_S, L_XR = L_R + 33280, XR_S = 4608,
              L_S1 = L_R, L_EX = L_R + 16384, LDS_BYTES = 163840;
constexpr u32 SPIN_CAP = 1u << 18;
constexpr int SC1 = 16;

struct Args { const u16 *q, *k, *v, *shk, *ssk, *ssd, *wg, *wu, *wd; u16* out; char* ws; };

__device__ __forceinline__ u32 f2u(float x) { return __builtin_bit_cast(u32, x); }
__device__ __forceinline__ float u2f(u32 x) { return __builtin_bit_cast(float, x); }
__device__ __forceinline__ float lo2f(u32 x) { return u2f(x << 16); }
__device__ __forceinline__ float hi2f(u32 x) { return u2f(x & 0xffff0000u); }
__device__ __forceinline__ u32 pk(float lo, float hi) { bf2 r = {(__bf16)lo, (__bf16)hi}; return __builtin_bit_cast(u32, r); }
__device__ __forceinline__ float rbf(float x) { return lo2f(pk(x, x)); }
__device__ __forceinline__ f32x4 mfma(v4u a, v4u b, f32x4 c) {
  return __builtin_amdgcn_mfma_f32_16x16x32_bf16(__builtin_bit_cast(s16x8, a), __builtin_bit_cast(s16x8, b), c, 0, 0, 0);
}
template <bool FIRST> __device__ __forceinline__ void mfv(f32x4& c, v4u a, v4u b) {
  if (FIRST) asm volatile("v_mfma_f32_16x16x32_bf16 %0, %1, %2, 0" : "=&v"(c) : "v"(a), "v"(b));
  else asm volatile("v_mfma_f32_16x16x32_bf16 %0, %1, %2, %0" : "+v"(c) : "v"(a), "v"(b));
}
template <bool FIRST> __device__ __forceinline__ void mfa(f32x4& c, v4u a, v4u b) {
  if (FIRST) asm volatile("v_mfma_f32_16x16x32_bf16 %0, %1, %2, 0" : "=&v"(c) : "a"(a), "v"(b));
  else asm volatile("v_mfma_f32_16x16x32_bf16 %0, %1, %2, %0" : "+v"(c) : "a"(a), "v"(b));
}
template <bool FIRST> __device__ __forceinline__ void mfb(f32x4& c, v4u a, v4u b) {
  if (FIRST) asm volatile("v_mfma_f32_16x16x32_bf16 %0, %1, %2, 0" : "=&v"(c) : "v"(a), "a"(b));
  else asm volatile("v_mfma_f32_16x16x32_bf16 %0, %1, %2, %0" : "+v"(c) : "v"(a), "a"(b));
}
__device__ __forceinline__ v2u trd(LAS char* lds, u32 off) {
  return __builtin_bit_cast(v2u, __builtin_amdgcn_ds_read_tr16_b64_v4i16((LAS s16x4*)(lds + off)));
}
__device__ __forceinline__ v4u mkrs(const void* p, u32 bytes) { u64 a = (u64)p; return (v4u){(u32)a, (u32)(a >> 32) & 0xffffu, bytes, 0x00020000u}; }
template <bool COH> __device__ __forceinline__ void dma(u32 lb, u32 lo, u32 voff, v4u rs, u32 sb, u32 so) {
  u32 t;
  if (COH) asm volatile("s_add_u32 m0, %1, %6\n\ts_add_u32 %0, %4, %5\n\tbuffer_load_dwordx4 %2, %3, %0 offen sc1 lds" : "=&s"(t) : "s"(lb), "v"(voff), "s"(rs), "s"(sb), "i"(so), "i"(lo) : "memory", "scc");
  else asm volatile("s_add_u32 m0, %1, %6\n\ts_add_u32 %0, %4, %5\n\tbuffer_load_dwordx4 %2, %3, %0 offen lds" : "=&s"(t) : "s"(lb), "v"(voff), "s"(rs), "s"(sb), "i"(so), "i"(lo) : "memory", "scc");
}
template <int CTRL, int RM> __device__ __forceinline__ float dppadd(float v) {
  return v + __builtin_bit_cast(float, __builtin_amdgcn_update_dpp(0, __builtin_bit_cast(int, v), CTRL, RM, 0xf, true));
}
__device__ __forceinline__ float hsum(float v) {
  v = dppadd<0x111, 0xf>(v); v = dppadd<0x112, 0xf>(v); v = dppadd<0x114, 0xf>(v); v = dppadd<0x118, 0xf>(v);
  return dppadd<0x142, 0xa>(v);
}
#define STAMP(i) do { if ((i) == 0) { u64 t0_; asm volatile("s_memrealtime %0\n\ts_waitcnt lgkmcnt(0)" : "=s"(t0_)::"memory"); pt0 = t0_; } if ((i) == PK) { u64 t_; asm volatile("s_memrealtime %0\n\ts_waitcnt lgkmcnt(0)" : "=s"(t_)::"memory"); ptk = t_; } } while (0)
constexpr int PK = 2;
template <int V> struct IC { static constexpr int value = V; };
template <int B, int E, class Fn> __device__ __forceinline__ void sfor(Fn&& f) { if constexpr (B < E) { f(IC<B>{}); sfor<B + 1, E>(f); } }
constexpr int cv_a0(int u) { return u < 16 ? u >> 2 : ((u - 16) >> 2) > 4 ? (u - 16) >> 2 : 4; }
constexpr int cv_first(int u) { int n = 0; for (int j = 0; j < u; ++j) n += (j < 16 ? 4 : 8) - cv_a0(j); return n; }
constexpr int cv_u(int n) { int u = 0; while (cv_first(u + 1) <= n) ++u; return u; }
constexpr int cv_slot(int i) { int s_ = cv_first(i < 16 ? i : i + 16) - 12; if (i < 31) { const int v = cv_slot(i + 1) - 2; if (v < s_) s_ = v; } return s_ & ~1; }
constexpr int cv_at(int n) { for (int i = 0; i < 32; ++i) if (cv_slot(i) == n) return i; return -1; }
constexpr int w2_n(int g) { return g >= 27 ? 0 : (g & 15) < 11 ? ((g & 1) ^ 1) : 3; }
constexpr int W2_EPI = 56 - 27 - NWE;
constexpr int w2_first(int g) { int n = NWE + (g > 15 ? W2_EPI : 0); for (int j = 0; j < g; ++j) n += w2_n(j); return n; }
constexpr int a_n(int g) { return g >= 28 ? 4 : 0; }
constexpr int vm_tail(int g) { return (g == 4) + ((g & 15) == 5) + 2 * ((g & 15) == 12) + (g == 13) + (g == 19) + (g == 25) + 2 * (g == 27); }
constexpr int vm_ops(int g) { return w2_n(g) + a_n(g) + ((g & 15) < 11 ? 2 : 0) + vm_tail(g); }
constexpr int vm_sum(int a, int e) { int n = 0; for (int j = a; j < e; ++j) n += vm_ops(j); return n; }
constexpr int vm_pre(int ps) { return ps ? W2_EPI + 4 : 1; }
constexpr int h_wait(int g) {
  return (g & 15) < 4 ? 2 * (3 - (g & 15)) + vm_pre(g >> 4) + vm_sum(g & 16, g) : vm_tail(g - 4) + vm_sum(g - 3, g);
}
static_assert(w2_first(32) == 56 && W2_EPI >= 0 && W2_EPI <= 14, "all w_down fragments are requested");

__global__ void __launch_bounds__(256, 1) fused(Args A) {
  __shared__ __attribute__((aligned(16))) char smem_[LDS_BYTES];
  LAS char* lds = (LAS char*)smem_;
  const u32 lds0 = (u32)(uintptr_t)lds;
  u64 pt0, ptk;
  STAMP(0);
  ptk = pt0;
  const int b = blockIdx.x, x = b & 7, y = b >> 3;
  const int l = threadIdx.x & 63, w = __builtin_amdgcn_readfirstlane(threadIdx.x >> 6);
  const int n16 = l & 15, q = l >> 4, rr = (l >> 2) & 3, p4 = l & 3;
  const __amdgpu_buffer_rsrc_t WS = __builtin_amdgcn_make_buffer_rsrc(A.ws, 0, WS_BYTES, 0x00020000);
  const v4u rWS = mkrs(A.ws, WS_BYTES);
  auto spin = [&](u32 off, u32 want) {
    for (u32 sp = 0; sp < SPIN_CAP; ++sp) {
      asm volatile("" ::: "memory");
      if (__all(__builtin_amdgcn_raw_buffer_load_b32(WS, off, 0, SC1) == want)) break;
    }
  };

  const int ti = 4 * x + (y >> 3), cj = y & 7;
  const int cA = 64 * cj + 16 * w + n16;
  const int srow = 256 * x + 8 * y + 2 * w;
  const int ch = w >> 1, par2 = w & 1, cB = 64 * x + 2 * y + ch;
  v2u shk; v4u sx0, sx1, kz[2];
  v2u kedge; u32 vtag, vdp;
  u32 l16 = l * 16;
  {
    const v4u rK = mkrs(A.k, S * D * 2), rV = mkrs(A.v, S * D * 2), rQ = mkrs(A.q, S * D * 2);
    const u32 c0 = ((l & 7) ^ ((l >> 3) & 2)) * 16, tv = (u32)((64 * ti + (l >> 3)) * 1024 + 128 * cj);
    const u32 va = tv + c0, vb = tv + (c0 ^ 64);
    const u32 vk = ((w & 1) ? vb : va) + (u32)(w * 8192 - 8192);
    dma<false>(lds0 + w * 1024, L_K, vk, rK, 0, 0);
    dma<false>(lds0 + w * 1024, L_K + 4096, vk + 32768, rK, 0, 0);
    if (w == 0) dma<false>(lds0, L_K + 8192, vk + 65536, rK, 0, 0);
    dma<false>(lds0 + w * 2048, L_V, va, rV, w * 16384, 0);
    dma<false>(lds0 + w * 2048, L_V + 1024, vb, rV, w * 16384, 8192);
    asm volatile("global_load_dwordx2 %0, %1, %2" : "=&v"(shk) : "v"(cA * 8), "s"(A.shk) : "memory");
    asm volatile("global_load_dwordx4 %0, %2, %3\n\tglobal_load_dwordx4 %1, %2, %3 offset:1024" : "=&v"(kz[0]), "=&v"(kz[1]) : "v"(l16), "s"(A.ssk + cB * S + 1024 * par2) : "memory");
    asm volatile("global_load_dwordx2 %0, %3, %4 offset:2048\n\tglobal_load_dword %1, %3, %5\n\tglobal_load_dword %2, %3, %6"
                 : "=&v"(kedge), "=&v"(vtag), "=&v"(vdp) : "v"(0), "s"(A.ssk + cB * S), "s"(A.ws + O_EP), "s"(A.ssd + 64 * x + 2 * y) : "memory");
    dma<false>(lds0 + w * 2048, L_Q, va, rQ, w * 16384, 0);
    dma<false>(lds0 + w * 2048, L_Q + 1024, vb, rQ, w * 16384, 8192);
    asm volatile("global_load_dwordx4 %0, %2, %3\n\tglobal_load_dwordx4 %1, %2, %3 offset:512" : "=&v"(sx0), "=&v"(sx1) : "v"((l >> 5) * 1024 + (l & 31) * 16), "s"(A.q + srow * D) : "memory");
  }
  const int rb = 8 * x + 2 * (y >> 3) + ((y >> 2) & 1), fg = y & 3;
  const v4u rW = mkrs(ch ? A.wu : A.wg, D * F * 2);
  u32 wrv = l < 56 ? l16 : 0xffffff00u, w1d[4];
  LAS char* pW = lds + L_WR + ((p4 >> 1) * 8 + rr) * WR_S + (4 * w + q) * 16 + (p4 & 1) * 8;
#pragma unroll
  for (int j = 0; j < 4; ++j) {
    int Z = 16 * j + 4 * w + q;
    asm volatile("" : "+v"(Z));
    const int c = (Z * 37) >> 8;
    w1d[j] = (j < 3 || Z < 56) ? O_W1F + (u32)(((8 * fg + c) * 16 + (rb >> 2)) * 7168 + (Z - 7 * c) * 1024 + (rb & 3) * 256 + (4 * ((n16 & 7) >> 1) + 2 * (n16 >> 3) + (n16 & 1)) * 16) : 0xffffff00u;
  }
  const u32 aAo = (u32)((8 * q + rr) * 128 + (((2 * w + (p4 >> 1)) ^ (rr & 2) ^ ((q & 1) << 2)) << 4) + (p4 & 1) * 8);
  LAS char* pA = lds + L_K + aAo;
  LAS char* pB = lds + L_K + (aAo ^ 64);
  u32 okv = O_KVT + cA * 4096 + (2040 - 32 - 64 * ti - 8 * q) * 2, oqt = O_QT + cA * 4096 + (64 * ti + 8 * q) * 2;
  LAS char* tw = lds + L_TZ + ch * TZ_C + (128 * par2 + l) * 16 + 32;
  u32 og = (l & 31) == 31 ? O_ST1 + (srow + (l >> 5)) * 8 : 0xffffff00u;
  u32 oci = (x == 0 && y < 8 && w == 0 && l < 24) ? O_C2 + l * 2048 + y * 256 : 0xffffff00u;
  asm volatile("" : "+v"(w1d[0]), "+v"(w1d[1]), "+v"(w1d[2]), "+v"(w1d[3]), "+v"(pA), "+v"(pB), "+v"(okv), "+v"(oqt), "+v"(tw), "+v"(og), "+v"(wrv), "+v"(pW), "+v"(oci));
  const int wU = b < 224 ? 2 * b + ch : 0;
  u32 ow[4], w2o = b < 224 ? O_W2T + (u32)(((wU & 7) * 56 + (wU >> 3)) * 4096 + par2 * 2048) + l16 : 0xffff0000u;
  {
    const int UU = 4 * (wU >> 3) + q, f = UU / 7, u = UU - 7 * f;
    const int col = 64 * (wU & 7) + 8 * rr + 4 * (p4 & 1) + 32 * (p4 >> 1) + 2 * par2;
#pragma unroll
    for (int d = 0; d < 4; ++d) ow[d] = (u32)(((56 * f + (u < 4 ? 2 * u + 8 * d : 8 * u + 2 * d)) * D + col) * 2);
  }
  asm volatile("" : "+v"(w2o), "+v"(ow[0]), "+v"(ow[1]), "+v"(ow[2]), "+v"(ow[3]));
  if (w == 0) *(LAS v4u*)(lds + L_XR + (l >> 5) * XR_S + 4096 + (l & 31) * 16) = (v4u){0, 0, 0, 0};
  SB();
  STAMP(23);
  asm volatile("s_waitcnt vmcnt(9)" : "+v"(shk)::"memory");
  BAR();
  STAMP(1);
  u32 tag, dpair;
  {
    v2u ka[2], kb[2], kc[2], va[2], vb[2];
#pragma unroll
    for (int it = 0; it < 2; ++it) { ka[it] = trd(pA, it * 4096 + 512); kb[it] = trd(pB, it * 4096 + 1024); kc[it] = trd(pB, it * 4096 + 1536); }
#pragma unroll
    for (int it = 0; it < 2; ++it) { va[it] = trd(pA, L_V - L_K + it * 4096); vb[it] = trd(pA, L_V - L_K + it * 4096 + 512); }
    SB();
#pragma unroll
    for (int j = 0; j < 4; ++j) dma<false>(lds0 + (8 * ch + 4 * par2) * WR_S, L_WR + j * WR_S, wrv, rW, (8 * rb + 4 * par2) * (F * 2) + fg * 896, j * (F * 2));
    SB();
    auto LO = [](u32 a, u32 c) { return (f32x2){lo2f(a), lo2f(c)}; };
    auto HI = [](u32 a, u32 c) { return (f32x2){hi2f(a), hi2f(c)}; };
    const f32x2 w0 = LO(shk.x, shk.x), w1 = HI(shk.x, shk.x), w2 = LO(shk.y, shk.y), w3 = HI(shk.y, shk.y);
    const f32x2 e[12] = {w0, HI(ka[0].x, ka[1].x), LO(ka[0].y, ka[1].y), HI(ka[0].y, ka[1].y), LO(kb[0].x, kb[1].x), HI(kb[0].x, kb[1].x), LO(kb[0].y, kb[1].y),
                         HI(kb[0].y, kb[1].y), LO(kc[0].x, kc[1].x), HI(kc[0].x, kc[1].x), LO(kc[0].y, kc[1].y), HI(kc[0].y, kc[1].y)};
    const f32x2 vv[8] = {LO(va[0].x, va[1].x), HI(va[0].x, va[1].x), LO(va[0].y, va[1].y), HI(va[0].y, va[1].y), LO(vb[0].x, vb[1].x), HI(vb[0].x, vb[1].x), LO(vb[0].y, vb[1].y), HI(vb[0].y, vb[1].y)};
    f32x2 kv[8];
#pragma unroll
    for (int s = 0; s < 8; ++s) kv[s] = (w3 * e[s + 1] + w2 * e[s + 2] + w1 * e[s + 3] + w0 * e[s + 4]) * vv[s];
#pragma unroll
    for (int it = 0; it < 2; ++it)
      __builtin_amdgcn_raw_buffer_store_b128((v4u){pk(kv[7][it], kv[6][it]), pk(kv[5][it], kv[4][it]), pk(kv[3][it], kv[2][it]), pk(kv[1][it], kv[0][it])}, WS, okv + (1 - it) * 64, 0, SC1);
    SB();
    asm volatile("s_waitcnt vmcnt(8)" : "+v"(kz[0]), "+v"(kz[1]), "+v"(kedge), "+v"(vtag), "+v"(vdp)::"memory");
    BAR();
    vtag += 1u;
    tag = __builtin_amdgcn_readfirstlane(vtag); dpair = __builtin_amdgcn_readfirstlane(vdp);
    v2u qa[2], qb[2];
#pragma unroll
    for (int it = 0; it < 2; ++it) { qa[it] = trd(pA, L_Q - L_K + it * 4096); qb[it] = trd(pA, L_Q - L_K + it * 4096 + 512); }
#pragma unroll
    for (int it = 0; it < 2; ++it) __builtin_amdgcn_raw_buffer_store_b128((v4u){qa[it].x, qa[it].y, qb[it].x, qb[it].y}, WS, oqt + it * 64, 0, SC1);
    __builtin_amdgcn_raw_buffer_store_b32(tag * 64u, WS, oci, 0, SC1);
  }
  SB();
  STAMP(24);
  {
    auto put = [&](LAS char* p, v4u d, u32 d4, u32 d5) {
      const u32 e0 = __builtin_amdgcn_alignbit(d.y, d.x, 16), e1 = __builtin_amdgcn_alignbit(d.z, d.y, 16), e2 = __builtin_amdgcn_alignbit(d.w, d.z, 16),
                e3 = __builtin_amdgcn_alignbit(d4, d.w, 16), e4 = __builtin_amdgcn_alignbit(d5, d4, 16);
      *(LAS v4u*)p = d; *(LAS v4u*)(p + TZ_S) = (v4u){e0, e1, e2, e3}; *(LAS v4u*)(p + 2 * TZ_S) = (v4u){d.y, d.z, d.w, d4}; *(LAS v4u*)(p + 3 * TZ_S) = (v4u){e1, e2, e3, e4};
    };
    auto nxt = [](u32 last, u32 v) { return (u32)__builtin_amdgcn_update_dpp((int)last, (int)v, 0x130, 0xf, 0xf, false); };
    put(tw, kz[0], nxt(__builtin_amdgcn_readlane(kz[1].x, 0), kz[0].x), nxt(__builtin_amdgcn_readlane(kz[1].y, 0), kz[0].y));
    put(tw + 1024, kz[1], nxt(par2 ? 0u : kedge.x, kz[1].x), nxt(par2 ? 0u : kedge.y, kz[1].y));
    const u32 f0 = __builtin_amdgcn_readlane(kz[0].x, 0), f1 = __builtin_amdgcn_readlane(kz[0].y, 0);
    if (l < 2 && !par2) put(tw - 32, (v4u){0, 0, 0, 0}, l ? f0 : 0u, l ? f1 : 0u);
  }
  SB();
  asm volatile("s_waitcnt vmcnt(5)" : "+v"(sx0), "+v"(sx1)::"memory");
  BAR();
  {
    v2u ta[4], tc[4];
#pragma unroll
    for (int j = 0; j < 4; ++j) { ta[j] = trd(pW, j * 256); tc[j] = trd(pW, j * 256 + 4 * WR_S); }
#pragma unroll
    for (int j = 0; j < 4; ++j) __builtin_amdgcn_raw_buffer_store_b128((v4u){ta[j].x, ta[j].y, tc[j].x, tc[j].y}, WS, w1d[j], 0, SC1);
  }
  SB();
  {
    const f32x2 v[8] = {{lo2f(sx0.x), hi2f(sx0.x)}, {lo2f(sx0.y), hi2f(sx0.y)}, {lo2f(sx0.z), hi2f(sx0.z)}, {lo2f(sx0.w), hi2f(sx0.w)},
                        {lo2f(sx1.x), hi2f(sx1.x)}, {lo2f(sx1.y), hi2f(sx1.y)}, {lo2f(sx1.z), hi2f(sx1.z)}, {lo2f(sx1.w), hi2f(sx1.w)}};
    const f32x2 s2 = ((v[0] + v[1]) + (v[2] + v[3])) + ((v[4] + v[5]) + (v[6] + v[7]));
    const float sm = hsum(s2[0] + s2[1]);
    const float m0 = u2f(__builtin_amdgcn_readlane(f2u(sm), 31)), m1 = u2f(__builtin_amdgcn_readlane(f2u(sm), 63));
    const float mean = (l < 32 ? m0 : m1) * (1.f / D);
    f32x2 ss = {0.f, 0.f};
#pragma unroll
    for (int j = 0; j < 8; ++j) { const f32x2 d = v[j] - mean; ss += d * d; }
    const float var = hsum(ss[0] + ss[1]) * (1.f / D);
    const u32 pay = pk(mean, __builtin_amdgcn_sqrtf(rbf(rbf(var) + 1e-5f)));
    __builtin_amdgcn_raw_buffer_store_b64((v2u){pay, vtag}, WS, og, 0, SC1);
  }
  SB();
  WAITV(1);
  BAR();
  STAMP(2);
  if (w == 0 && l == 0) __builtin_amdgcn_raw_buffer_store_b32(tag, WS, O_F1 + (cj * 32 + ti) * 128, 0, SC1);
  u32 w2raw[8];
  asm volatile("global_load_dword %0, %8, %12\n\tglobal_load_dword %1, %8, %12 offset:1024\n\tglobal_load_dword %2, %9, %12\n\tglobal_load_dword %3, %9, %12 offset:1024\n\t"
               "global_load_dword %4, %10, %12\n\tglobal_load_dword %5, %10, %12 offset:1024\n\tglobal_load_dword %6, %11, %12\n\tglobal_load_dword %7, %11, %12 offset:1024"
               : "=&v"(w2raw[0]), "=&v"(w2raw[1]), "=&v"(w2raw[2]), "=&v"(w2raw[3]), "=&v"(w2raw[4]), "=&v"(w2raw[5]), "=&v"(w2raw[6]), "=&v"(w2raw[7])
               : "v"(ow[0]), "v"(ow[1]), "v"(ow[2]), "v"(ow[3]), "s"(A.wd) : "memory");
  v4u T[32];
  {
    const u32 tb = lds0 + L_TZ + ch * TZ_C + ((n16 + 1) & 3) * TZ_S + (u32)((((n16 + 1) >> 2) + 2 * q + 8 * par2) * 8);
#pragma unroll
    for (int i = 0; i < 32; ++i) {
      v2u a, c;
      asm volatile("ds_read_b64 %0, %2 offset:%3\n\tds_read_b64 %1, %2 offset:%4" : "=&v"(a), "=&v"(c) : "v"(tb), "i"(128 * i), "i"(128 * i + 8) : "memory");
      T[i] = (v4u){a.x, a.y, c.x, c.y};
    }
    asm volatile("s_waitcnt lgkmcnt(0)" : "+v"(T[0]), "+v"(T[1]), "+v"(T[2]), "+v"(T[3]), "+v"(T[4]), "+v"(T[5]), "+v"(T[6]), "+v"(T[7]), "+v"(T[8]), "+v"(T[9]), "+v"(T[10]),
                 "+v"(T[11]), "+v"(T[12]), "+v"(T[13]), "+v"(T[14]), "+v"(T[15])::"memory");
    asm volatile("" : "+v"(T[16]), "+v"(T[17]), "+v"(T[18]), "+v"(T[19]), "+v"(T[20]), "+v"(T[21]), "+v"(T[22]), "+v"(T[23]), "+v"(T[24]), "+v"(T[25]), "+v"(T[26]),
                 "+v"(T[27]), "+v"(T[28]), "+v"(T[29]), "+v"(T[30]), "+v"(T[31])::"memory");
  }
  const int tB = 512 * par2 + 16 * n16 + 4 * q;
  LAS char* xp = lds + L_XR + ch * XR_S + (2032 - 16 * n16 + 32 * par2 + 8 * q) * 2 - 3584;
  LAS char* kp = lds + L_XR + ch * XR_S + (2044 - 1280 - tB) * 2;
  LAS char* gp = lds + L_S1 + tB * 8;
  LAS char* ep = lds + L_EX + w * 4096 + l * 16;
  LAS char* er = lds + L_EX + (w ^ 1) * 4096 + l * 16;
  u32 qo = O_QT + cB * 4096 + tB * 2;
  u32 oh = O_HT + (u32)((8 * (2 * par2 + (q & 1)) + (n16 >> 1)) * 32768 + (cB >> 5) * 2048 + (cB & 31) * 64 + (((2 * (n16 & 1) + (q >> 1)) ^ (2 * ((cB >> 3) & 1))) << 4));
  asm volatile("" : "+v"(xp), "+v"(kp), "+v"(gp), "+v"(ep), "+v"(er), "+v"(qo), "+v"(oh), "+v"(l16));
  const int hp = 4 * (x >> 2) + (y >> 3), fch = 8 * (x & 3) + (y & 7);
  const int ct = y & 7, rbl = y >> 3;
  const u32 ring = L_R + w * 12288;
  const int rot = fch & 3, spar = (fch >> 2) & 1;
  u32 w2v = O_W2T + (u32)((ct * 56 + 14 * w) * 4096) + l16;
  u32 ost = (fch < 8 && rr == q) ? O_ST2 + (u32)(rot * 32768 + (128 * hp + 32 * w + 16 * spar + n16) * 16) : 0xffff0000u;
  u32 og2 = O_ST2 + (u32)((l >> 5) * 65536 + (128 * hp + 32 * w + (l & 31)) * 16);
  u32 oact = O_ACT + (u32)(((8 * hp + 2 * w) * 224 + 7 * fch + q) * 256 + n16 * 16), oact2 = q < 3 ? oact + 1024 : 0xff000000u;
  u32 of3 = O_F3 + (u32)(((x * 4 + rbl) * 4 + w) * 128 + (l & 7) * 4), of3w = l == 0 ? O_F3 + (u32)(((hp * 4 + w) * 4 + (fch >> 3)) * 128 + (fch & 7) * 4) : 0xffff0000u;
  u32 av = O_ACT + (u32)(((8 * x + 2 * rbl) * 224 + 56 * w) * 256) + l16;
  LAS char* hx[2];
#pragma unroll
  for (int pr = 0; pr < 2; ++pr) hx[pr] = lds + ring + (8 * q + rr) * 64 + (((2 * (pr ^ spar) + (p4 >> 1)) ^ (2 * (q & 1))) << 4) + (p4 & 1) * 8;
  LAS char* pcs = lds + ring + 10240 + (n16 == 0 ? q * 16 : 128 + l * 16);
  LAS char* pcr = lds + L_R + 10240 + q * 16;
  asm volatile("" : "+v"(av), "+v"(pcs), "+v"(pcr), "+v"(w2v), "+v"(ost), "+v"(og2), "+v"(oact), "+v"(oact2), "+v"(of3), "+v"(of3w), "+v"(hx[0]), "+v"(hx[1]));
  STAMP(21);
  if (w == 0) spin(O_F1 + ((x ^ (4 * (l >> 5))) * 32 + (l & 31)) * 128, tag);
  BAR();
  STAMP(3);

  dma<true>(lds0 + ch * XR_S + par2 * 2048, L_XR, l16, rWS, cB * 4096 + par2 * 2048, O_KVT);
  dma<true>(lds0 + ch * XR_S + par2 * 2048, L_XR + 1024, l16, rWS, cB * 4096 + par2 * 2048, O_KVT + 1024);
#pragma unroll
  for (int i = 0; i < 4; ++i) dma<true>(lds0 + w * 4096, L_S1 + i * 1024, l16, rWS, w * 4096, O_ST1 + i * 1024);
  v2u xq[4];
  asm volatile("buffer_load_dwordx2 %0, %4, %5, 0 offen sc1\n\tbuffer_load_dwordx2 %1, %4, %5, 0 offen offset:512 sc1\n\t"
               "buffer_load_dwordx2 %2, %4, %5, 0 offen offset:2048 sc1\n\tbuffer_load_dwordx2 %3, %4, %5, 0 offen offset:2560 sc1"
               : "=&v"(xq[0]), "=&v"(xq[1]), "=&v"(xq[2]), "=&v"(xq[3]) : "v"(qo), "s"(rWS) : "memory");
  SB();
  asm volatile("s_waitcnt vmcnt(8)" : "+v"(w2raw[0]), "+v"(w2raw[1]), "+v"(w2raw[2]), "+v"(w2raw[3]), "+v"(w2raw[4]), "+v"(w2raw[5]), "+v"(w2raw[6]), "+v"(w2raw[7])::"memory");
  BAR();
  STAMP(4);
  v4u W2[56];
  auto w2l = [&](int i) { W2[i] = __builtin_amdgcn_raw_buffer_load_b128(WS, w2v + (i & 3) * 1024, (i >> 2) * 4096, SC1); };
  u32 hpk[4];
  GAS u32* pc2 = (GAS u32*)(A.ws + O_C2 + (l & 7) * 2048 + x * 256);
  auto put_h = [&](int h) {
    const auto s0 = __builtin_amdgcn_permlane16_swap(hpk[0], hpk[2], false, false);
    const auto s1 = __builtin_amdgcn_permlane16_swap(hpk[1], hpk[3], false, false);
    __builtin_amdgcn_raw_buffer_store_b128((v4u){s0[0], s1[0], s0[1], s1[1]}, WS, oh + h * (32 * 32768), 0, SC1);
  };
  const u32 oc2 = O_C2 + x * 2048 + (l & 7) * 256, c2want = tag * 64u + 32u;
  {
    u32 w1so, flw;
    auto w1l = [&](int i) {
      if ((i & 3) == 0) asm volatile("s_add_u32 m0, %1, %2\n\ts_add_u32 %0, %3, %4\n\tbuffer_load_dwordx4 %5, %6, %0 offen sc1 lds"
                                     : "=&s"(w1so) : "s"(lds0 + w * 28672), "i"(i * 1024), "s"(fch * 114688 + w * 28672), "i"(O_W1F + i * 1024), "v"(l16), "s"(rWS) : "memory", "scc");
      else asm volatile("buffer_load_dwordx4 %0, %1, %2 offen offset:%3 sc1 lds" ::"v"(l16), "s"(rWS), "s"(w1so), "i"((i & 3) * 1024) : "memory");
    };
    const float dsc = ch ? hi2f(dpair) : lo2f(dpair);
    asm volatile("" : "+v"(pc2));
    f32x4 cacc[8], mn[2], ot[2];
    v4u B[32], g1[8], w2f[2]; v2u kr[4];
    float xf[16], ln[16], kd[16], tm[16], hh[8];
    auto fix = [&](int h) {
      u32 df = 0;
#pragma unroll
      for (int i = 4 * h; i < 4 * h + 4; ++i) df |= (g1[i].y ^ vtag) | (g1[i].w ^ vtag);
      if (__builtin_amdgcn_ballot_w64(df != 0)) {
        bool ok;
        for (u32 sp = 0; sp < SPIN_CAP; ++sp) {
          asm volatile("" ::: "memory");
          ok = true;
#pragma unroll
          for (int i = 4 * h; i < 4 * h + 4; ++i) {
            g1[i] = __builtin_amdgcn_raw_buffer_load_b128(WS, O_ST1 + (tB + 256 * ((i >> 1) & 1) + 1024 * h) * 8 + (i & 1) * 16, 0, SC1);
            ok &= (g1[i].y == tag) & (g1[i].w == tag);
          }
          if (__all(ok)) break;
        }
#pragma unroll
        for (int j = 8 * h; j < 8 * h + 8; ++j) { const u32 pay = g1[j >> 1][(j & 1) * 2]; ln[j] = (xf[j] - lo2f(pay)) * __builtin_amdgcn_rcpf(hi2f(pay)); }
      }
    };
    auto give = [&](int h) {
      if (par2) { *(LAS f32x4*)(ep + 2048 * h) = cacc[4 * h]; *(LAS f32x4*)(ep + 2048 * h + 1024) = cacc[4 * h + 1]; mn[0] = cacc[4 * h + 2]; mn[1] = cacc[4 * h + 3]; asm volatile("" : "+v"(mn[0]), "+v"(mn[1])); }
      else { *(LAS f32x4*)(ep + 2048 * h) = cacc[4 * h + 2]; *(LAS f32x4*)(ep + 2048 * h + 1024) = cacc[4 * h + 3]; mn[0] = cacc[4 * h]; mn[1] = cacc[4 * h + 1]; asm volatile("" : "+v"(mn[0]), "+v"(mn[1])); }
    };
    auto pre = [&](auto J_, auto P_) {
      constexpr int j = decltype(J_)::value, ph = decltype(P_)::value;
      const u32 pay = g1[j >> 1][(j & 1) * 2];
      if constexpr (ph == 0) { xf[j] = (j & 1) ? hi2f(xq[j >> 2][(j & 3) >> 1]) : lo2f(xq[j >> 2][(j & 3) >> 1]); asm volatile("" : "+v"(xf[j])); }
      if constexpr (ph == 1) { tm[j] = __builtin_amdgcn_rcpf(hi2f(pay)); asm volatile("" : "+v"(tm[j])); }
      if constexpr (ph == 2) { ln[j] = xf[j] - lo2f(pay); asm volatile("" : "+v"(ln[j])); }
      if constexpr (ph == 3) { ln[j] *= tm[j]; asm volatile("" : "+v"(ln[j])); }
      if constexpr (ph == 4) { const u32 kk = kr[j >> 2][1 - ((j & 3) >> 1)]; kd[j] = (j & 1) ? lo2f(kk) : hi2f(kk); asm volatile("" : "+v"(kd[j])); }
    };
    sfor<0, 32>([&](auto U_) { constexpr int u = decltype(U_)::value; if constexpr (cv_slot(u) < 0) B[u] = *(LAS v4u*)(xp + 128 * (31 - u)); });
    SB();
    sfor<0, 144>([&](auto N_) {
      constexpr int n = decltype(N_)::value, u = cv_u(n), i = u < 16 ? u : u - 16, a = cv_a0(u) + n - cv_first(u), eps = 4 * a - i + 3;
      mfb<i == 0>(cacc[a], T[eps], B[i]);
      SB();
      constexpr int ub = cv_at(n);
      if constexpr (ub >= 0) B[ub] = *(LAS v4u*)(xp + 128 * (31 - ub));
      if constexpr (n < 112 && n % 4 == 1) w1l(n / 4);
      if constexpr (n == 2 || n == 3) {
        constexpr int pp = n - 2;
#pragma unroll
        for (int d = 0; d < 4; ++d) w2f[pp][d] = __builtin_amdgcn_perm(w2raw[2 * d + 1], w2raw[2 * d], pp ? 0x07060302u : 0x05040100u);
        __builtin_amdgcn_raw_buffer_store_b128(w2f[pp], WS, w2o + pp * 1024, 0, SC1);
      }
      if constexpr (n >= 11 && n < 27 && n % 4 == 3) { constexpr int aa = (n - 11) / 4; kr[aa] = *(LAS v2u*)(kp + 2560 - 512 * (aa & 1) - 2048 * (aa >> 1)); }
      if constexpr (n >= 23 && n < 39 && n % 2 == 1) pre(IC<(n - 23) / 2>{}, IC<4>{});
      if constexpr (n == 43) STAMP(25);
      if constexpr (n == 46) STAMP(26);
      if constexpr (n == 67) STAMP(27);
      if constexpr (n == 111) STAMP(28);
      if constexpr (n == 113) STAMP(22);
      if constexpr (n == 44) give(0);
      if constexpr (n == 45) {
        asm volatile("s_waitcnt vmcnt(11) lgkmcnt(0)" : "+v"(xq[0]), "+v"(xq[1]), "+v"(xq[2]), "+v"(xq[3])::"memory");
        __builtin_amdgcn_s_barrier();
        asm volatile("" ::: "memory");
        if (w == 0 && l < 8) __hip_atomic_fetch_add(pc2 + 8192, 1u, RLX);
      }
      if constexpr (n == 46 || n == 47) { g1[2 * (n - 46)] = *(LAS v4u*)(gp + (n - 46) * 2048); g1[2 * (n - 46) + 1] = *(LAS v4u*)(gp + (n - 46) * 2048 + 16); }
      if constexpr (n == 48) { ot[0] = *(LAS f32x4*)er; ot[1] = *(LAS f32x4*)(er + 1024); }
      if constexpr (n >= 50 && n < 58) { pre(IC<n - 50>{}, IC<0>{}); pre(IC<n - 50>{}, IC<1>{}); pre(IC<n - 50>{}, IC<2>{}); if constexpr (n > 50) pre(IC<n - 51>{}, IC<3>{}); }
      if constexpr (n == 58) { pre(IC<7>{}, IC<3>{}); fix(0); }
      if constexpr (n >= 59 && n < 63) {
#pragma unroll
        for (int j = 2 * (n - 59); j < 2 * (n - 59) + 2; ++j) { hh[j] = xf[j] + ln[j] * ((kd[j] * dsc + mn[j >> 2][j & 3]) + ot[j >> 2][j & 3]); asm volatile("" : "+v"(hh[j])); }
      }
      if constexpr (n == 63 || n == 64) { hpk[2 * (n - 63)] = pk(hh[4 * (n - 63)], hh[4 * (n - 63) + 1]); hpk[2 * (n - 63) + 1] = pk(hh[4 * (n - 63) + 2], hh[4 * (n - 63) + 3]); }
      if constexpr (n == 66) put_h(0);
      if constexpr (n == 98) asm volatile("buffer_load_dword %0, %1, %2, 0 offen sc1" : "=&v"(flw) : "v"(oc2 + 32768), "s"(rWS) : "memory");
      if constexpr (n == 112) {
        asm volatile("s_waitcnt vmcnt(3)" : "+v"(flw)::"memory");
        __builtin_amdgcn_s_barrier();
        if (w == 0 && l < 8) __hip_atomic_fetch_add(pc2, 1u, RLX);
        if (!__all(flw == c2want)) spin(oc2 + 32768, c2want);
      }
      if constexpr (n > 112 && n % 2 == 1 && (n - 113) / 2 < NWE) w2l((n - 113) / 2);
      if constexpr (n >= 90 && n < 98 && n % 2 == 0) { constexpr int j = 4 + (n - 90) / 2; g1[j] = *(LAS v4u*)(gp + 8192 + ((j >> 1) & 1) * 2048 + (j & 1) * 16); }
      if constexpr (n >= 104) pre(IC<8 + (n - 104) / 5>{}, IC<(n - 104) % 5>{});
      SB();
    });
    asm volatile("s_nop 7" ::: "memory");
    SB();
    STAMP(5);
    give(1);
    fix(1);
    WAITV(NWE < 16 ? NWE : 16);
    BAR();
#pragma unroll
    for (int i = 16; i < NWE; ++i) w2l(i);
#pragma unroll
    for (int aa = 0; aa < 2; ++aa) {
      const f32x4 o = *(LAS f32x4*)(er + 2048 + aa * 1024);
#pragma unroll
      for (int i = 0; i < 4; ++i) hh[4 * aa + i] = xf[8 + 4 * aa + i] + ln[8 + 4 * aa + i] * ((kd[8 + 4 * aa + i] * dsc + mn[aa][i]) + o[i]);
      hpk[2 * aa] = pk(hh[4 * aa], hh[4 * aa + 1]); hpk[2 * aa + 1] = pk(hh[4 * aa + 2], hh[4 * aa + 3]);
    }
    asm volatile("" : "+v"(hpk[0]), "+v"(hpk[1]), "+v"(hpk[2]), "+v"(hpk[3]));
    STAMP(6);
  }

  v4u ones = {0x3f803f80u, 0x3f803f80u, 0x3f803f80u, 0x3f803f80u};
  LAS char* wp0 = lds + l * 16;
  asm volatile("" : "+v"(ones), "+v"(wp0));
  u32 sg[4];
#pragma unroll
  for (int g = 0; g < 4; ++g) sg[g] = (u32)((g + rot) & 3);
  LAS char* wp = wp0 + sg[0] * 28672;
  v4u wf[WD + 1], hf[2][2], g2[2];
#pragma unroll
  for (int i = 0; i < WD; ++i) wf[i] = *(LAS v4u*)(wp + i * 1024);
  STAMP(7);
  if (w == 0) spin(oc2, c2want);
  BAR();
  STAMP(8);
  f32x4 cs[7];
  u32 fl3 = 0, fl2 = 0;
  v4u AC[28][2];
  const u32 ao[2] = {(u32)((w & 1) * (224 * 256)), (u32)(((w & 1) ^ 1) * (224 * 256))};
  auto ald = [&](int u) {
#pragma unroll
    for (int c = 0; c < 2; ++c) { AC[u][c] = __builtin_amdgcn_raw_buffer_load_b128(WS, av + (u % 14 & 3) * 1024, ao[c] + (u % 14 >> 2) * 4096 + (u / 14) * (64 * 224 * 256), SC1); SB(); }
  };
  {
    u32 hso;
    auto hdma = [&](int g, int n) {
      if (n == 0) asm volatile("s_add_u32 m0, %1, %2\n\ts_add_u32 %0, %3, %4\n\tbuffer_load_dwordx4 %5, %6, %0 offen sc1 lds"
                               : "=&s"(hso) : "s"(lds0 + ring), "i"((g % 5) * 2048), "s"((4 * hp + w) * 32768 + sg[(g >> 2) & 3] * 8192), "i"(O_HT + (g >> 4) * (32 * 32768) + (g & 3) * 2048), "v"(l16), "s"(rWS) : "memory", "scc");
      else asm volatile("buffer_load_dwordx4 %0, %1, %2 offen offset:1024 sc1 lds" ::"v"(l16), "s"(rWS), "s"(hso) : "memory");
    };
    auto hfrag = [&](int g, int pr, int t) { return trd(hx[pr], (g % 5) * 2048 + t * 256); };
    f32x4 acc[7][2], sS, sQ;
    auto epilogue = [&](auto P_) {
      constexpr int ps = decltype(P_)::value;
      if (!__all((g2[0].y == tag) & (g2[0].w == tag) & (g2[1].y == tag) & (g2[1].w == tag))) {
        for (u32 sp = 0; sp < SPIN_CAP; ++sp) {
          asm volatile("" ::: "memory");
#pragma unroll
          for (int k = 0; k < 2; ++k) g2[k] = __builtin_amdgcn_raw_buffer_load_b128(WS, og2, k * 32768 + ps * 16384, SC1);
          if (__all((g2[0].y == tag) & (g2[0].w == tag) & (g2[1].y == tag) & (g2[1].w == tag))) break;
        }
      }
      const u32 s1 = f2u(u2f(g2[0].x) + u2f(g2[1].x)), s2 = f2u(u2f(g2[0].z) + u2f(g2[1].z));
      const auto h1 = __builtin_amdgcn_permlane32_swap(s1, s1, false, false), h2 = __builtin_amdgcn_permlane32_swap(s2, s2, false, false);
      const float mean = (u2f(h1[0]) + u2f(h1[1])) * (1.f / D);
      const float v2 = __builtin_fmaxf((u2f(h2[0]) + u2f(h2[1])) * (1.f / D) - mean * mean, 0.f) + 1e-5f;
      const u32 e0 = f2u(-mean), e1 = f2u(-1.44269504089f * __builtin_amdgcn_rsqf(v2)), e2 = f2u(v2);
      const auto b0 = __builtin_amdgcn_permlane16_swap(e0, e0, false, false), b1 = __builtin_amdgcn_permlane16_swap(e1, e1, false, false),
                 b2 = __builtin_amdgcn_permlane16_swap(e2, e2, false, false);
      v4u sa[2], sb[2];
#pragma unroll
      for (int pr = 0; pr < 2; ++pr) {
        const bool up = (pr ^ spar) != 0;
        const float nm = u2f(up ? b0[1] : b0[0]), k1 = u2f(up ? b1[1] : b1[0]), vv = u2f(up ? b2[1] : b2[0]);
        u32 d[8];
        f32x2 a[7], bb[7], e[7];
#pragma unroll
        for (int t = 0; t < 7; ++t) a[t] = (f32x2){acc[t][pr][0], acc[t][pr][1]} + nm * (f32x2){cs[t][0], cs[t][1]};
        SB();
#pragma unroll
        for (int t = 0; t < 7; ++t) e[t] = a[t] * k1;
        SB();
#pragma unroll
        for (int t = 0; t < 7; ++t) {
          e[t] = (f32x2){__builtin_amdgcn_exp2f(e[t][0]), __builtin_amdgcn_exp2f(e[t][1])};
          bb[t] = (f32x2){acc[t][pr][2], acc[t][pr][3]} + nm * (f32x2){cs[t][2], cs[t][3]};
          if (ps == 0 && 7 * (1 - pr) + t < W2_EPI) w2l(w2_first(16) - W2_EPI + 7 * (1 - pr) + t);
          if (ps == 1 && t < 3) ald(8 + 3 * pr + t);
          SB();
        }
#pragma unroll
        for (int t = 0; t < 7; ++t) e[t] = e[t] * vv + vv;
        SB();
#pragma unroll
        for (int t = 0; t < 7; ++t) { e[t] = (f32x2){__builtin_amdgcn_rcpf(e[t][0]), __builtin_amdgcn_rcpf(e[t][1])}; a[t] *= bb[t]; SB(); }
#pragma unroll
        for (int t = 0; t < 7; ++t) a[t] *= e[t];
        SB();
#pragma unroll
        for (int t = 0; t < 7; ++t) d[t] = pk(a[t][0], a[t][1]);
        sa[pr] = (v4u){d[0], d[1], d[2], d[3]};
        const auto x0 = __builtin_amdgcn_permlane32_swap(d[4], d[6], false, false), x1 = __builtin_amdgcn_permlane32_swap(d[5], d[5], false, false);
        const auto y0 = __builtin_amdgcn_permlane16_swap(x0[0], x1[0], false, false), y1 = __builtin_amdgcn_permlane16_swap(x0[1], x1[1], false, false);
        sb[pr] = (v4u){y0[0], y0[1], y1[0], y1[1]};
        asm volatile("" : "+v"(sa[pr]), "+v"(sb[pr]));
        SB();
      }
#pragma unroll
      for (int pr = 0; pr < 2; ++pr) {
        const u32 ro = (u32)((pr ^ spar) * (224 * 256) + ps * (64 * 224 * 256));
        __builtin_amdgcn_raw_buffer_store_b128(sa[pr], WS, oact + ro, 0, SC1);
        __builtin_amdgcn_raw_buffer_store_b128(sb[pr], WS, oact2 + ro, 0, SC1);
      }
      SB();
    };
    hdma(0, 0); hdma(0, 1); hdma(1, 0); hdma(1, 1);
    {
      LAS char* c0 = wp0 + w * 1024;
      LAS char* c1 = wp0 + (w == 3 ? 3 : w + 4) * 1024;
      LAS char* c2 = c0 + 57344;
      LAS char* c3 = c1 + 57344;
      asm volatile("" : "+v"(c0), "+v"(c1), "+v"(c2), "+v"(c3));
      auto crd = [&](int k, int hi) { return *(LAS v4u*)((k < 8 ? (hi ? c1 : c0) : (hi ? c3 : c2)) + (k & 7) * 7168); };
      v4u fa[16], fb[16]; f32x4 ca, cb;
#pragma unroll
      for (int k = 0; k < 4; ++k) { fa[k] = crd(k, 0); fb[k] = crd(k, 1); }
      SB();
      sfor<0, 16>([&](auto K_) {
        constexpr int k = decltype(K_)::value;
        mfv<k == 0>(ca, fa[k], ones); SB();
        if constexpr (k + 4 < 16) fa[k + 4] = crd(k + 4, 0);
        SB();
        mfv<k == 0>(cb, fb[k], ones); SB();
        if constexpr (k + 4 < 16) fb[k + 4] = crd(k + 4, 1);
        if constexpr (k >= 2 && k < 8) hdma(2 + (k - 2) / 2, k & 1);
        if constexpr (k == 12) put_h(1);
        SB();
      });
      WAITV(9);
#pragma unroll
      for (int pr = 0; pr < 2; ++pr) { const v2u a = hfrag(0, pr, 0), c = hfrag(0, pr, 1); hf[0][pr] = (v4u){a.x, a.y, c.x, c.y}; }
      asm volatile("s_nop 7" ::: "memory");
      *(LAS f32x4*)pcs = ca; *(LAS f32x4*)(pcs + 64) = cb;
    }
    SB();
    STAMP(20);
    sfor<0, 32>([&](auto G_) {
      constexpr int g = decltype(G_)::value, s = g & 15;
      v4u(&hc)[2] = hf[g & 1]; v4u(&hn)[2] = hf[(g + 1) & 1];
      LAS char* wn = wp;
      if constexpr ((g & 3) == 3 && g < 31) wn = wp0 + sg[((g + 1) >> 2) & 3] * 28672;
      v2u ha;
      sfor<0, 7>([&](auto T_) {
        constexpr int t = decltype(T_)::value, it = g * 7 + t, nx = it + WD, ng = nx / 7, nt = nx % 7;
        if constexpr (t == 0) asm volatile("" : "+v"(wf[it % 8]), "+v"(wf[(it + 1) % 8]), "+v"(wf[(it + 2) % 8]), "+v"(wf[(it + 3) % 8]), "+v"(hc[0]), "+v"(hc[1]));
        if constexpr (t == 4) asm volatile("" : "+v"(wf[it % 8]), "+v"(wf[(it + 1) % 8]), "+v"(wf[(it + 2) % 8]));
        mfv<s == 0>(acc[t][0], wf[it % (WD + 1)], hc[0]);
        SB();
        if constexpr (nx < 224) wf[nx % (WD + 1)] = *(LAS v4u*)((ng > g ? wn : wp) + (ng & 3) * 7168 + nt * 1024);
        SB();
        mfv<s == 0>(acc[t][1], wf[it % (WD + 1)], hc[1]);
        SB();
        if constexpr (s < 15 && t < 4) {
          if constexpr (t == 0) { constexpr int hw = h_wait(g); WAITV(hw); }
          if constexpr (t & 1) { const v2u hb = hfrag(g + 1, t >> 1, 1); hn[t >> 1] = (v4u){ha.x, ha.y, hb.x, hb.y}; } else ha = hfrag(g + 1, t >> 1, 0);
        }
        if constexpr (t >= 4 && t - 4 < w2_n(g)) w2l(w2_first(g) + t - 4);
        if constexpr (s < 11 && t >= 5) hdma(g + 5, t - 5);
        if constexpr (g == 28 && t == 4) {
          constexpr int nw = vm_sum(26, 28);
          asm volatile("s_waitcnt vmcnt(%1)" : "+v"(fl3) : "n"(nw) : "memory");
          if (!__all(fl3 == tag)) spin(of3, tag);
        }
        if constexpr (g >= 28 && (t == 4 || t == 6)) ald(2 * (g - 28) + (t - 4) / 2);
        if constexpr (g == 5 || g == 6) { if constexpr (t >= 3 && 4 * (g - 5) + t - 3 < 7) { constexpr int k = 4 * (g - 5) + t - 3; cs[k] = *(LAS f32x4*)(pcr + (k & 3) * 12288 + (k >> 2) * 64); } }
        SB();
      });
      if constexpr (s < 4) { mfv<s == 0>(sS, ones, hc[0]); mfv<s == 0>(sQ, hc[0], hc[0]); }
      if constexpr (g == 4) {
        constexpr int nw = vm_sum(0, 4) + w2_n(4) + 2;
        WAITV(nw);
        __builtin_amdgcn_s_barrier();
        asm volatile("" ::: "memory");
        if (w == 0) { if (l < 8) __hip_atomic_fetch_add(pc2 + 4096, 1u, RLX); }
        else __builtin_amdgcn_raw_buffer_store_b32(vtag, WS, 0xffffff00u, 0, SC1);
      }
      if constexpr (g == 13) asm volatile("buffer_load_dword %0, %1, %2, 0 offen sc1" : "=&v"(fl2) : "v"(oc2 + 16384), "s"(rWS) : "memory");
      if constexpr (s == 5) {
        float q01, q23, qq;
        asm volatile("v_cndmask_b32 %0, %3, %4, %7\n\tv_cndmask_b32 %1, %5, %6, %7\n\tv_cndmask_b32 %2, %0, %1, %8"
                     : "=&v"(q01), "=&v"(q23), "=&v"(qq) : "v"(sQ[0]), "v"(sQ[1]), "v"(sQ[2]), "v"(sQ[3]), "s"(0xaaaaaaaaaaaaaaaaull), "s"(0xccccccccccccccccull));
        __builtin_amdgcn_raw_buffer_store_b128((v4u){f2u(sS[0]), vtag, f2u(qq), vtag}, WS, ost + (g >> 4) * 16384, 0, SC1);
      }
      if constexpr (s == 12) {
        asm volatile("buffer_load_dwordx4 %0, %2, %3, %4 offen sc1\n\tbuffer_load_dwordx4 %1, %2, %3, %5 offen sc1" : "=&v"(g2[0]), "=&v"(g2[1])
                     : "v"(og2), "s"(rWS), "s"((g >> 4) * 16384), "s"(32768 + (g >> 4) * 16384) : "memory");
      }
      if constexpr (g == 19) {
        constexpr int nw = vm_sum(16, 19) + w2_n(19) + 2;
        WAITV(nw);
        __builtin_amdgcn_raw_buffer_store_b32(vtag, WS, of3w, 0, SC1);
      }
      if constexpr (g == 27) {
        dma<true>(lds0 + ring, 4096, l16, rWS, (32 * (w >> 1) + 4 * x + rbl) * 32768 + (2 * ct + (w & 1)) * 2048, O_HT);
        dma<true>(lds0 + ring, 5120, l16, rWS, (32 * (w >> 1) + 4 * x + rbl) * 32768 + (2 * ct + (w & 1)) * 2048, O_HT + 1024);
      }
      if constexpr (g == 25) asm volatile("buffer_load_dword %0, %1, %2, 0 offen sc1" : "=&v"(fl3) : "v"(of3), "s"(rWS) : "memory");
      SB();
      wp = wn;
      if constexpr ((g & 7) == 7) STAMP(16 + (g >> 3));
      if constexpr (s == 15) {
        constexpr int nw = g == 15 ? vm_sum(14, 16) : vm_sum(29, 32);
        asm volatile("s_waitcnt vmcnt(%3)\n\ts_nop 7" : "+v"(g2[0]), "+v"(g2[1]), "+v"(fl2) : "n"(nw) : "memory");
        SB();
        if constexpr (g == 15) {
          if (!__all(fl2 == c2want)) spin(oc2 + 16384, c2want);
#pragma unroll
          for (int k = 16; k < 21; ++k) { hdma(k, 0); hdma(k, 1); }
        }
        epilogue(IC<(g >> 4)>{});
        if constexpr (g == 15) {
          WAITV(8 + vm_pre(1));
#pragma unroll
          for (int pr = 0; pr < 2; ++pr) { const v2u a = hfrag(16, pr, 0), c = hfrag(16, pr, 1); hf[0][pr] = (v4u){a.x, a.y, c.x, c.y}; }
          SB();
        }
        if constexpr (g == 15) STAMP(9);
      }
    });
  }
  STAMP(10);

  {
    __builtin_amdgcn_s_barrier();
    asm volatile("" ::: "memory");
    v2u hr[4];
#pragma unroll
    for (int i = 0; i < 4; ++i)
      hr[i] = trd(lds, L_R + (u32)(((w & 2) + (i >> 1)) * 12288 + 4096 + (8 * q + 4 * (i & 1) + rr) * 64 + (((2 * (w & 1) + (p4 >> 1)) ^ (2 * (q & 1))) << 4) + (p4 & 1) * 8));
    LAS char* pw = lds + w * 4096 + l * 16;
    u32 f3b;
    f32x4 o[4][2], mine[4];
    SB();
    sfor<0, 28>([&](auto U_) {
      constexpr int u = decltype(U_)::value;
#pragma unroll
      for (int c = 0; c < 2; ++c)
#pragma unroll
        for (int pp = 0; pp < 4; ++pp) mfa<u % 14 == 0>(o[pp][c], W2[4 * (u % 14) + pp], AC[u][c]);
      SB();
      if constexpr (u == 5) {
        WAITV(0);
        __builtin_amdgcn_raw_buffer_store_b32(vtag, WS, of3w + 16384, 0, SC1);
        SB();
      }
      if constexpr (u == UPOLL) { f3b = __builtin_amdgcn_raw_buffer_load_b32(WS, of3 + 16384, 0, SC1); SB(); }
      if constexpr (u == UCHK) {
        if (!__all(f3b == tag)) spin(of3 + 16384, tag);
        STAMP(12);
#pragma unroll
        for (int v = 14; v < 20; ++v) ald(v);
      }
      if constexpr (u > UCHK && u <= UCHK + 8) ald(u - UCHK + 19);
      if constexpr (u % 14 == 13) {
        asm volatile("s_nop 7" ::: "memory");
        SB();
        constexpr int hfb = 2 * (u / 14);
#pragma unroll
        for (int pp = 0; pp < 4; ++pp) *(LAS f32x4*)(pw + ((hfb + ((w & 1) ^ 1)) * 16384) + pp * 1024) = o[pp][1];
        if ((w & 2) == hfb) {
#pragma unroll
          for (int pp = 0; pp < 4; ++pp) { mine[pp] = o[pp][0]; asm volatile("" : "+v"(mine[pp])); }
        } else {
#pragma unroll
          for (int pp = 0; pp < 4; ++pp) *(LAS f32x4*)(pw + ((hfb + (w & 1)) * 16384) + pp * 1024) = o[pp][0];
        }
        if constexpr (u == 13) STAMP(11);
        SB();
      }
    });
    STAMP(13);
    BAR();
#pragma unroll
    for (int j = 1; j < 4; ++j)
#pragma unroll
      for (int pp = 0; pp < 4; ++pp) mine[pp] += *(LAS f32x4*)(lds + w * 16384 + ((w + j) & 3) * 4096 + pp * 1024 + l * 16);
    u32 r[8];
#pragma unroll
    for (int i = 0; i < 4; ++i) {
      r[2 * i] = pk(lo2f(hr[i].x) + mine[0][i], hi2f(hr[i].x) + mine[1][i]);
      r[2 * i + 1] = pk(lo2f(hr[i].y) + mine[2][i], hi2f(hr[i].y) + mine[3][i]);
    }
    u16* op = A.out + (1024 * (w >> 1) + 128 * x + 32 * rbl + 16 * (w & 1) + n16) * D + 64 * ct + 8 * q;
    *(v4u*)op = (v4u){r[0], r[1], r[2], r[3]};
    *(v4u*)(op + 32) = (v4u){r[4], r[5], r[6], r[7]};
    if (b == 0 && w == 0 && l == 0) __builtin_amdgcn_raw_buffer_store_b32(tag, WS, O_EP, 0, SC1);
  }
  STAMP(14);
  WAITV(0);
  STAMP(15);
  { const u64 until = pt0 + 5000ull + 100ull * (ptk - pt0); while (__builtin_amdgcn_s_memrealtime() < until) __builtin_amdgcn_s_sleep(8); }
}
}

extern "C" void kernel_launch(void* const* d_in, const int* in_sizes, int n_in, void* d_out, int out_size, void* d_ws, size_t ws_size, hipStream_t stream) {
  fk::Args a;
  a.q = (const fk::u16*)d_in[0]; a.k = (const fk::u16*)d_in[1]; a.v = (const fk::u16*)d_in[2]; a.shk = (const fk::u16*)d_in[3];
  a.ssk = (const fk::u16*)d_in[4]; a.ssd = (const fk::u16*)d_in[5]; a.wg = (const fk::u16*)d_in[7]; a.wu = (const fk::u16*)d_in[8];
  a.wd = (const fk::u16*)d_in[9]; a.out = (fk::u16*)d_out; a.ws = (char*)d_ws;
  fk::fused<<<256, 256, 0, stream>>>(a);
}
```

```cpp
#define PFC 1
#define PFT 0
#include <hip/hip_runtime.h>
#include <stdint.h>
#define NWE 20
#define UPOLL 8
#define UCHK 11

namespace fk {
constexpr int WD = 7;
typedef unsigned u32;
typedef unsigned short u16;
typedef unsigned long long u64;
typedef u32 v4u __attribute__((ext_vector_type(4)));
typedef u32 v2u __attribute__((ext_vector_type(2)));
typedef float f32x4 __attribute__((ext_vector_type(4)));
typedef float f32x2 __attribute__((ext_vector_type(2)));
typedef short s16x4 __attribute__((ext_vector_type(4)));
typedef short s16x8 __attribute__((ext_vector_type(8)));
typedef __bf16 bf2 __attribute__((ext_vector_type(2)));
#define LAS __attribute__((address_space(3)))
#define GAS __attribute__((address_space(1)))
#define RLX __ATOMIC_RELAXED, __HIP_MEMORY_SCOPE_AGENT
#define SB() __builtin_amdgcn_sched_barrier(0)
#define WAITV(n) asm volatile("s_waitcnt vmcnt(%0)" ::"n"(n) : "memory")
#define BAR() do { asm volatile("s_waitcnt lgkmcnt(0)" ::: "memory"); __builtin_amdgcn_s_barrier(); asm volatile("" ::: "memory"); } while (0)

constexpr int S = 2048, D = 512, F = 1792;
constexpr u32 O_EP = 0, O_C2 = 16384, O_ST1 = 65536, O_F1 = 98304, O_ST2 = 131072, O_F3 = 262144;
constexpr u32 O_KVT = 1u << 20, O_QT = 3u << 20, O_HT = 5u << 20, O_W2T = 7u << 20, O_ACT = 9u << 20, O_W1F = 17u << 20, WS_BYTES = 21u << 20;
constexpr u32 O_DIAG = 24u << 20;
constexpr u32 L_R = 114688, L_K = L_R, L_V = L_R + 9216, L_Q = L_R + 17408, L_WR = 0, WR_S = 1056, L_TZ = 36864, TZ_S = 4160, TZ_C = 4 * TZ_S, L_XR = L_R + 33280, XR_S = 4608,
              L_S1 = L_R, L_EX = L_R + 16384, LDS_BYTES = 163840;
constexpr u32 SPIN_CAP = 1u << 18;
constexpr int SC1 = 16;

struct Args { const u16 *q, *k, *v, *shk, *ssk, *ssd, *wg, *wu, *wd; u16* out; char* ws; };

__device__ __forceinline__ u32 f2u(float x) { return __builtin_bit_cast(u32, x); }
__device__ __forceinline__ float u2f(u32 x) { return __builtin_bit_cast(float, x); }
__device__ __forceinline__ float lo2f(u32 x) { return u2f(x << 16); }
__device__ __forceinline__ float hi2f(u32 x) { return u2f(x & 0xffff0000u); }
__device__ __forceinline__ u32 pk(float lo, float hi) { bf2 r = {(__bf16)lo, (__bf16)hi}; return __builtin_bit_cast(u32, r); }
__device__ __forceinline__ float rbf(float x) { return lo2f(pk(x, x)); }
__device__ __forceinline__ f32x4 mfma(v4u a, v4u b, f32x4 c) {
  return __builtin_amdgcn_mfma_f32_16x16x32_bf16(__builtin_bit_cast(s16x8, a), __builtin_bit_cast(s16x8, b), c, 0, 0, 0);
}
template <bool FIRST> __device__ __forceinline__ void mfv(f32x4& c, v4u a, v4u b) {
  if (FIRST) asm volatile("v_mfma_f32_16x16x32_bf16 %0, %1, %2, 0" : "=&v"(c) : "v"(a), "v"(b));
  else asm volatile("v_mfma_f32_16x16x32_bf16 %0, %1, %2, %0" : "+v"(c) : "v"(a), "v"(b));
}
template <bool FIRST> __device__ __forceinline__ void mfa(f32x4& c, v4u a, v4u b) {
  if (FIRST) asm volatile("v_mfma_f32_16x16x32_bf16 %0, %1, %2, 0" : "=&v"(c) : "a"(a), "v"(b));
  else asm volatile("v_mfma_f32_16x16x32_bf16 %0, %1, %2, %0" : "+v"(c) : "a"(a), "v"(b));
}
template <bool FIRST> __device__ __forceinline__ void mfb(f32x4& c, v4u a, v4u b) {
  if (FIRST) asm volatile("v_mfma_f32_16x16x32_bf16 %0, %1, %2, 0" : "=&v"(c) : "v"(a), "a"(b));
  else asm volatile("v_mfma_f32_16x16x32_bf16 %0, %1, %2, %0" : "+v"(c) : "v"(a), "a"(b));
}
__device__ __forceinline__ v2u trd(LAS char* lds, u32 off) {
  return __builtin_bit_cast(v2u, __builtin_amdgcn_ds_read_tr16_b64_v4i16((LAS s16x4*)(lds + off)));
}
__device__ __forceinline__ v4u mkrs(const void* p, u32 bytes) { u64 a = (u64)p; return (v4u){(u32)a, (u32)(a >> 32) & 0xffffu, bytes, 0x00020000u}; }
template <bool COH> __device__ __forceinline__ void dma(u32 lb, u32 lo, u32 voff, v4u rs, u32 sb, u32 so) {
  u32 t;
  if (COH) asm volatile("s_add_u32 m0, %1, %6\n\ts_add_u32 %0, %4, %5\n\tbuffer_load_dwordx4 %2, %3, %0 offen sc1 lds" : "=&s"(t) : "s"(lb), "v"(voff), "s"(rs), "s"(sb), "i"(so), "i"(lo) : "memory", "scc");
  else asm volatile("s_add_u32 m0, %1, %6\n\ts_add_u32 %0, %4, %5\n\tbuffer_load_dwordx4 %2, %3, %0 offen lds" : "=&s"(t) : "s"(lb), "v"(voff), "s"(rs), "s"(sb), "i"(so), "i"(lo) : "memory", "scc");
}
template <int CTRL, int RM> __device__ __forceinline__ float dppadd(float v) {
  return v + __builtin_bit_cast(float, __builtin_amdgcn_update_dpp(0, __builtin_bit_cast(int, v), CTRL, RM, 0xf, true));
}
__device__ __forceinline__ float hsum(float v) {
  v = dppadd<0x111, 0xf>(v); v = dppadd<0x112, 0xf>(v); v = dppadd<0x114, 0xf>(v); v = dppadd<0x118, 0xf>(v);
  return dppadd<0x142, 0xa>(v);
}
#define STAMP(i)
template <int V> struct IC { static constexpr int value = V; };
template <int B, int E, class Fn> __device__ __forceinline__ void sfor(Fn&& f) { if constexpr (B < E) { f(IC<B>{}); sfor<B + 1, E>(f); } }
constexpr int cv_a0(int u) { return u < 16 ? u >> 2 : ((u - 16) >> 2) > 4 ? (u - 16) >> 2 : 4; }
constexpr int cv_first(int u) { int n = 0; for (int j = 0; j < u; ++j) n += (j < 16 ? 4 : 8) - cv_a0(j); return n; }
constexpr int cv_u(int n) { int u = 0; while (cv_first(u + 1) <= n) ++u; return u; }
constexpr int cv_slot(int i) { int s_ = cv_first(i < 16 ? i : i + 16) - 12; if (i < 31) { const int v = cv_slot(i + 1) - 2; if (v < s_) s_ = v; } return s_ & ~1; }
constexpr int cv_at(int n) { for (int i = 0; i < 32; ++i) if (cv_slot(i) == n) return i; return -1; }
constexpr int w2_n(int g) { return g >= 27 ? 0 : (g & 15) < 11 ? ((g & 1) ^ 1) : 3; }
constexpr int W2_EPI = 56 - 27 - NWE;
constexpr int w2_first(int g) { int n = NWE + (g > 15 ? W2_EPI : 0); for (int j = 0; j < g; ++j) n += w2_n(j); return n; }
constexpr int a_n(int g) { return g >= 28 ? 4 : 0; }
constexpr int vm_tail(int g) { return (g == 4) + ((g & 15) == 5) + 2 * ((g & 15) == 12) + (g == 13) + (g == 19) + (g == 25) + 2 * (g == 27); }
constexpr int vm_ops(int g) { return w2_n(g) + a_n(g) + ((g & 15) < 11 ? 2 : 0) + vm_tail(g); }
constexpr int vm_sum(int a, int e) { int n = 0; for (int j = a; j < e; ++j) n += vm_ops(j); return n; }
constexpr int vm_pre(int ps) { return ps ? W2_EPI + 4 : 1; }
constexpr int h_wait(int g) {
  return (g & 15) < 4 ? 2 * (3 - (g & 15)) + vm_pre(g >> 4) + vm_sum(g & 16, g) : vm_tail(g - 4) + vm_sum(g - 3, g);
}
static_assert(w2_first(32) == 56 && W2_EPI >= 0 && W2_EPI <= 14, "all w_down fragments are requested");

__global__ void __launch_bounds__(256, 1) fused(Args A) {
  __shared__ __attribute__((aligned(16))) char smem_[LDS_BYTES];
  LAS char* lds = (LAS char*)smem_;
  const u32 lds0 = (u32)(uintptr_t)lds;
  STAMP(0);
  const int b = blockIdx.x, x = b & 7, y = b >> 3;
  const int l = threadIdx.x & 63, w = __builtin_amdgcn_readfirstlane(threadIdx.x >> 6);
  const int n16 = l & 15, q = l >> 4, rr = (l >> 2) & 3, p4 = l & 3;
  const __amdgpu_buffer_rsrc_t WS = __builtin_amdgcn_make_buffer_rsrc(A.ws, 0, WS_BYTES, 0x00020000);
  const v4u rWS = mkrs(A.ws, WS_BYTES);
  auto spin = [&](u32 off, u32 want) {
    for (u32 sp = 0; sp < SPIN_CAP; ++sp) {
      asm volatile("" ::: "memory");
      if (__all(__builtin_amdgcn_raw_buffer_load_b32(WS, off, 0, SC1) == want)) break;
    }
  };

  const int ti = 4 * x + (y >> 3), cj = y & 7;
  const int cA = 64 * cj + 16 * w + n16;
  const int srow = 256 * x + 8 * y + 2 * w;
  const int ch = w >> 1, par2 = w & 1, cB = 64 * x + 2 * y + ch;
  v2u shk; v4u sx0, sx1, kz[2];
  v2u kedge; u32 vtag, vdp;
  u32 l16 = l * 16;
  {
    const v4u rK = mkrs(A.k, S * D * 2), rV = mkrs(A.v, S * D * 2), rQ = mkrs(A.q, S * D * 2);
    const u32 c0 = ((l & 7) ^ ((l >> 3) & 2)) * 16, tv = (u32)((64 * ti + (l >> 3)) * 1024 + 128 * cj);
    const u32 va = tv + c0, vb = tv + (c0 ^ 64);
    const u32 vk = ((w & 1) ? vb : va) + (u32)(w * 8192 - 8192);
    dma<false>(lds0 + w * 1024, L_K, vk, rK, 0, 0);
    dma<false>(lds0 + w * 1024, L_K + 4096, vk + 32768, rK, 0, 0);
    if (w == 0) dma<false>(lds0, L_K + 8192, vk + 65536, rK, 0, 0);
    dma<false>(lds0 + w * 2048, L_V, va, rV, w * 16384, 0);
    dma<false>(lds0 + w * 2048, L_V + 1024, vb, rV, w * 16384, 8192);
    asm volatile("global_load_dwordx2 %0, %1, %2" : "=&v"(shk) : "v"(cA * 8), "s"(A.shk) : "memory");
    asm volatile("global_load_dwordx4 %0, %2, %3\n\tglobal_load_dwordx4 %1, %2, %3 offset:1024" : "=&v"(kz[0]), "=&v"(kz[1]) : "v"(l16), "s"(A.ssk + cB * S + 1024 * par2) : "memory");
    asm volatile("global_load_dwordx2 %0, %3, %4 offset:2048\n\tglobal_load_dword %1, %3, %5\n\tglobal_load_dword %2, %3, %6"
                 : "=&v"(kedge), "=&v"(vtag), "=&v"(vdp) : "v"(0), "s"(A.ssk + cB * S), "s"(A.ws + O_EP), "s"(A.ssd + 64 * x + 2 * y) : "memory");
    dma<false>(lds0 + w * 2048, L_Q, va, rQ, w * 16384, 0);
    dma<false>(lds0 + w * 2048, L_Q + 1024, vb, rQ, w * 16384, 8192);
    asm volatile("global_load_dwordx4 %0, %2, %3\n\tglobal_load_dwordx4 %1, %2, %3 offset:512" : "=&v"(sx0), "=&v"(sx1) : "v"((l >> 5) * 1024 + (l & 31) * 16), "s"(A.q + srow * D) : "memory");
  }
  u32 pfd;
  {
    u64 pc;
    asm volatile("s_getpc_b64 %0" : "=s"(pc));
    constexpr u32 CODE = 36864;
    const u32 co = (u32)(((y & 7) * 4 + w) * 1280 + (l < 10 ? l : 0) * 128 + 4096) % CODE;
    const u32 to = l < 10 ? 0u : l < 22 ? (u32)(l - 9) * (2u << 20) - 4096u : 0u;
    const u64 pa = (l < 10 || !PFT) ? (PFC ? pc + co : pc) : (l == 22 ? (u64)A.out : (u64)A.ws + (to < WS_BYTES ? to : 0u));
    asm volatile("global_load_dword %0, %1, off" : "=&v"(pfd) : "v"(pa & ~3ull) : "memory");
  }
  const int rb = 8 * x + 2 * (y >> 3) + ((y >> 2) & 1), fg = y & 3;
  const v4u rW = mkrs(ch ? A.wu : A.wg, D * F * 2);
  u32 wrv = l < 56 ? l16 : 0xffffff00u, w1d[4];
  LAS char* pW = lds + L_WR + ((p4 >> 1) * 8 + rr) * WR_S + (4 * w + q) * 16 + (p4 & 1) * 8;
#pragma unroll
  for (int j = 0; j < 4; ++j) {
    int Z = 16 * j + 4 * w + q;
    asm volatile("" : "+v"(Z));
    const int c = (Z * 37) >> 8;
    w1d[j] = (j < 3 || Z < 56) ? O_W1F + (u32)(((8 * fg + c) * 16 + (rb >> 2)) * 7168 + (Z - 7 * c) * 1024 + (rb & 3) * 256 + (4 * ((n16 & 7) >> 1) + 2 * (n16 >> 3) + (n16 & 1)) * 16) : 0xffffff00u;
  }
  const u32 aAo = (u32)((8 * q + rr) * 128 + (((2 * w + (p4 >> 1)) ^ (rr & 2) ^ ((q & 1) << 2)) << 4) + (p4 & 1) * 8);
  LAS char* pA = lds + L_K + aAo;
  LAS char* pB = lds + L_K + (aAo ^ 64);
  u32 okv = O_KVT + cA * 4096 + (2040 - 32 - 64 * ti - 8 * q) * 2, oqt = O_QT + cA * 4096 + (64 * ti + 8 * q) * 2;
  LAS char* tw = lds + L_TZ + ch * TZ_C + (128 * par2 + l) * 16 + 32;
  u32 og = (l & 31) == 31 ? O_ST1 + (srow + (l >> 5)) * 8 : 0xffffff00u;
  u32 oci = (x == 0 && y < 8 && w == 0 && l < 24) ? O_C2 + l * 2048 + y * 256 : 0xffffff00u;
  asm volatile("" : "+v"(w1d[0]), "+v"(w1d[1]), "+v"(w1d[2]), "+v"(w1d[3]), "+v"(pA), "+v"(pB), "+v"(okv), "+v"(oqt), "+v"(tw), "+v"(og), "+v"(wrv), "+v"(pW), "+v"(oci));
  const int wU = b < 224 ? 2 * b + ch : 0;
  u32 ow[4], w2o = b < 224 ? O_W2T + (u32)(((wU & 7) * 56 + (wU >> 3)) * 4096 + par2 * 2048) + l16 : 0xffff0000u;
  {
    const int UU = 4 * (wU >> 3) + q, f = UU / 7, u = UU - 7 * f;
    const int col = 64 * (wU & 7) + 8 * rr + 4 * (p4 & 1) + 32 * (p4 >> 1) + 2 * par2;
#pragma unroll
    for (int d = 0; d < 4; ++d) ow[d] = (u32)(((56 * f + (u < 4 ? 2 * u + 8 * d : 8 * u + 2 * d)) * D + col) * 2);
  }
  asm volatile("" : "+v"(w2o), "+v"(ow[0]), "+v"(ow[1]), "+v"(ow[2]), "+v"(ow[3]));
  if (w == 0) *(LAS v4u*)(lds + L_XR + (l >> 5) * XR_S + 4096 + (l & 31) * 16) = (v4u){0, 0, 0, 0};
  SB();
  STAMP(23);
  asm volatile("s_waitcnt vmcnt(10)" : "+v"(shk)::"memory");
  BAR();
  STAMP(1);
  u32 tag, dpair;
  {
    v2u ka[2], kb[2], kc[2], va[2], vb[2];
#pragma unroll
    for (int it = 0; it < 2; ++it) { ka[it] = trd(pA, it * 4096 + 512); kb[it] = trd(pB, it * 4096 + 1024); kc[it] = trd(pB, it * 4096 + 1536); }
#pragma unroll
    for (int it = 0; it < 2; ++it) { va[it] = trd(pA, L_V - L_K + it * 4096); vb[it] = trd(pA, L_V - L_K + it * 4096 + 512); }
    SB();
#pragma unroll
    for (int j = 0; j < 4; ++j) dma<false>(lds0 + (8 * ch + 4 * par2) * WR_S, L_WR + j * WR_S, wrv, rW, (8 * rb + 4 * par2) * (F * 2) + fg * 896, j * (F * 2));
    SB();
    auto LO = [](u32 a, u32 c) { return (f32x2){lo2f(a), lo2f(c)}; };
    auto HI = [](u32 a, u32 c) { return (f32x2){hi2f(a), hi2f(c)}; };
    const f32x2 w0 = LO(shk.x, shk.x), w1 = HI(shk.x, shk.x), w2 = LO(shk.y, shk.y), w3 = HI(shk.y, shk.y);
    const f32x2 e[12] = {w0, HI(ka[0].x, ka[1].x), LO(ka[0].y, ka[1].y), HI(ka[0].y, ka[1].y), LO(kb[0].x, kb[1].x), HI(kb[0].x, kb[1].x), LO(kb[0].y, kb[1].y),
                         HI(kb[0].y, kb[1].y), LO(kc[0].x, kc[1].x), HI(kc[0].x, kc[1].x), LO(kc[0].y, kc[1].y), HI(kc[0].y, kc[1].y)};
    const f32x2 vv[8] = {LO(va[0].x, va[1].x), HI(va[0].x, va[1].x), LO(va[0].y, va[1].y), HI(va[0].y, va[1].y), LO(vb[0].x, vb[1].x), HI(vb[0].x, vb[1].x), LO(vb[0].y, vb[1].y), HI(vb[0].y, vb[1].y)};
    f32x2 kv[8];
#pragma unroll
    for (int s = 0; s < 8; ++s) kv[s] = (w3 * e[s + 1] + w2 * e[s + 2] + w1 * e[s + 3] + w0 * e[s + 4]) * vv[s];
#pragma unroll
    for (int it = 0; it < 2; ++it)
      __builtin_amdgcn_raw_buffer_store_b128((v4u){pk(kv[7][it], kv[6][it]), pk(kv[5][it], kv[4][it]), pk(kv[3][it], kv[2][it]), pk(kv[1][it], kv[0][it])}, WS, okv + (1 - it) * 64, 0, SC1);
    SB();
    asm volatile("s_waitcnt vmcnt(9)" : "+v"(kz[0]), "+v"(kz[1]), "+v"(kedge), "+v"(vtag), "+v"(vdp)::"memory");
    BAR();
    vtag += 1u;
    tag = __builtin_amdgcn_readfirstlane(vtag); dpair = __builtin_amdgcn_readfirstlane(vdp);
    v2u qa[2], qb[2];
#pragma unroll
    for (int it = 0; it < 2; ++it) { qa[it] = trd(pA, L_Q - L_K + it * 4096); qb[it] = trd(pA, L_Q - L_K + it * 4096 + 512); }
#pragma unroll
    for (int it = 0; it < 2; ++it) __builtin_amdgcn_raw_buffer_store_b128((v4u){qa[it].x, qa[it].y, qb[it].x, qb[it].y}, WS, oqt + it * 64, 0, SC1);
    __builtin_amdgcn_raw_buffer_store_b32(tag * 64u, WS, oci, 0, SC1);
  }
  SB();
  STAMP(24);
  {
    auto put = [&](LAS char* p, v4u d, u32 d4, u32 d5) {
      const u32 e0 = __builtin_amdgcn_alignbit(d.y, d.x, 16), e1 = __builtin_amdgcn_alignbit(d.z, d.y, 16), e2 = __builtin_amdgcn_alignbit(d.w, d.z, 16),
                e3 = __builtin_amdgcn_alignbit(d4, d.w, 16), e4 = __builtin_amdgcn_alignbit(d5, d4, 16);
      *(LAS v4u*)p = d; *(LAS v4u*)(p + TZ_S) = (v4u){e0, e1, e2, e3}; *(LAS v4u*)(p + 2 * TZ_S) = (v4u){d.y, d.z, d.w, d4}; *(LAS v4u*)(p + 3 * TZ_S) = (v4u){e1, e2, e3, e4};
    };
    auto nxt = [](u32 last, u32 v) { return (u32)__builtin_amdgcn_update_dpp((int)last, (int)v, 0x130, 0xf, 0xf, false); };
    put(tw, kz[0], nxt(__builtin_amdgcn_readlane(kz[1].x, 0), kz[0].x), nxt(__builtin_amdgcn_readlane(kz[1].y, 0), kz[0].y));
    put(tw + 1024, kz[1], nxt(par2 ? 0u : kedge.x, kz[1].x), nxt(par2 ? 0u : kedge.y, kz[1].y));
    const u32 f0 = __builtin_amdgcn_readlane(kz[0].x, 0), f1 = __builtin_amdgcn_readlane(kz[0].y, 0);
    if (l < 2 && !par2) put(tw - 32, (v4u){0, 0, 0, 0}, l ? f0 : 0u, l ? f1 : 0u);
  }
  SB();
  asm volatile("s_waitcnt vmcnt(5)" : "+v"(sx0), "+v"(sx1), "+v"(pfd)::"memory");
  BAR();
  {
    v2u ta[4], tc[4];
#pragma unroll
    for (int j = 0; j < 4; ++j) { ta[j] = trd(pW, j * 256); tc[j] = trd(pW, j * 256 + 4 * WR_S); }
#pragma unroll
    for (int j = 0; j < 4; ++j) __builtin_amdgcn_raw_buffer_store_b128((v4u){ta[j].x, ta[j].y, tc[j].x, tc[j].y}, WS, w1d[j], 0, SC1);
  }
  SB();
  {
    const f32x2 v[8] = {{lo2f(sx0.x), hi2f(sx0.x)}, {lo2f(sx0.y), hi2f(sx0.y)}, {lo2f(sx0.z), hi2f(sx0.z)}, {lo2f(sx0.w), hi2f(sx0.w)},
                        {lo2f(sx1.x), hi2f(sx1.x)}, {lo2f(sx1.y), hi2f(sx1.y)}, {lo2f(sx1.z), hi2f(sx1.z)}, {lo2f(sx1.w), hi2f(sx1.w)}};
    const f32x2 s2 = ((v[0] + v[1]) + (v[2] + v[3])) + ((v[4] + v[5]) + (v[6] + v[7]));
    const float sm = hsum(s2[0] + s2[1]);
    const float m0 = u2f(__builtin_amdgcn_readlane(f2u(sm), 31)), m1 = u2f(__builtin_amdgcn_readlane(f2u(sm), 63));
    const float mean = (l < 32 ? m0 : m1) * (1.f / D);
    f32x2 ss = {0.f, 0.f};
#pragma unroll
    for (int j = 0; j < 8; ++j) { const f32x2 d = v[j] - mean; ss += d * d; }
    const float var = hsum(ss[0] + ss[1]) * (1.f / D);
    const u32 pay = pk(mean, __builtin_amdgcn_sqrtf(rbf(rbf(var) + 1e-5f)));
    __builtin_amdgcn_raw_buffer_store_b64((v2u){pay, vtag}, WS, og, 0, SC1);
  }
  SB();
  WAITV(1);
  BAR();
  STAMP(2);
  if (w == 0 && l == 0) __builtin_amdgcn_raw_buffer_store_b32(tag, WS, O_F1 + (cj * 32 + ti) * 128, 0, SC1);
  u32 w2raw[8];
  asm volatile("global_load_dword %0, %8, %12\n\tglobal_load_dword %1, %8, %12 offset:1024\n\tglobal_load_dword %2, %9, %12\n\tglobal_load_dword %3, %9, %12 offset:1024\n\t"
               "global_load_dword %4, %10, %12\n\tglobal_load_dword %5, %10, %12 offset:1024\n\tglobal_load_dword %6, %11, %12\n\tglobal_load_dword %7, %11, %12 offset:1024"
               : "=&v"(w2raw[0]), "=&v"(w2raw[1]), "=&v"(w2raw[2]), "=&v"(w2raw[3]), "=&v"(w2raw[4]), "=&v"(w2raw[5]), "=&v"(w2raw[6]), "=&v"(w2raw[7])
               : "v"(ow[0]), "v"(ow[1]), "v"(ow[2]), "v"(ow[3]), "s"(A.wd) : "memory");
  v4u T[32];
  {
    const u32 tb = lds0 + L_TZ + ch * TZ_C + ((n16 + 1) & 3) * TZ_S + (u32)((((n16 + 1) >> 2) + 2 * q + 8 * par2) * 8);
#pragma unroll
    for (int i = 0; i < 32; ++i) {
      v2u a, c;
      asm volatile("ds_read_b64 %0, %2 offset:%3\n\tds_read_b64 %1, %2 offset:%4" : "=&v"(a), "=&v"(c) : "v"(tb), "i"(128 * i), "i"(128 * i + 8) : "memory");
      T[i] = (v4u){a.x, a.y, c.x, c.y};
    }
    asm volatile("s_waitcnt lgkmcnt(0)" : "+v"(T[0]), "+v"(T[1]), "+v"(T[2]), "+v"(T[3]), "+v"(T[4]), "+v"(T[5]), "+v"(T[6]), "+v"(T[7]), "+v"(T[8]), "+v"(T[9]), "+v"(T[10]),
                 "+v"(T[11]), "+v"(T[12]), "+v"(T[13]), "+v"(T[14]), "+v"(T[15])::"memory");
    asm volatile("" : "+v"(T[16]), "+v"(T[17]), "+v"(T[18]), "+v"(T[19]), "+v"(T[20]), "+v"(T[21]), "+v"(T[22]), "+v"(T[23]), "+v"(T[24]), "+v"(T[25]), "+v"(T[26]),
                 "+v"(T[27]), "+v"(T[28]), "+v"(T[29]), "+v"(T[30]), "+v"(T[31])::"memory");
  }
  const int tB = 512 * par2 + 16 * n16 + 4 * q;
  LAS char* xp = lds + L_XR + ch * XR_S + (2032 - 16 * n16 + 32 * par2 + 8 * q) * 2 - 3584;
  LAS char* kp = lds + L_XR + ch * XR_S + (2044 - 1280 - tB) * 2;
  LAS char* gp = lds + L_S1 + tB * 8;
  LAS char* ep = lds + L_EX + w * 4096 + l * 16;
  LAS char* er = lds + L_EX + (w ^ 1) * 4096 + l * 16;
  u32 qo = O_QT + cB * 4096 + tB * 2;
  u32 oh = O_HT + (u32)((8 * (2 * par2 + (q & 1)) + (n16 >> 1)) * 32768 + (cB >> 5) * 2048 + (cB & 31) * 64 + (((2 * (n16 & 1) + (q >> 1)) ^ (2 * ((cB >> 3) & 1))) << 4));
  asm volatile("" : "+v"(xp), "+v"(kp), "+v"(gp), "+v"(ep), "+v"(er), "+v"(qo), "+v"(oh), "+v"(l16));
  const int hp = 4 * (x >> 2) + (y >> 3), fch = 8 * (x & 3) + (y & 7);
  const int ct = y & 7, rbl = y >> 3;
  const u32 ring = L_R + w * 12288;
  const int rot = fch & 3, spar = (fch >> 2) & 1;
  u32 w2v = O_W2T + (u32)((ct * 56 + 14 * w) * 4096) + l16;
  u32 ost = (fch < 8 && rr == q) ? O_ST2 + (u32)(rot * 32768 + (128 * hp + 32 * w + 16 * spar + n16) * 16) : 0xffff0000u;
  u32 og2 = O_ST2 + (u32)((l >> 5) * 65536 + (128 * hp + 32 * w + (l & 31)) * 16);
  u32 oact = O_ACT + (u32)(((8 * hp + 2 * w) * 224 + 7 * fch + q) * 256 + n16 * 16), oact2 = q < 3 ? oact + 1024 : 0xff000000u;
  u32 of3 = O_F3 + (u32)(((x * 4 + rbl) * 4 + w) * 128 + (l & 7) * 4), of3w = l == 0 ? O_F3 + (u32)(((hp * 4 + w) * 4 + (fch >> 3)) * 128 + (fch & 7) * 4) : 0xffff0000u;
  u32 av = O_ACT + (u32)(((8 * x + 2 * rbl) * 224 + 56 * w) * 256) + l16;
  LAS char* hx[2];
#pragma unroll
  for (int pr = 0; pr < 2; ++pr) hx[pr] = lds + ring + (8 * q + rr) * 64 + (((2 * (pr ^ spar) + (p4 >> 1)) ^ (2 * (q & 1))) << 4) + (p4 & 1) * 8;
  LAS char* pcs = lds + ring + 10240 + (n16 == 0 ? q * 16 : 128 + l * 16);
  LAS char* pcr = lds + L_R + 10240 + q * 16;
  asm volatile("" : "+v"(av), "+v"(pcs), "+v"(pcr), "+v"(w2v), "+v"(ost), "+v"(og2), "+v"(oact), "+v"(oact2), "+v"(of3), "+v"(of3w), "+v"(hx[0]), "+v"(hx[1]));
  STAMP(21);
  if (w == 0) spin(O_F1 + ((x ^ (4 * (l >> 5))) * 32 + (l & 31)) * 128, tag);
  BAR();
  STAMP(3);

  dma<true>(lds0 + ch * XR_S + par2 * 2048, L_XR, l16, rWS, cB * 4096 + par2 * 2048, O_KVT);
  dma<true>(lds0 + ch * XR_S + par2 * 2048, L_XR + 1024, l16, rWS, cB * 4096 + par2 * 2048, O_KVT + 1024);
#pragma unroll
  for (int i = 0; i < 4; ++i) dma<true>(lds0 + w * 4096, L_S1 + i * 1024, l16, rWS, w * 4096, O_ST1 + i * 1024);
  v2u xq[4];
  asm volatile("buffer_load_dwordx2 %0, %4, %5, 0 offen sc1\n\tbuffer_load_dwordx2 %1, %4, %5, 0 offen offset:512 sc1\n\t"
               "buffer_load_dwordx2 %2, %4, %5, 0 offen offset:2048 sc1\n\tbuffer_load_dwordx2 %3, %4, %5, 0 offen offset:2560 sc1"
               : "=&v"(xq[0]), "=&v"(xq[1]), "=&v"(xq[2]), "=&v"(xq[3]) : "v"(qo), "s"(rWS) : "memory");
  SB();
  asm volatile("s_waitcnt vmcnt(8)" : "+v"(w2raw[0]), "+v"(w2raw[1]), "+v"(w2raw[2]), "+v"(w2raw[3]), "+v"(w2raw[4]), "+v"(w2raw[5]), "+v"(w2raw[6]), "+v"(w2raw[7])::"memory");
  BAR();
  STAMP(4);
  v4u W2[56];
  auto w2l = [&](int i) { W2[i] = __builtin_amdgcn_raw_buffer_load_b128(WS, w2v + (i & 3) * 1024, (i >> 2) * 4096, SC1); };
  u32 hpk[4];
  GAS u32* pc2 = (GAS u32*)(A.ws + O_C2 + (l & 7) * 2048 + x * 256);
  auto put_h = [&](int h) {
    const auto s0 = __builtin_amdgcn_permlane16_swap(hpk[0], hpk[2], false, false);
    const auto s1 = __builtin_amdgcn_permlane16_swap(hpk[1], hpk[3], false, false);
    __builtin_amdgcn_raw_buffer_store_b128((v4u){s0[0], s1[0], s0[1], s1[1]}, WS, oh + h * (32 * 32768), 0, SC1);
  };
  const u32 oc2 = O_C2 + x * 2048 + (l & 7) * 256, c2want = tag * 64u + 32u;
  {
    u32 w1so, flw;
    auto w1l = [&](int i) {
      if ((i & 3) == 0) asm volatile("s_add_u32 m0, %1, %2\n\ts_add_u32 %0, %3, %4\n\tbuffer_load_dwordx4 %5, %6, %0 offen sc1 lds"
                                     : "=&s"(w1so) : "s"(lds0 + w * 28672), "i"(i * 1024), "s"(fch * 114688 + w * 28672), "i"(O_W1F + i * 1024), "v"(l16), "s"(rWS) : "memory", "scc");
      else asm volatile("buffer_load_dwordx4 %0, %1, %2 offen offset:%3 sc1 lds" ::"v"(l16), "s"(rWS), "s"(w1so), "i"((i & 3) * 1024) : "memory");
    };
    const float dsc = ch ? hi2f(dpair) : lo2f(dpair);
    asm volatile("" : "+v"(pc2));
    f32x4 cacc[8], mn[2], ot[2];
    v4u B[32], g1[8], w2f[2]; v2u kr[4];
    float xf[16], ln[16], kd[16], tm[16], hh[8];
    auto fix = [&](int h) {
      u32 df = 0;
#pragma unroll
      for (int i = 4 * h; i < 4 * h + 4; ++i) df |= (g1[i].y ^ vtag) | (g1[i].w ^ vtag);
      if (__builtin_amdgcn_ballot_w64(df != 0)) {
        bool ok;
        for (u32 sp = 0; sp < SPIN_CAP; ++sp) {
          asm volatile("" ::: "memory");
          ok = true;
#pragma unroll
          for (int i = 4 * h; i < 4 * h + 4; ++i) {
            g1[i] = __builtin_amdgcn_raw_buffer_load_b128(WS, O_ST1 + (tB + 256 * ((i >> 1) & 1) + 1024 * h) * 8 + (i & 1) * 16, 0, SC1);
            ok &= (g1[i].y == tag) & (g1[i].w == tag);
          }
          if (__all(ok)) break;
        }
#pragma unroll
        for (int j = 8 * h; j < 8 * h + 8; ++j) { const u32 pay = g1[j >> 1][(j & 1) * 2]; ln[j] = (xf[j] - lo2f(pay)) * __builtin_amdgcn_rcpf(hi2f(pay)); }
      }
    };
    auto give = [&](int h) {
      if (par2) { *(LAS f32x4*)(ep + 2048 * h) = cacc[4 * h]; *(LAS f32x4*)(ep + 2048 * h + 1024) = cacc[4 * h + 1]; mn[0] = cacc[4 * h + 2]; mn[1] = cacc[4 * h + 3]; asm volatile("" : "+v"(mn[0]), "+v"(mn[1])); }
      else { *(LAS f32x4*)(ep + 2048 * h) = cacc[4 * h + 2]; *(LAS f32x4*)(ep + 2048 * h + 1024) = cacc[4 * h + 3]; mn[0] = cacc[4 * h]; mn[1] = cacc[4 * h + 1]; asm volatile("" : "+v"(mn[0]), "+v"(mn[1])); }
    };
    auto pre = [&](auto J_, auto P_) {
      constexpr int j = decltype(J_)::value, ph = decltype(P_)::value;
      const u32 pay = g1[j >> 1][(j & 1) * 2];
      if constexpr (ph == 0) { xf[j] = (j & 1) ? hi2f(xq[j >> 2][(j & 3) >> 1]) : lo2f(xq[j >> 2][(j & 3) >> 1]); asm volatile("" : "+v"(xf[j])); }
      if constexpr (ph == 1) { tm[j] = __builtin_amdgcn_rcpf(hi2f(pay)); asm volatile("" : "+v"(tm[j])); }
      if constexpr (ph == 2) { ln[j] = xf[j] - lo2f(pay); asm volatile("" : "+v"(ln[j])); }
      if constexpr (ph == 3) { ln[j] *= tm[j]; asm volatile("" : "+v"(ln[j])); }
      if constexpr (ph == 4) { const u32 kk = kr[j >> 2][1 - ((j & 3) >> 1)]; kd[j] = (j & 1) ? lo2f(kk) : hi2f(kk); asm volatile("" : "+v"(kd[j])); }
    };
    sfor<0, 32>([&](auto U_) { constexpr int u = decltype(U_)::value; if constexpr (cv_slot(u) < 0) B[u] = *(LAS v4u*)(xp + 128 * (31 - u)); });
    SB();
    sfor<0, 144>([&](auto N_) {
      constexpr int n = decltype(N_)::value, u = cv_u(n), i = u < 16 ? u : u - 16, a = cv_a0(u) + n - cv_first(u), eps = 4 * a - i + 3;
      mfb<i == 0>(cacc[a], T[eps], B[i]);
      SB();
      constexpr int ub = cv_at(n);
      if constexpr (ub >= 0) B[ub] = *(LAS v4u*)(xp + 128 * (31 - ub));
      if constexpr (n < 112 && n % 4 == 1) w1l(n / 4);
      if constexpr (n == 2 || n == 3) {
        constexpr int pp = n - 2;
#pragma unroll
        for (int d = 0; d < 4; ++d) w2f[pp][d] = __builtin_amdgcn_perm(w2raw[2 * d + 1], w2raw[2 * d], pp ? 0x07060302u : 0x05040100u);
        __builtin_amdgcn_raw_buffer_store_b128(w2f[pp], WS, w2o + pp * 1024, 0, SC1);
      }
      if constexpr (n >= 11 && n < 27 && n % 4 == 3) { constexpr int aa = (n - 11) / 4; kr[aa] = *(LAS v2u*)(kp + 2560 - 512 * (aa & 1) - 2048 * (aa >> 1)); }
      if constexpr (n >= 23 && n < 39 && n % 2 == 1) pre(IC<(n - 23) / 2>{}, IC<4>{});
      if constexpr (n == 43) STAMP(25);
      if constexpr (n == 46) STAMP(26);
      if constexpr (n == 67) STAMP(27);
      if constexpr (n == 111) STAMP(28);
      if constexpr (n == 113) STAMP(22);
      if constexpr (n == 44) give(0);
      if constexpr (n == 45) {
        asm volatile("s_waitcnt vmcnt(11) lgkmcnt(0)" : "+v"(xq[0]), "+v"(xq[1]), "+v"(xq[2]), "+v"(xq[3])::"memory");
        __builtin_amdgcn_s_barrier();
        asm volatile("" ::: "memory");
        if (w == 0 && l < 8) __hip_atomic_fetch_add(pc2 + 8192, 1u, RLX);
      }
      if constexpr (n == 46 || n == 47) { g1[2 * (n - 46)] = *(LAS v4u*)(gp + (n - 46) * 2048); g1[2 * (n - 46) + 1] = *(LAS v4u*)(gp + (n - 46) * 2048 + 16); }
      if constexpr (n == 48) { ot[0] = *(LAS f32x4*)er; ot[1] = *(LAS f32x4*)(er + 1024); }
      if constexpr (n >= 50 && n < 58) { pre(IC<n - 50>{}, IC<0>{}); pre(IC<n - 50>{}, IC<1>{}); pre(IC<n - 50>{}, IC<2>{}); if constexpr (n > 50) pre(IC<n - 51>{}, IC<3>{}); }
      if constexpr (n == 58) { pre(IC<7>{}, IC<3>{}); fix(0); }
      if constexpr (n >= 59 && n < 63) {
#pragma unroll
        for (int j = 2 * (n - 59); j < 2 * (n - 59) + 2; ++j) { hh[j] = xf[j] + ln[j] * ((kd[j] * dsc + mn[j >> 2][j & 3]) + ot[j >> 2][j & 3]); asm volatile("" : "+v"(hh[j])); }
      }
      if constexpr (n == 63 || n == 64) { hpk[2 * (n - 63)] = pk(hh[4 * (n - 63)], hh[4 * (n - 63) + 1]); hpk[2 * (n - 63) + 1] = pk(hh[4 * (n - 63) + 2], hh[4 * (n - 63) + 3]); }
      if constexpr (n == 66) put_h(0);
      if constexpr (n == 98) asm volatile("buffer_load_dword %0, %1, %2, 0 offen sc1" : "=&v"(flw) : "v"(oc2 + 32768), "s"(rWS) : "memory");
      if constexpr (n == 112) {
        asm volatile("s_waitcnt vmcnt(3)" : "+v"(flw)::"memory");
        __builtin_amdgcn_s_barrier();
        if (w == 0 && l < 8) __hip_atomic_fetch_add(pc2, 1u, RLX);
        if (!__all(flw == c2want)) spin(oc2 + 32768, c2want);
      }
      if constexpr (n > 112 && n % 2 == 1 && (n - 113) / 2 < NWE) w2l((n - 113) / 2);
      if constexpr (n >= 90 && n < 98 && n % 2 == 0) { constexpr int j = 4 + (n - 90) / 2; g1[j] = *(LAS v4u*)(gp + 8192 + ((j >> 1) & 1) * 2048 + (j & 1) * 16); }
      if constexpr (n >= 104) pre(IC<8 + (n - 104) / 5>{}, IC<(n - 104) % 5>{});
      SB();
    });
    asm volatile("s_nop 7" ::: "memory");
    SB();
    STAMP(5);
    give(1);
    fix(1);
    WAITV(NWE < 16 ? NWE : 16);
    BAR();
#pragma unroll
    for (int i = 16; i < NWE; ++i) w2l(i);
#pragma unroll
    for (int aa = 0; aa < 2; ++aa) {
      const f32x4 o = *(LAS f32x4*)(er + 2048 + aa * 1024);
#pragma unroll
      for (int i = 0; i < 4; ++i) hh[4 * aa + i] = xf[8 + 4 * aa + i] + ln[8 + 4 * aa + i] * ((kd[8 + 4 * aa + i] * dsc + mn[aa][i]) + o[i]);
      hpk[2 * aa] = pk(hh[4 * aa], hh[4 * aa + 1]); hpk[2 * aa + 1] = pk(hh[4 * aa + 2], hh[4 * aa + 3]);
    }
    asm volatile("" : "+v"(hpk[0]), "+v"(hpk[1]), "+v"(hpk[2]), "+v"(hpk[3]));
    STAMP(6);
  }

  v4u ones = {0x3f803f80u, 0x3f803f80u, 0x3f803f80u, 0x3f803f80u};
  LAS char* wp0 = lds + l * 16;
  asm volatile("" : "+v"(ones), "+v"(wp0));
  u32 sg[4];
#pragma unroll
  for (int g = 0; g < 4; ++g) sg[g] = (u32)((g + rot) & 3);
  LAS char* wp = wp0 + sg[0] * 28672;
  v4u wf[WD + 1], hf[2][2], g2[2];
#pragma unroll
  for (int i = 0; i < WD; ++i) wf[i] = *(LAS v4u*)(wp + i * 1024);
  STAMP(7);
  if (w == 0) spin(oc2, c2want);
  BAR();
  STAMP(8);
  f32x4 cs[7];
  u32 fl3 = 0, fl2 = 0;
  v4u AC[28][2];
  const u32 ao[2] = {(u32)((w & 1) * (224 * 256)), (u32)(((w & 1) ^ 1) * (224 * 256))};
  auto ald = [&](int u) {
#pragma unroll
    for (int c = 0; c < 2; ++c) { AC[u][c] = __builtin_amdgcn_raw_buffer_load_b128(WS, av + (u % 14 & 3) * 1024, ao[c] + (u % 14 >> 2) * 4096 + (u / 14) * (64 * 224 * 256), SC1); SB(); }
  };
  {
    u32 hso;
    auto hdma = [&](int g, int n) {
      if (n == 0) asm volatile("s_add_u32 m0, %1, %2\n\ts_add_u32 %0, %3, %4\n\tbuffer_load_dwordx4 %5, %6, %0 offen sc1 lds"
                               : "=&s"(hso) : "s"(lds0 + ring), "i"((g % 5) * 2048), "s"((4 * hp + w) * 32768 + sg[(g >> 2) & 3] * 8192), "i"(O_HT + (g >> 4) * (32 * 32768) + (g & 3) * 2048), "v"(l16), "s"(rWS) : "memory", "scc");
      else asm volatile("buffer_load_dwordx4 %0, %1, %2 offen offset:1024 sc1 lds" ::"v"(l16), "s"(rWS), "s"(hso) : "memory");
    };
    auto hfrag = [&](int g, int pr, int t) { return trd(hx[pr], (g % 5) * 2048 + t * 256); };
    f32x4 acc[7][2], sS, sQ;
    auto epilogue = [&](auto P_) {
      constexpr int ps = decltype(P_)::value;
      if (!__all((g2[0].y == tag) & (g2[0].w == tag) & (g2[1].y == tag) & (g2[1].w == tag))) {
        for (u32 sp = 0; sp < SPIN_CAP; ++sp) {
          asm volatile("" ::: "memory");
#pragma unroll
          for (int k = 0; k < 2; ++k) g2[k] = __builtin_amdgcn_raw_buffer_load_b128(WS, og2, k * 32768 + ps * 16384, SC1);
          if (__all((g2[0].y == tag) & (g2[0].w == tag) & (g2[1].y == tag) & (g2[1].w == tag))) break;
        }
      }
      const u32 s1 = f2u(u2f(g2[0].x) + u2f(g2[1].x)), s2 = f2u(u2f(g2[0].z) + u2f(g2[1].z));
      const auto h1 = __builtin_amdgcn_permlane32_swap(s1, s1, false, false), h2 = __builtin_amdgcn_permlane32_swap(s2, s2, false, false);
      const float mean = (u2f(h1[0]) + u2f(h1[1])) * (1.f / D);
      const float v2 = __builtin_fmaxf((u2f(h2[0]) + u2f(h2[1])) * (1.f / D) - mean * mean, 0.f) + 1e-5f;
      const u32 e0 = f2u(-mean), e1 = f2u(-1.44269504089f * __builtin_amdgcn_rsqf(v2)), e2 = f2u(v2);
      const auto b0 = __builtin_amdgcn_permlane16_swap(e0, e0, false, false), b1 = __builtin_amdgcn_permlane16_swap(e1, e1, false, false),
                 b2 = __builtin_amdgcn_permlane16_swap(e2, e2, false, false);
      v4u sa[2], sb[2];
#pragma unroll
      for (int pr = 0; pr < 2; ++pr) {
        const bool up = (pr ^ spar) != 0;
        const float nm = u2f(up ? b0[1] : b0[0]), k1 = u2f(up ? b1[1] : b1[0]), vv = u2f(up ? b2[1] : b2[0]);
        u32 d[8];
        f32x2 a[7], bb[7], e[7];
#pragma unroll
        for (int t = 0; t < 7; ++t) a[t] = (f32x2){acc[t][pr][0], acc[t][pr][1]} + nm * (f32x2){cs[t][0], cs[t][1]};
        SB();
#pragma unroll
        for (int t = 0; t < 7; ++t) e[t] = a[t] * k1;
        SB();
#pragma unroll
        for (int t = 0; t < 7; ++t) {
          e[t] = (f32x2){__builtin_amdgcn_exp2f(e[t][0]), __builtin_amdgcn_exp2f(e[t][1])};
          bb[t] = (f32x2){acc[t][pr][2], acc[t][pr][3]} + nm * (f32x2){cs[t][2], cs[t][3]};
          if (ps == 0 && 7 * (1 - pr) + t < W2_EPI) w2l(w2_first(16) - W2_EPI + 7 * (1 - pr) + t);
          if (ps == 1 && t < 3) ald(8 + 3 * pr + t);
          SB();
        }
#pragma unroll
        for (int t = 0; t < 7; ++t) e[t] = e[t] * vv + vv;
        SB();
#pragma unroll
        for (int t = 0; t < 7; ++t) { e[t] = (f32x2){__builtin_amdgcn_rcpf(e[t][0]), __builtin_amdgcn_rcpf(e[t][1])}; a[t] *= bb[t]; SB(); }
#pragma unroll
        for (int t = 0; t < 7; ++t) a[t] *= e[t];
        SB();
#pragma unroll
        for (int t = 0; t < 7; ++t) d[t] = pk(a[t][0], a[t][1]);
        sa[pr] = (v4u){d[0], d[1], d[2], d[3]};
        const auto x0 = __builtin_amdgcn_permlane32_swap(d[4], d[6], false, false), x1 = __builtin_amdgcn_permlane32_swap(d[5], d[5], false, false);
        const auto y0 = __builtin_amdgcn_permlane16_swap(x0[0], x1[0], false, false), y1 = __builtin_amdgcn_permlane16_swap(x0[1], x1[1], false, false);
        sb[pr] = (v4u){y0[0], y0[1], y1[0], y1[1]};
        asm volatile("" : "+v"(sa[pr]), "+v"(sb[pr]));
        SB();
      }
#pragma unroll
      for (int pr = 0; pr < 2; ++pr) {
        const u32 ro = (u32)((pr ^ spar) * (224 * 256) + ps * (64 * 224 * 256));
        __builtin_amdgcn_raw_buffer_store_b128(sa[pr], WS, oact + ro, 0, SC1);
        __builtin_amdgcn_raw_buffer_store_b128(sb[pr], WS, oact2 + ro, 0, SC1);
      }
      SB();
    };
    hdma(0, 0); hdma(0, 1); hdma(1, 0); hdma(1, 1);
    {
      LAS char* c0 = wp0 + w * 1024;
      LAS char* c1 = wp0 + (w == 3 ? 3 : w + 4) * 1024;
      LAS char* c2 = c0 + 57344;
      LAS char* c3 = c1 + 57344;
      asm volatile("" : "+v"(c0), "+v"(c1), "+v"(c2), "+v"(c3));
      auto crd = [&](int k, int hi) { return *(LAS v4u*)((k < 8 ? (hi ? c1 : c0) : (hi ? c3 : c2)) + (k & 7) * 7168); };
      v4u fa[16], fb[16]; f32x4 ca, cb;
#pragma unroll
      for (int k = 0; k < 4; ++k) { fa[k] = crd(k, 0); fb[k] = crd(k, 1); }
      SB();
      sfor<0, 16>([&](auto K_) {
        constexpr int k = decltype(K_)::value;
        mfv<k == 0>(ca, fa[k], ones); SB();
        if constexpr (k + 4 < 16) fa[k + 4] = crd(k + 4, 0);
        SB();
        mfv<k == 0>(cb, fb[k], ones); SB();
        if constexpr (k + 4 < 16) fb[k + 4] = crd(k + 4, 1);
        if constexpr (k >= 2 && k < 8) hdma(2 + (k - 2) / 2, k & 1);
        if constexpr (k == 12) put_h(1);
        SB();
      });
      WAITV(9);
#pragma unroll
      for (int pr = 0; pr < 2; ++pr) { const v2u a = hfrag(0, pr, 0), c = hfrag(0, pr, 1); hf[0][pr] = (v4u){a.x, a.y, c.x, c.y}; }
      asm volatile("s_nop 7" ::: "memory");
      *(LAS f32x4*)pcs = ca; *(LAS f32x4*)(pcs + 64) = cb;
    }
    SB();
    STAMP(20);
    sfor<0, 32>([&](auto G_) {
      constexpr int g = decltype(G_)::value, s = g & 15;
      v4u(&hc)[2] = hf[g & 1]; v4u(&hn)[2] = hf[(g + 1) & 1];
      LAS char* wn = wp;
      if constexpr ((g & 3) == 3 && g < 31) wn = wp0 + sg[((g + 1) >> 2) & 3] * 28672;
      v2u ha;
      sfor<0, 7>([&](auto T_) {
        constexpr int t = decltype(T_)::value, it = g * 7 + t, nx = it + WD, ng = nx / 7, nt = nx % 7;
        if constexpr (t == 0) asm volatile("" : "+v"(wf[it % 8]), "+v"(wf[(it + 1) % 8]), "+v"(wf[(it + 2) % 8]), "+v"(wf[(it + 3) % 8]), "+v"(hc[0]), "+v"(hc[1]));
        if constexpr (t == 4) asm volatile("" : "+v"(wf[it % 8]), "+v"(wf[(it + 1) % 8]), "+v"(wf[(it + 2) % 8]));
        mfv<s == 0>(acc[t][0], wf[it % (WD + 1)], hc[0]);
        SB();
        if constexpr (nx < 224) wf[nx % (WD + 1)] = *(LAS v4u*)((ng > g ? wn : wp) + (ng & 3) * 7168 + nt * 1024);
        SB();
        mfv<s == 0>(acc[t][1], wf[it % (WD + 1)], hc[1]);
        SB();
        if constexpr (s < 15 && t < 4) {
          if constexpr (t == 0) { constexpr int hw = h_wait(g); WAITV(hw); }
          if constexpr (t & 1) { const v2u hb = hfrag(g + 1, t >> 1, 1); hn[t >> 1] = (v4u){ha.x, ha.y, hb.x, hb.y}; } else ha = hfrag(g + 1, t >> 1, 0);
        }
        if constexpr (t >= 4 && t - 4 < w2_n(g)) w2l(w2_first(g) + t - 4);
        if constexpr (s < 11 && t >= 5) hdma(g + 5, t - 5);
        if constexpr (g == 28 && t == 4) {
          constexpr int nw = vm_sum(26, 28);
          asm volatile("s_waitcnt vmcnt(%1)" : "+v"(fl3) : "n"(nw) : "memory");
          if (!__all(fl3 == tag)) spin(of3, tag);
        }
        if constexpr (g >= 28 && (t == 4 || t == 6)) ald(2 * (g - 28) + (t - 4) / 2);
        if constexpr (g == 5 || g == 6) { if constexpr (t >= 3 && 4 * (g - 5) + t - 3 < 7) { constexpr int k = 4 * (g - 5) + t - 3; cs[k] = *(LAS f32x4*)(pcr + (k & 3) * 12288 + (k >> 2) * 64); } }
        SB();
      });
      if constexpr (s < 4) { mfv<s == 0>(sS, ones, hc[0]); mfv<s == 0>(sQ, hc[0], hc[0]); }
      if constexpr (g == 4) {
        constexpr int nw = vm_sum(0, 4) + w2_n(4) + 2;
        WAITV(nw);
        __builtin_amdgcn_s_barrier();
        asm volatile("" ::: "memory");
        if (w == 0) { if (l < 8) __hip_atomic_fetch_add(pc2 + 4096, 1u, RLX); }
        else __builtin_amdgcn_raw_buffer_store_b32(vtag, WS, 0xffffff00u, 0, SC1);
      }
      if constexpr (g == 13) asm volatile("buffer_load_dword %0, %1, %2, 0 offen sc1" : "=&v"(fl2) : "v"(oc2 + 16384), "s"(rWS) : "memory");
      if constexpr (s == 5) {
        float q01, q23, qq;
        asm volatile("v_cndmask_b32 %0, %3, %4, %7\n\tv_cndmask_b32 %1, %5, %6, %7\n\tv_cndmask_b32 %2, %0, %1, %8"
                     : "=&v"(q01), "=&v"(q23), "=&v"(qq) : "v"(sQ[0]), "v"(sQ[1]), "v"(sQ[2]), "v"(sQ[3]), "s"(0xaaaaaaaaaaaaaaaaull), "s"(0xccccccccccccccccull));
        __builtin_amdgcn_raw_buffer_store_b128((v4u){f2u(sS[0]), vtag, f2u(qq), vtag}, WS, ost + (g >> 4) * 16384, 0, SC1);
      }
      if constexpr (s == 12) {
        asm volatile("buffer_load_dwordx4 %0, %2, %3, %4 offen sc1\n\tbuffer_load_dwordx4 %1, %2, %3, %5 offen sc1" : "=&v"(g2[0]), "=&v"(g2[1])
                     : "v"(og2), "s"(rWS), "s"((g >> 4) * 16384), "s"(32768 + (g >> 4) * 16384) : "memory");
      }
      if constexpr (g == 19) {
        constexpr int nw = vm_sum(16, 19) + w2_n(19) + 2;
        WAITV(nw);
        __builtin_amdgcn_raw_buffer_store_b32(vtag, WS, of3w, 0, SC1);
      }
      if constexpr (g == 27) {
        dma<true>(lds0 + ring, 4096, l16, rWS, (32 * (w >> 1) + 4 * x + rbl) * 32768 + (2 * ct + (w & 1)) * 2048, O_HT);
        dma<true>(lds0 + ring, 5120, l16, rWS, (32 * (w >> 1) + 4 * x + rbl) * 32768 + (2 * ct + (w & 1)) * 2048, O_HT + 1024);
      }
      if constexpr (g == 25) asm volatile("buffer_load_dword %0, %1, %2, 0 offen sc1" : "=&v"(fl3) : "v"(of3), "s"(rWS) : "memory");
      SB();
      wp = wn;
      if constexpr ((g & 7) == 7) STAMP(16 + (g >> 3));
      if constexpr (s == 15) {
        constexpr int nw = g == 15 ? vm_sum(14, 16) : vm_sum(29, 32);
        asm volatile("s_waitcnt vmcnt(%3)\n\ts_nop 7" : "+v"(g2[0]), "+v"(g2[1]), "+v"(fl2) : "n"(nw) : "memory");
        SB();
        if constexpr (g == 15) {
          if (!__all(fl2 == c2want)) spin(oc2 + 16384, c2want);
#pragma unroll
          for (int k = 16; k < 21; ++k) { hdma(k, 0); hdma(k, 1); }
        }
        epilogue(IC<(g >> 4)>{});
        if constexpr (g == 15) {
          WAITV(8 + vm_pre(1));
#pragma unroll
          for (int pr = 0; pr < 2; ++pr) { const v2u a = hfrag(16, pr, 0), c = hfrag(16, pr, 1); hf[0][pr] = (v4u){a.x, a.y, c.x, c.y}; }
          SB();
        }
        if constexpr (g == 15) STAMP(9);
      }
    });
  }
  STAMP(10);

  {
    __builtin_amdgcn_s_barrier();
    asm volatile("" ::: "memory");
    v2u hr[4];
#pragma unroll
    for (int i = 0; i < 4; ++i)
      hr[i] = trd(lds, L_R + (u32)(((w & 2) + (i >> 1)) * 12288 + 4096 + (8 * q + 4 * (i & 1) + rr) * 64 + (((2 * (w & 1) + (p4 >> 1)) ^ (2 * (q & 1))) << 4) + (p4 & 1) * 8));
    LAS char* pw = lds + w * 4096 + l * 16;
    u32 f3b;
    f32x4 o[4][2], mine[4];
    SB();
    sfor<0, 28>([&](auto U_) {
      constexpr int u = decltype(U_)::value;
#pragma unroll
      for (int c = 0; c < 2; ++c)
#pragma unroll
        for (int pp = 0; pp < 4; ++pp) mfa<u % 14 == 0>(o[pp][c], W2[4 * (u % 14) + pp], AC[u][c]);
      SB();
      if constexpr (u == 5) {
        WAITV(0);
        __builtin_amdgcn_raw_buffer_store_b32(vtag, WS, of3w + 16384, 0, SC1);
        SB();
      }
      if constexpr (u == UPOLL) { f3b = __builtin_amdgcn_raw_buffer_load_b32(WS, of3 + 16384, 0, SC1); SB(); }
      if constexpr (u == UCHK) {
        if (!__all(f3b == tag)) spin(of3 + 16384, tag);
        STAMP(12);
#pragma unroll
        for (int v = 14; v < 20; ++v) ald(v);
      }
      if constexpr (u > UCHK && u <= UCHK + 8) ald(u - UCHK + 19);
      if constexpr (u % 14 == 13) {
        asm volatile("s_nop 7" ::: "memory");
        SB();
        constexpr int hfb = 2 * (u / 14);
#pragma unroll
        for (int pp = 0; pp < 4; ++pp) *(LAS f32x4*)(pw + ((hfb + ((w & 1) ^ 1)) * 16384) + pp * 1024) = o[pp][1];
        if ((w & 2) == hfb) {
#pragma unroll
          for (int pp = 0; pp < 4; ++pp) { mine[pp] = o[pp][0]; asm volatile("" : "+v"(mine[pp])); }
        } else {
#pragma unroll
          for (int pp = 0; pp < 4; ++pp) *(LAS f32x4*)(pw + ((hfb + (w & 1)) * 16384) + pp * 1024) = o[pp][0];
        }
        if constexpr (u == 13) STAMP(11);
        SB();
      }
    });
    STAMP(13);
    BAR();
#pragma unroll
    for (int j = 1; j < 4; ++j)
#pragma unroll
      for (int pp = 0; pp < 4; ++pp) mine[pp] += *(LAS f32x4*)(lds + w * 16384 + ((w + j) & 3) * 4096 + pp * 1024 + l * 16);
    u32 r[8];
#pragma unroll
    for (int i = 0; i < 4; ++i) {
      r[2 * i] = pk(lo2f(hr[i].x) + mine[0][i], hi2f(hr[i].x) + mine[1][i]);
      r[2 * i + 1] = pk(lo2f(hr[i].y) + mine[2][i], hi2f(hr[i].y) + mine[3][i]);
    }
    u16* op = A.out + (1024 * (w >> 1) + 128 * x + 32 * rbl + 16 * (w & 1) + n16) * D + 64 * ct + 8 * q;
    *(v4u*)op = (v4u){r[0], r[1], r[2], r[3]};
    *(v4u*)(op + 32) = (v4u){r[4], r[5], r[6], r[7]};
    if (b == 0 && w == 0 && l == 0) __builtin_amdgcn_raw_buffer_store_b32(tag, WS, O_EP, 0, SC1);
  }
}
}

extern "C" void kernel_launch(void* const* d_in, const int* in_sizes, int n_in, void* d_out, int out_size, void* d_ws, size_t ws_size, hipStream_t stream) {
  fk::Args a;
  a.q = (const fk::u16*)d_in[0]; a.k = (const fk::u16*)d_in[1]; a.v = (const fk::u16*)d_in[2]; a.shk = (const fk::u16*)d_in[3];
  a.ssk = (const fk::u16*)d_in[4]; a.ssd = (const fk::u16*)d_in[5]; a.wg = (const fk::u16*)d_in[7]; a.wu = (const fk::u16*)d_in[8];
  a.wd = (const fk::u16*)d_in[9]; a.out = (fk::u16*)d_out; a.ws = (char*)d_ws;
  fk::fused<<<256, 256, 0, stream>>>(a);
}
```

```cpp
#include <hip/hip_runtime.h>
#include <stdint.h>
#define NWE 20
#define PFM 0
#define UPOLL 8
#define UCHK 11

namespace fk {
constexpr int WD = 7;
typedef unsigned u32;
typedef unsigned short u16;
typedef unsigned long long u64;
typedef u32 v4u __attribute__((ext_vector_type(4)));
typedef u32 v2u __attribute__((ext_vector_type(2)));
typedef float f32x4 __attribute__((ext_vector_type(4)));
typedef float f32x2 __attribute__((ext_vector_type(2)));
typedef short s16x4 __attribute__((ext_vector_type(4)));
typedef short s16x8 __attribute__((ext_vector_type(8)));
typedef __bf16 bf2 __attribute__((ext_vector_type(2)));
#define LAS __attribute__((address_space(3)))
#define GAS __attribute__((address_space(1)))
#define RLX __ATOMIC_RELAXED, __HIP_MEMORY_SCOPE_AGENT
#define SB() __builtin_amdgcn_sched_barrier(0)
#define WAITV(n) asm volatile("s_waitcnt vmcnt(%0)" ::"n"(n) : "memory")
#define BAR() do { asm volatile("s_waitcnt lgkmcnt(0)" ::: "memory"); __builtin_amdgcn_s_barrier(); asm volatile("" ::: "memory"); } while (0)

constexpr int S = 2048, D = 512, F = 1792;
constexpr u32 O_EP = 0, O_C2 = 16384, O_ST1 = 65536, O_F1 = 98304, O_ST2 = 131072, O_F3 = 262144;
constexpr u32 O_KVT = 1u << 20, O_QT = 3u << 20, O_HT = 5u << 20, O_W2T = 7u << 20, O_ACT = 9u << 20, O_W1F = 17u << 20, WS_BYTES = 21u << 20;
constexpr u32 O_DIAG = 24u << 20;
constexpr u32 L_R = 114688, L_K = L_R, L_V = L_R + 9216, L_Q = L_R + 17408, L_WR = 0, WR_S = 1056, L_TZ = 36864, TZ_S = 4160, TZ_C = 4 * TZ_S, L_XR = L_R + 33280, XR_S = 4608,
              L_S1 = L_R, L_EX = L_R + 16384, LDS_BYTES = 163840;
constexpr u32 SPIN_CAP = 1u << 18;
constexpr int SC1 = 16;

struct Args { const u16 *q, *k, *v, *shk, *ssk, *ssd, *wg, *wu, *wd; u16* out; char* ws; };

__device__ __forceinline__ u32 f2u(float x) { return __builtin_bit_cast(u32, x); }
__device__ __forceinline__ float u2f(u32 x) { return __builtin_bit_cast(float, x); }
__device__ __forceinline__ float lo2f(u32 x) { return u2f(x << 16); }
__device__ __forceinline__ float hi2f(u32 x) { return u2f(x & 0xffff0000u); }
__device__ __forceinline__ u32 pk(float lo, float hi) { bf2 r = {(__bf16)lo, (__bf16)hi}; return __builtin_bit_cast(u32, r); }
__device__ __forceinline__ float rbf(float x) { return lo2f(pk(x, x)); }
__device__ __forceinline__ f32x4 mfma(v4u a, v4u b, f32x4 c) {
  return __builtin_amdgcn_mfma_f32_16x16x32_bf16(__builtin_bit_cast(s16x8, a), __builtin_bit_cast(s16x8, b), c, 0, 0, 0);
}
template <bool FIRST> __device__ __forceinline__ void mfv(f32x4& c, v4u a, v4u b) {
  if (FIRST) asm volatile("v_mfma_f32_16x16x32_bf16 %0, %1, %2, 0" : "=&v"(c) : "v"(a), "v"(b));
  else asm volatile("v_mfma_f32_16x16x32_bf16 %0, %1, %2, %0" : "+v"(c) : "v"(a), "v"(b));
}
template <bool FIRST> __device__ __forceinline__ void mfa(f32x4& c, v4u a, v4u b) {
  if (FIRST) asm volatile("v_mfma_f32_16x16x32_bf16 %0, %1, %2, 0" : "=&v"(c) : "a"(a), "v"(b));
  else asm volatile("v_mfma_f32_16x16x32_bf16 %0, %1, %2, %0" : "+v"(c) : "a"(a), "v"(b));
}
template <bool FIRST> __device__ __forceinline__ void mfb(f32x4& c, v4u a, v4u b) {
  if (FIRST) asm volatile("v_mfma_f32_16x16x32_bf16 %0, %1, %2, 0" : "=&v"(c) : "v"(a), "a"(b));
  else asm volatile("v_mfma_f32_16x16x32_bf16 %0, %1, %2, %0" : "+v"(c) : "v"(a), "a"(b));
}
__device__ __forceinline__ v2u trd(LAS char* lds, u32 off) {
  return __builtin_bit_cast(v2u, __builtin_amdgcn_ds_read_tr16_b64_v4i16((LAS s16x4*)(lds + off)));
}
__device__ __forceinline__ v4u mkrs(const void* p, u32 bytes) { u64 a = (u64)p; return (v4u){(u32)a, (u32)(a >> 32) & 0xffffu, bytes, 0x00020000u}; }
template <bool COH> __device__ __forceinline__ void dma(u32 lb, u32 lo, u32 voff, v4u rs, u32 sb, u32 so) {
  u32 t;
  if (COH) asm volatile("s_add_u32 m0, %1, %6\n\ts_add_u32 %0, %4, %5\n\tbuffer_load_dwordx4 %2, %3, %0 offen sc1 lds" : "=&s"(t) : "s"(lb), "v"(voff), "s"(rs), "s"(sb), "i"(so), "i"(lo) : "memory", "scc");
  else asm volatile("s_add_u32 m0, %1, %6\n\ts_add_u32 %0, %4, %5\n\tbuffer_load_dwordx4 %2, %3, %0 offen lds" : "=&s"(t) : "s"(lb), "v"(voff), "s"(rs), "s"(sb), "i"(so), "i"(lo) : "memory", "scc");
}
template <int CTRL, int RM> __device__ __forceinline__ float dppadd(float v) {
  return v + __builtin_bit_cast(float, __builtin_amdgcn_update_dpp(0, __builtin_bit_cast(int, v), CTRL, RM, 0xf, true));
}
__device__ __forceinline__ float hsum(float v) {
  v = dppadd<0x111, 0xf>(v); v = dppadd<0x112, 0xf>(v); v = dppadd<0x114, 0xf>(v); v = dppadd<0x118, 0xf>(v);
  return dppadd<0x142, 0xa>(v);
}
#define STAMP(i)
template <int V> struct IC { static constexpr int value = V; };
template <int B, int E, class Fn> __device__ __forceinline__ void sfor(Fn&& f) { if constexpr (B < E) { f(IC<B>{}); sfor<B + 1, E>(f); } }
constexpr int cv_a0(int u) { return u < 16 ? u >> 2 : ((u - 16) >> 2) > 4 ? (u - 16) >> 2 : 4; }
constexpr int cv_first(int u) { int n = 0; for (int j = 0; j < u; ++j) n += (j < 16 ? 4 : 8) - cv_a0(j); return n; }
constexpr int cv_u(int n) { int u = 0; while (cv_first(u + 1) <= n) ++u; return u; }
constexpr int cv_slot(int i) { int s_ = cv_first(i < 16 ? i : i + 16) - 12; if (i < 31) { const int v = cv_slot(i + 1) - 2; if (v < s_) s_ = v; } return s_ & ~1; }
constexpr int cv_at(int n) { for (int i = 0; i < 32; ++i) if (cv_slot(i) == n) return i; return -1; }
constexpr int w2_n(int g) { return g >= 27 ? 0 : (g & 15) < 11 ? ((g & 1) ^ 1) : 3; }
constexpr int W2_EPI = 56 - 27 - NWE;
constexpr int w2_first(int g) { int n = NWE + (g > 15 ? W2_EPI : 0); for (int j = 0; j < g; ++j) n += w2_n(j); return n; }
constexpr int a_n(int g) { return g >= 28 ? 4 : 0; }
constexpr int vm_tail(int g) { return (g == 4) + ((g & 15) == 5) + 2 * ((g & 15) == 12) + (g == 13) + (g == 19) + (g == 25) + 2 * (g == 27); }
constexpr int vm_ops(int g) { return w2_n(g) + a_n(g) + ((g & 15) < 11 ? 2 : 0) + vm_tail(g); }
constexpr int vm_sum(int a, int e) { int n = 0; for (int j = a; j < e; ++j) n += vm_ops(j); return n; }
constexpr int vm_pre(int ps) { return ps ? W2_EPI + 4 : 1; }
constexpr int h_wait(int g) {
  return (g & 15) < 4 ? 2 * (3 - (g & 15)) + vm_pre(g >> 4) + vm_sum(g & 16, g) : vm_tail(g - 4) + vm_sum(g - 3, g);
}
static_assert(w2_first(32) == 56 && W2_EPI >= 0 && W2_EPI <= 14, "all w_down fragments are requested");

__global__ void __launch_bounds__(256, 1) fused(Args A) {
  __shared__ __attribute__((aligned(16))) char smem_[LDS_BYTES];
  LAS char* lds = (LAS char*)smem_;
  const u32 lds0 = (u32)(uintptr_t)lds;
  STAMP(0);
  const int b = blockIdx.x, x = b & 7, y = b >> 3;
  const int l = threadIdx.x & 63, w = __builtin_amdgcn_readfirstlane(threadIdx.x >> 6);
  const int n16 = l & 15, q = l >> 4, rr = (l >> 2) & 3, p4 = l & 3;
  u32 pfd;
  {
    u64 pc;
    asm volatile("s_getpc_b64 %0" : "=s"(pc));
    constexpr u32 CODE = 36864;
    const u32 co = PFM == 0 ? (u32)((w == 0 ? 0 : 1 + (y + w) % 4) * 8192 + l * 128) : l < 48 ? (u32)(l * 128) : (u32)(6144 + (((y & 7) * 4 + w) * 8 + (l & 7)) * 128);
    asm volatile("global_load_dword %0, %1, %2" : "=&v"(pfd) : "v"(co < CODE ? co : 0u), "s"(pc & ~3ull) : "memory");
  }
  const __amdgpu_buffer_rsrc_t WS = __builtin_amdgcn_make_buffer_rsrc(A.ws, 0, WS_BYTES, 0x00020000);
  const v4u rWS = mkrs(A.ws, WS_BYTES);
  auto spin = [&](u32 off, u32 want) {
    for (u32 sp = 0; sp < SPIN_CAP; ++sp) {
      asm volatile("" ::: "memory");
      if (__all(__builtin_amdgcn_raw_buffer_load_b32(WS, off, 0, SC1) == want)) break;
    }
  };

  const int ti = 4 * x + (y >> 3), cj = y & 7;
  const int cA = 64 * cj + 16 * w + n16;
  const int srow = 256 * x + 8 * y + 2 * w;
  const int ch = w >> 1, par2 = w & 1, cB = 64 * x + 2 * y + ch;
  v2u shk; v4u sx0, sx1, kz[2];
  v2u kedge; u32 vtag, vdp;
  u32 l16 = l * 16;
  {
    const v4u rK = mkrs(A.k, S * D * 2), rV = mkrs(A.v, S * D * 2), rQ = mkrs(A.q, S * D * 2);
    const u32 c0 = ((l & 7) ^ ((l >> 3) & 2)) * 16, tv = (u32)((64 * ti + (l >> 3)) * 1024 + 128 * cj);
    const u32 va = tv + c0, vb = tv + (c0 ^ 64);
    const u32 vk = ((w & 1) ? vb : va) + (u32)(w * 8192 - 8192);
    dma<false>(lds0 + w * 1024, L_K, vk, rK, 0, 0);
    dma<false>(lds0 + w * 1024, L_K + 4096, vk + 32768, rK, 0, 0);
    if (w == 0) dma<false>(lds0, L_K + 8192, vk + 65536, rK, 0, 0);
    dma<false>(lds0 + w * 2048, L_V, va, rV, w * 16384, 0);
    dma<false>(lds0 + w * 2048, L_V + 1024, vb, rV, w * 16384, 8192);
    asm volatile("global_load_dwordx2 %0, %1, %2" : "=&v"(shk) : "v"(cA * 8), "s"(A.shk) : "memory");
    asm volatile("global_load_dwordx4 %0, %2, %3\n\tglobal_load_dwordx4 %1, %2, %3 offset:1024" : "=&v"(kz[0]), "=&v"(kz[1]) : "v"(l16), "s"(A.ssk + cB * S + 1024 * par2) : "memory");
    asm volatile("global_load_dwordx2 %0, %3, %4 offset:2048\n\tglobal_load_dword %1, %3, %5\n\tglobal_load_dword %2, %3, %6"
                 : "=&v"(kedge), "=&v"(vtag), "=&v"(vdp) : "v"(0), "s"(A.ssk + cB * S), "s"(A.ws + O_EP), "s"(A.ssd + 64 * x + 2 * y) : "memory");
    dma<false>(lds0 + w * 2048, L_Q, va, rQ, w * 16384, 0);
    dma<false>(lds0 + w * 2048, L_Q + 1024, vb, rQ, w * 16384, 8192);
    asm volatile("global_load_dwordx4 %0, %2, %3\n\tglobal_load_dwordx4 %1, %2, %3 offset:512" : "=&v"(sx0), "=&v"(sx1) : "v"((l >> 5) * 1024 + (l & 31) * 16), "s"(A.q + srow * D) : "memory");
  }
  const int rb = 8 * x + 2 * (y >> 3) + ((y >> 2) & 1), fg = y & 3;
  const v4u rW = mkrs(ch ? A.wu : A.wg, D * F * 2);
  u32 wrv = l < 56 ? l16 : 0xffffff00u, w1d[4];
  LAS char* pW = lds + L_WR + ((p4 >> 1) * 8 + rr) * WR_S + (4 * w + q) * 16 + (p4 & 1) * 8;
#pragma unroll
  for (int j = 0; j < 4; ++j) {
    int Z = 16 * j + 4 * w + q;
    asm volatile("" : "+v"(Z));
    const int c = (Z * 37) >> 8;
    w1d[j] = (j < 3 || Z < 56) ? O_W1F + (u32)(((8 * fg + c) * 16 + (rb >> 2)) * 7168 + (Z - 7 * c) * 1024 + (rb & 3) * 256 + (4 * ((n16 & 7) >> 1) + 2 * (n16 >> 3) + (n16 & 1)) * 16) : 0xffffff00u;
  }
  const u32 aAo = (u32)((8 * q + rr) * 128 + (((2 * w + (p4 >> 1)) ^ (rr & 2) ^ ((q & 1) << 2)) << 4) + (p4 & 1) * 8);
  LAS char* pA = lds + L_K + aAo;
  LAS char* pB = lds + L_K + (aAo ^ 64);
  u32 okv = O_KVT + cA * 4096 + (2040 - 32 - 64 * ti - 8 * q) * 2, oqt = O_QT + cA * 4096 + (64 * ti + 8 * q) * 2;
  LAS char* tw = lds + L_TZ + ch * TZ_C + (128 * par2 + l) * 16 + 32;
  u32 og = (l & 31) == 31 ? O_ST1 + (srow + (l >> 5)) * 8 : 0xffffff00u;
  u32 oci = (x == 0 && y < 8 && w == 0 && l < 24) ? O_C2 + l * 2048 + y * 256 : 0xffffff00u;
  asm volatile("" : "+v"(w1d[0]), "+v"(w1d[1]), "+v"(w1d[2]), "+v"(w1d[3]), "+v"(pA), "+v"(pB), "+v"(okv), "+v"(oqt), "+v"(tw), "+v"(og), "+v"(wrv), "+v"(pW), "+v"(oci));
  const int wU = b < 224 ? 2 * b + ch : 0;
  u32 ow[4], w2o = b < 224 ? O_W2T + (u32)(((wU & 7) * 56 + (wU >> 3)) * 4096 + par2 * 2048) + l16 : 0xffff0000u;
  {
    const int UU = 4 * (wU >> 3) + q, f = UU / 7, u = UU - 7 * f;
    const int col = 64 * (wU & 7) + 8 * rr + 4 * (p4 & 1) + 32 * (p4 >> 1) + 2 * par2;
#pragma unroll
    for (int d = 0; d < 4; ++d) ow[d] = (u32)(((56 * f + (u < 4 ? 2 * u + 8 * d : 8 * u + 2 * d)) * D + col) * 2);
  }
  asm volatile("" : "+v"(w2o), "+v"(ow[0]), "+v"(ow[1]), "+v"(ow[2]), "+v"(ow[3]));
  if (w == 0) *(LAS v4u*)(lds + L_XR + (l >> 5) * XR_S + 4096 + (l & 31) * 16) = (v4u){0, 0, 0, 0};
  SB();
  STAMP(23);
  asm volatile("s_waitcnt vmcnt(9)" : "+v"(shk), "+v"(pfd)::"memory");
  BAR();
  STAMP(1);
  u32 tag, dpair;
  {
    v2u ka[2], kb[2], kc[2], va[2], vb[2];
#pragma unroll
    for (int it = 0; it < 2; ++it) { ka[it] = trd(pA, it * 4096 + 512); kb[it] = trd(pB, it * 4096 + 1024); kc[it] = trd(pB, it * 4096 + 1536); }
#pragma unroll
    for (int it = 0; it < 2; ++it) { va[it] = trd(pA, L_V - L_K + it * 4096); vb[it] = trd(pA, L_V - L_K + it * 4096 + 512); }
    SB();
#pragma unroll
    for (int j = 0; j < 4; ++j) dma<false>(lds0 + (8 * ch + 4 * par2) * WR_S, L_WR + j * WR_S, wrv, rW, (8 * rb + 4 * par2) * (F * 2) + fg * 896, j * (F * 2));
    SB();
    auto LO = [](u32 a, u32 c) { return (f32x2){lo2f(a), lo2f(c)}; };
    auto HI = [](u32 a, u32 c) { return (f32x2){hi2f(a), hi2f(c)}; };
    const f32x2 w0 = LO(shk.x, shk.x), w1 = HI(shk.x, shk.x), w2 = LO(shk.y, shk.y), w3 = HI(shk.y, shk.y);
    const f32x2 e[12] = {w0, HI(ka[0].x, ka[1].x), LO(ka[0].y, ka[1].y), HI(ka[0].y, ka[1].y), LO(kb[0].x, kb[1].x), HI(kb[0].x, kb[1].x), LO(kb[0].y, kb[1].y),
                         HI(kb[0].y, kb[1].y), LO(kc[0].x, kc[1].x), HI(kc[0].x, kc[1].x), LO(kc[0].y, kc[1].y), HI(kc[0].y, kc[1].y)};
    const f32x2 vv[8] = {LO(va[0].x, va[1].x), HI(va[0].x, va[1].x), LO(va[0].y, va[1].y), HI(va[0].y, va[1].y), LO(vb[0].x, vb[1].x), HI(vb[0].x, vb[1].x), LO(vb[0].y, vb[1].y), HI(vb[0].y, vb[1].y)};
    f32x2 kv[8];
#pragma unroll
    for (int s = 0; s < 8; ++s) kv[s] = (w3 * e[s + 1] + w2 * e[s + 2] + w1 * e[s + 3] + w0 * e[s + 4]) * vv[s];
#pragma unroll
    for (int it = 0; it < 2; ++it)
      __builtin_amdgcn_raw_buffer_store_b128((v4u){pk(kv[7][it], kv[6][it]), pk(kv[5][it], kv[4][it]), pk(kv[3][it], kv[2][it]), pk(kv[1][it], kv[0][it])}, WS, okv + (1 - it) * 64, 0, SC1);
    SB();
    asm volatile("s_waitcnt vmcnt(8)" : "+v"(kz[0]), "+v"(kz[1]), "+v"(kedge), "+v"(vtag), "+v"(vdp)::"memory");
    BAR();
    vtag += 1u;
    tag = __builtin_amdgcn_readfirstlane(vtag); dpair = __builtin_amdgcn_readfirstlane(vdp);
    v2u qa[2], qb[2];
#pragma unroll
    for (int it = 0; it < 2; ++it) { qa[it] = trd(pA, L_Q - L_K + it * 4096); qb[it] = trd(pA, L_Q - L_K + it * 4096 + 512); }
#pragma unroll
    for (int it = 0; it < 2; ++it) __builtin_amdgcn_raw_buffer_store_b128((v4u){qa[it].x, qa[it].y, qb[it].x, qb[it].y}, WS, oqt + it * 64, 0, SC1);
    __builtin_amdgcn_raw_buffer_store_b32(tag * 64u, WS, oci, 0, SC1);
  }
  SB();
  STAMP(24);
  {
    auto put = [&](LAS char* p, v4u d, u32 d4, u32 d5) {
      const u32 e0 = __builtin_amdgcn_alignbit(d.y, d.x, 16), e1 = __builtin_amdgcn_alignbit(d.z, d.y, 16), e2 = __builtin_amdgcn_alignbit(d.w, d.z, 16),
                e3 = __builtin_amdgcn_alignbit(d4, d.w, 16), e4 = __builtin_amdgcn_alignbit(d5, d4, 16);
      *(LAS v4u*)p = d; *(LAS v4u*)(p + TZ_S) = (v4u){e0, e1, e2, e3}; *(LAS v4u*)(p + 2 * TZ_S) = (v4u){d.y, d.z, d.w, d4}; *(LAS v4u*)(p + 3 * TZ_S) = (v4u){e1, e2, e3, e4};
    };
    auto nxt = [](u32 last, u32 v) { return (u32)__builtin_amdgcn_update_dpp((int)last, (int)v, 0x130, 0xf, 0xf, false); };
    put(tw, kz[0], nxt(__builtin_amdgcn_readlane(kz[1].x, 0), kz[0].x), nxt(__builtin_amdgcn_readlane(kz[1].y, 0), kz[0].y));
    put(tw + 1024, kz[1], nxt(par2 ? 0u : kedge.x, kz[1].x), nxt(par2 ? 0u : kedge.y, kz[1].y));
    const u32 f0 = __builtin_amdgcn_readlane(kz[0].x, 0), f1 = __builtin_amdgcn_readlane(kz[0].y, 0);
    if (l < 2 && !par2) put(tw - 32, (v4u){0, 0, 0, 0}, l ? f0 : 0u, l ? f1 : 0u);
  }
  SB();
  asm volatile("s_waitcnt vmcnt(5)" : "+v"(sx0), "+v"(sx1)::"memory");
  BAR();
  {
    v2u ta[4], tc[4];
#pragma unroll
    for (int j = 0; j < 4; ++j) { ta[j] = trd(pW, j * 256); tc[j] = trd(pW, j * 256 + 4 * WR_S); }
#pragma unroll
    for (int j = 0; j < 4; ++j) __builtin_amdgcn_raw_buffer_store_b128((v4u){ta[j].x, ta[j].y, tc[j].x, tc[j].y}, WS, w1d[j], 0, SC1);
  }
  SB();
  {
    const f32x2 v[8] = {{lo2f(sx0.x), hi2f(sx0.x)}, {lo2f(sx0.y), hi2f(sx0.y)}, {lo2f(sx0.z), hi2f(sx0.z)}, {lo2f(sx0.w), hi2f(sx0.w)},
                        {lo2f(sx1.x), hi2f(sx1.x)}, {lo2f(sx1.y), hi2f(sx1.y)}, {lo2f(sx1.z), hi2f(sx1.z)}, {lo2f(sx1.w), hi2f(sx1.w)}};
    const f32x2 s2 = ((v[0] + v[1]) + (v[2] + v[3])) + ((v[4] + v[5]) + (v[6] + v[7]));
    const float sm = hsum(s2[0] + s2[1]);
    const float m0 = u2f(__builtin_amdgcn_readlane(f2u(sm), 31)), m1 = u2f(__builtin_amdgcn_readlane(f2u(sm), 63));
    const float mean = (l < 32 ? m0 : m1) * (1.f / D);
    f32x2 ss = {0.f, 0.f};
#pragma unroll
    for (int j = 0; j < 8; ++j) { const f32x2 d = v[j] - mean; ss += d * d; }
    const float var = hsum(ss[0] + ss[1]) * (1.f / D);
    const u32 pay = pk(mean, __builtin_amdgcn_sqrtf(rbf(rbf(var) + 1e-5f)));
    __builtin_amdgcn_raw_buffer_store_b64((v2u){pay, vtag}, WS, og, 0, SC1);
  }
  SB();
  WAITV(1);
  BAR();
  STAMP(2);
  if (w == 0 && l == 0) __builtin_amdgcn_raw_buffer_store_b32(tag, WS, O_F1 + (cj * 32 + ti) * 128, 0, SC1);
  u32 w2raw[8];
  asm volatile("global_load_dword %0, %8, %12\n\tglobal_load_dword %1, %8, %12 offset:1024\n\tglobal_load_dword %2, %9, %12\n\tglobal_load_dword %3, %9, %12 offset:1024\n\t"
               "global_load_dword %4, %10, %12\n\tglobal_load_dword %5, %10, %12 offset:1024\n\tglobal_load_dword %6, %11, %12\n\tglobal_load_dword %7, %11, %12 offset:1024"
               : "=&v"(w2raw[0]), "=&v"(w2raw[1]), "=&v"(w2raw[2]), "=&v"(w2raw[3]), "=&v"(w2raw[4]), "=&v"(w2raw[5]), "=&v"(w2raw[6]), "=&v"(w2raw[7])
               : "v"(ow[0]), "v"(ow[1]), "v"(ow[2]), "v"(ow[3]), "s"(A.wd) : "memory");
  v4u T[32];
  {
    const u32 tb = lds0 + L_TZ + ch * TZ_C + ((n16 + 1) & 3) * TZ_S + (u32)((((n16 + 1) >> 2) + 2 * q + 8 * par2) * 8);
#pragma unroll
    for (int i = 0; i < 32; ++i) {
      v2u a, c;
      asm volatile("ds_read_b64 %0, %2 offset:%3\n\tds_read_b64 %1, %2 offset:%4" : "=&v"(a), "=&v"(c) : "v"(tb), "i"(128 * i), "i"(128 * i + 8) : "memory");
      T[i] = (v4u){a.x, a.y, c.x, c.y};
    }
    asm volatile("s_waitcnt lgkmcnt(0)" : "+v"(T[0]), "+v"(T[1]), "+v"(T[2]), "+v"(T[3]), "+v"(T[4]), "+v"(T[5]), "+v"(T[6]), "+v"(T[7]), "+v"(T[8]), "+v"(T[9]), "+v"(T[10]),
                 "+v"(T[11]), "+v"(T[12]), "+v"(T[13]), "+v"(T[14]), "+v"(T[15])::"memory");
    asm volatile("" : "+v"(T[16]), "+v"(T[17]), "+v"(T[18]), "+v"(T[19]), "+v"(T[20]), "+v"(T[21]), "+v"(T[22]), "+v"(T[23]), "+v"(T[24]), "+v"(T[25]), "+v"(T[26]),
                 "+v"(T[27]), "+v"(T[28]), "+v"(T[29]), "+v"(T[30]), "+v"(T[31])::"memory");
  }
  const int tB = 512 * par2 + 16 * n16 + 4 * q;
  LAS char* xp = lds + L_XR + ch * XR_S + (2032 - 16 * n16 + 32 * par2 + 8 * q) * 2 - 3584;
  LAS char* kp = lds + L_XR + ch * XR_S + (2044 - 1280 - tB) * 2;
  LAS char* gp = lds + L_S1 + tB * 8;
  LAS char* ep = lds + L_EX + w * 4096 + l * 16;
  LAS char* er = lds + L_EX + (w ^ 1) * 4096 + l * 16;
  u32 qo = O_QT + cB * 4096 + tB * 2;
  u32 oh = O_HT + (u32)((8 * (2 * par2 + (q & 1)) + (n16 >> 1)) * 32768 + (cB >> 5) * 2048 + (cB & 31) * 64 + (((2 * (n16 & 1) + (q >> 1)) ^ (2 * ((cB >> 3) & 1))) << 4));
  asm volatile("" : "+v"(xp), "+v"(kp), "+v"(gp), "+v"(ep), "+v"(er), "+v"(qo), "+v"(oh), "+v"(l16));
  const int hp = 4 * (x >> 2) + (y >> 3), fch = 8 * (x & 3) + (y & 7);
  const int ct = y & 7, rbl = y >> 3;
  const u32 ring = L_R + w * 12288;
  const int rot = fch & 3, spar = (fch >> 2) & 1;
  u32 w2v = O_W2T + (u32)((ct * 56 + 14 * w) * 4096) + l16;
  u32 ost = (fch < 8 && rr == q) ? O_ST2 + (u32)(rot * 32768 + (128 * hp + 32 * w + 16 * spar + n16) * 16) : 0xffff0000u;
  u32 og2 = O_ST2 + (u32)((l >> 5) * 65536 + (128 * hp + 32 * w + (l & 31)) * 16);
  u32 oact = O_ACT + (u32)(((8 * hp + 2 * w) * 224 + 7 * fch + q) * 256 + n16 * 16), oact2 = q < 3 ? oact + 1024 : 0xff000000u;
  u32 of3 = O_F3 + (u32)(((x * 4 + rbl) * 4 + w) * 128 + (l & 7) * 4), of3w = l == 0 ? O_F3 + (u32)(((hp * 4 + w) * 4 + (fch >> 3)) * 128 + (fch & 7) * 4) : 0xffff0000u;
  u32 av = O_ACT + (u32)(((8 * x + 2 * rbl) * 224 + 56 * w) * 256) + l16;
  LAS char* hx[2];
#pragma unroll
  for (int pr = 0; pr < 2; ++pr) hx[pr] = lds + ring + (8 * q + rr) * 64 + (((2 * (pr ^ spar) + (p4 >> 1)) ^ (2 * (q & 1))) << 4) + (p4 & 1) * 8;
  LAS char* pcs = lds + ring + 10240 + (n16 == 0 ? q * 16 : 128 + l * 16);
  LAS char* pcr = lds + L_R + 10240 + q * 16;
  asm volatile("" : "+v"(av), "+v"(pcs), "+v"(pcr), "+v"(w2v), "+v"(ost), "+v"(og2), "+v"(oact), "+v"(oact2), "+v"(of3), "+v"(of3w), "+v"(hx[0]), "+v"(hx[1]));
  STAMP(21);
  if (w == 0) spin(O_F1 + ((x ^ (4 * (l >> 5))) * 32 + (l & 31)) * 128, tag);
  BAR();
  STAMP(3);

  dma<true>(lds0 + ch * XR_S + par2 * 2048, L_XR, l16, rWS, cB * 4096 + par2 * 2048, O_KVT);
  dma<true>(lds0 + ch * XR_S + par2 * 2048, L_XR + 1024, l16, rWS, cB * 4096 + par2 * 2048, O_KVT + 1024);
#pragma unroll
  for (int i = 0; i < 4; ++i) dma<true>(lds0 + w * 4096, L_S1 + i * 1024, l16, rWS, w * 4096, O_ST1 + i * 1024);
  v2u xq[4];
  asm volatile("buffer_load_dwordx2 %0, %4, %5, 0 offen sc1\n\tbuffer_load_dwordx2 %1, %4, %5, 0 offen offset:512 sc1\n\t"
               "buffer_load_dwordx2 %2, %4, %5, 0 offen offset:2048 sc1\n\tbuffer_load_dwordx2 %3, %4, %5, 0 offen offset:2560 sc1"
               : "=&v"(xq[0]), "=&v"(xq[1]), "=&v"(xq[2]), "=&v"(xq[3]) : "v"(qo), "s"(rWS) : "memory");
  SB();
  asm volatile("s_waitcnt vmcnt(8)" : "+v"(w2raw[0]), "+v"(w2raw[1]), "+v"(w2raw[2]), "+v"(w2raw[3]), "+v"(w2raw[4]), "+v"(w2raw[5]), "+v"(w2raw[6]), "+v"(w2raw[7])::"memory");
  BAR();
  STAMP(4);
  v4u W2[56];
  auto w2l = [&](int i) { W2[i] = __builtin_amdgcn_raw_buffer_load_b128(WS, w2v + (i & 3) * 1024, (i >> 2) * 4096, SC1); };
  u32 hpk[4];
  GAS u32* pc2 = (GAS u32*)(A.ws + O_C2 + (l & 7) * 2048 + x * 256);
  auto put_h = [&](int h) {
    const auto s0 = __builtin_amdgcn_permlane16_swap(hpk[0], hpk[2], false, false);
    const auto s1 = __builtin_amdgcn_permlane16_swap(hpk[1], hpk[3], false, false);
    __builtin_amdgcn_raw_buffer_store_b128((v4u){s0[0], s1[0], s0[1], s1[1]}, WS, oh + h * (32 * 32768), 0, SC1);
  };
  const u32 oc2 = O_C2 + x * 2048 + (l & 7) * 256, c2want = tag * 64u + 32u;
  {
    u32 w1so, flw;
    auto w1l = [&](int i) {
      if ((i & 3) == 0) asm volatile("s_add_u32 m0, %1, %2\n\ts_add_u32 %0, %3, %4\n\tbuffer_load_dwordx4 %5, %6, %0 offen sc1 lds"
                                     : "=&s"(w1so) : "s"(lds0 + w * 28672), "i"(i * 1024), "s"(fch * 114688 + w * 28672), "i"(O_W1F + i * 1024), "v"(l16), "s"(rWS) : "memory", "scc");
      else asm volatile("buffer_load_dwordx4 %0, %1, %2 offen offset:%3 sc1 lds" ::"v"(l16), "s"(rWS), "s"(w1so), "i"((i & 3) * 1024) : "memory");
    };
    const float dsc = ch ? hi2f(dpair) : lo2f(dpair);
    asm volatile("" : "+v"(pc2));
    f32x4 cacc[8], mn[2], ot[2];
    v4u B[32], g1[8], w2f[2]; v2u kr[4];
    float xf[16], ln[16], kd[16], tm[16], hh[8];
    auto fix = [&](int h) {
      u32 df = 0;
#pragma unroll
      for (int i = 4 * h; i < 4 * h + 4; ++i) df |= (g1[i].y ^ vtag) | (g1[i].w ^ vtag);
      if (__builtin_amdgcn_ballot_w64(df != 0)) {
        bool ok;
        for (u32 sp = 0; sp < SPIN_CAP; ++sp) {
          asm volatile("" ::: "memory");
          ok = true;
#pragma unroll
          for (int i = 4 * h; i < 4 * h + 4; ++i) {
            g1[i] = __builtin_amdgcn_raw_buffer_load_b128(WS, O_ST1 + (tB + 256 * ((i >> 1) & 1) + 1024 * h) * 8 + (i & 1) * 16, 0, SC1);
            ok &= (g1[i].y == tag) & (g1[i].w == tag);
          }
          if (__all(ok)) break;
        }
#pragma unroll
        for (int j = 8 * h; j < 8 * h + 8; ++j) { const u32 pay = g1[j >> 1][(j & 1) * 2]; ln[j] = (xf[j] - lo2f(pay)) * __builtin_amdgcn_rcpf(hi2f(pay)); }
      }
    };
    auto give = [&](int h) {
      if (par2) { *(LAS f32x4*)(ep + 2048 * h) = cacc[4 * h]; *(LAS f32x4*)(ep + 2048 * h + 1024) = cacc[4 * h + 1]; mn[0] = cacc[4 * h + 2]; mn[1] = cacc[4 * h + 3]; asm volatile("" : "+v"(mn[0]), "+v"(mn[1])); }
      else { *(LAS f32x4*)(ep + 2048 * h) = cacc[4 * h + 2]; *(LAS f32x4*)(ep + 2048 * h + 1024) = cacc[4 * h + 3]; mn[0] = cacc[4 * h]; mn[1] = cacc[4 * h + 1]; asm volatile("" : "+v"(mn[0]), "+v"(mn[1])); }
    };
    auto pre = [&](auto J_, auto P_) {
      constexpr int j = decltype(J_)::value, ph = decltype(P_)::value;
      const u32 pay = g1[j >> 1][(j & 1) * 2];
      if constexpr (ph == 0) { xf[j] = (j & 1) ? hi2f(xq[j >> 2][(j & 3) >> 1]) : lo2f(xq[j >> 2][(j & 3) >> 1]); asm volatile("" : "+v"(xf[j])); }
      if constexpr (ph == 1) { tm[j] = __builtin_amdgcn_rcpf(hi2f(pay)); asm volatile("" : "+v"(tm[j])); }
      if constexpr (ph == 2) { ln[j] = xf[j] - lo2f(pay); asm volatile("" : "+v"(ln[j])); }
      if constexpr (ph == 3) { ln[j] *= tm[j]; asm volatile("" : "+v"(ln[j])); }
      if constexpr (ph == 4) { const u32 kk = kr[j >> 2][1 - ((j & 3) >> 1)]; kd[j] = (j & 1) ? lo2f(kk) : hi2f(kk); asm volatile("" : "+v"(kd[j])); }
    };
    sfor<0, 32>([&](auto U_) { constexpr int u = decltype(U_)::value; if constexpr (cv_slot(u) < 0) B[u] = *(LAS v4u*)(xp + 128 * (31 - u)); });
    SB();
    sfor<0, 144>([&](auto N_) {
      constexpr int n = decltype(N_)::value, u = cv_u(n), i = u < 16 ? u : u - 16, a = cv_a0(u) + n - cv_first(u), eps = 4 * a - i + 3;
      mfb<i == 0>(cacc[a], T[eps], B[i]);
      SB();
      constexpr int ub = cv_at(n);
      if constexpr (ub >= 0) B[ub] = *(LAS v4u*)(xp + 128 * (31 - ub));
      if constexpr (n < 112 && n % 4 == 1) w1l(n / 4);
      if constexpr (n == 2 || n == 3) {
        constexpr int pp = n - 2;
#pragma unroll
        for (int d = 0; d < 4; ++d) w2f[pp][d] = __builtin_amdgcn_perm(w2raw[2 * d + 1], w2raw[2 * d], pp ? 0x07060302u : 0x05040100u);
        __builtin_amdgcn_raw_buffer_store_b128(w2f[pp], WS, w2o + pp * 1024, 0, SC1);
      }
      if constexpr (n >= 11 && n < 27 && n % 4 == 3) { constexpr int aa = (n - 11) / 4; kr[aa] = *(LAS v2u*)(kp + 2560 - 512 * (aa & 1) - 2048 * (aa >> 1)); }
      if constexpr (n >= 23 && n < 39 && n % 2 == 1) pre(IC<(n - 23) / 2>{}, IC<4>{});
      if constexpr (n == 43) STAMP(25);
      if constexpr (n == 46) STAMP(26);
      if constexpr (n == 67) STAMP(27);
      if constexpr (n == 111) STAMP(28);
      if constexpr (n == 113) STAMP(22);
      if constexpr (n == 44) give(0);
      if constexpr (n == 45) {
        asm volatile("s_waitcnt vmcnt(11) lgkmcnt(0)" : "+v"(xq[0]), "+v"(xq[1]), "+v"(xq[2]), "+v"(xq[3])::"memory");
        __builtin_amdgcn_s_barrier();
        asm volatile("" ::: "memory");
        if (w == 0 && l < 8) __hip_atomic_fetch_add(pc2 + 8192, 1u, RLX);
      }
      if constexpr (n == 46 || n == 47) { g1[2 * (n - 46)] = *(LAS v4u*)(gp + (n - 46) * 2048); g1[2 * (n - 46) + 1] = *(LAS v4u*)(gp + (n - 46) * 2048 + 16); }
      if constexpr (n == 48) { ot[0] = *(LAS f32x4*)er; ot[1] = *(LAS f32x4*)(er + 1024); }
      if constexpr (n >= 50 && n < 58) { pre(IC<n - 50>{}, IC<0>{}); pre(IC<n - 50>{}, IC<1>{}); pre(IC<n - 50>{}, IC<2>{}); if constexpr (n > 50) pre(IC<n - 51>{}, IC<3>{}); }
      if constexpr (n == 58) { pre(IC<7>{}, IC<3>{}); fix(0); }
      if constexpr (n >= 59 && n < 63) {
#pragma unroll
        for (int j = 2 * (n - 59); j < 2 * (n - 59) + 2; ++j) { hh[j] = xf[j] + ln[j] * ((kd[j] * dsc + mn[j >> 2][j & 3]) + ot[j >> 2][j & 3]); asm volatile("" : "+v"(hh[j])); }
      }
      if constexpr (n == 63 || n == 64) { hpk[2 * (n - 63)] = pk(hh[4 * (n - 63)], hh[4 * (n - 63) + 1]); hpk[2 * (n - 63) + 1] = pk(hh[4 * (n - 63) + 2], hh[4 * (n - 63) + 3]); }
      if constexpr (n == 66) put_h(0);
      if constexpr (n == 98) asm volatile("buffer_load_dword %0, %1, %2, 0 offen sc1" : "=&v"(flw) : "v"(oc2 + 32768), "s"(rWS) : "memory");
      if constexpr (n == 112) {
        asm volatile("s_waitcnt vmcnt(3)" : "+v"(flw)::"memory");
        __builtin_amdgcn_s_barrier();
        if (w == 0 && l < 8) __hip_atomic_fetch_add(pc2, 1u, RLX);
        if (!__all(flw == c2want)) spin(oc2 + 32768, c2want);
      }
      if constexpr (n > 112 && n % 2 == 1 && (n - 113) / 2 < NWE) w2l((n - 113) / 2);
      if constexpr (n >= 90 && n < 98 && n % 2 == 0) { constexpr int j = 4 + (n - 90) / 2; g1[j] = *(LAS v4u*)(gp + 8192 + ((j >> 1) & 1) * 2048 + (j & 1) * 16); }
      if constexpr (n >= 104) pre(IC<8 + (n - 104) / 5>{}, IC<(n - 104) % 5>{});
      SB();
    });
    asm volatile("s_nop 7" ::: "memory");
    SB();
    STAMP(5);
    give(1);
    fix(1);
    WAITV(NWE < 16 ? NWE : 16);
    BAR();
#pragma unroll
    for (int i = 16; i < NWE; ++i) w2l(i);
#pragma unroll
    for (int aa = 0; aa < 2; ++aa) {
      const f32x4 o = *(LAS f32x4*)(er + 2048 + aa * 1024);
#pragma unroll
      for (int i = 0; i < 4; ++i) hh[4 * aa + i] = xf[8 + 4 * aa + i] + ln[8 + 4 * aa + i] * ((kd[8 + 4 * aa + i] * dsc + mn[aa][i]) + o[i]);
      hpk[2 * aa] = pk(hh[4 * aa], hh[4 * aa + 1]); hpk[2 * aa + 1] = pk(hh[4 * aa + 2], hh[4 * aa + 3]);
    }
    asm volatile("" : "+v"(hpk[0]), "+v"(hpk[1]), "+v"(hpk[2]), "+v"(hpk[3]));
    STAMP(6);
  }

  v4u ones = {0x3f803f80u, 0x3f803f80u, 0x3f803f80u, 0x3f803f80u};
  LAS char* wp0 = lds + l * 16;
  asm volatile("" : "+v"(ones), "+v"(wp0));
  u32 sg[4];
#pragma unroll
  for (int g = 0; g < 4; ++g) sg[g] = (u32)((g + rot) & 3);
  LAS char* wp = wp0 + sg[0] * 28672;
  v4u wf[WD + 1], hf[2][2], g2[2];
#pragma unroll
  for (int i = 0; i < WD; ++i) wf[i] = *(LAS v4u*)(wp + i * 1024);
  STAMP(7);
  if (w == 0) spin(oc2, c2want);
  BAR();
  STAMP(8);
  f32x4 cs[7];
  u32 fl3 = 0, fl2 = 0;
  v4u AC[28][2];
  const u32 ao[2] = {(u32)((w & 1) * (224 * 256)), (u32)(((w & 1) ^ 1) * (224 * 256))};
  auto ald = [&](int u) {
#pragma unroll
    for (int c = 0; c < 2; ++c) { AC[u][c] = __builtin_amdgcn_raw_buffer_load_b128(WS, av + (u % 14 & 3) * 1024, ao[c] + (u % 14 >> 2) * 4096 + (u / 14) * (64 * 224 * 256), SC1); SB(); }
  };
  {
    u32 hso;
    auto hdma = [&](int g, int n) {
      if (n == 0) asm volatile("s_add_u32 m0, %1, %2\n\ts_add_u32 %0, %3, %4\n\tbuffer_load_dwordx4 %5, %6, %0 offen sc1 lds"
                               : "=&s"(hso) : "s"(lds0 + ring), "i"((g % 5) * 2048), "s"((4 * hp + w) * 32768 + sg[(g >> 2) & 3] * 8192), "i"(O_HT + (g >> 4) * (32 * 32768) + (g & 3) * 2048), "v"(l16), "s"(rWS) : "memory", "scc");
      else asm volatile("buffer_load_dwordx4 %0, %1, %2 offen offset:1024 sc1 lds" ::"v"(l16), "s"(rWS), "s"(hso) : "memory");
    };
    auto hfrag = [&](int g, int pr, int t) { return trd(hx[pr], (g % 5) * 2048 + t * 256); };
    f32x4 acc[7][2], sS, sQ;
    auto epilogue = [&](auto P_) {
      constexpr int ps = decltype(P_)::value;
      if (!__all((g2[0].y == tag) & (g2[0].w == tag) & (g2[1].y == tag) & (g2[1].w == tag))) {
        for (u32 sp = 0; sp < SPIN_CAP; ++sp) {
          asm volatile("" ::: "memory");
#pragma unroll
          for (int k = 0; k < 2; ++k) g2[k] = __builtin_amdgcn_raw_buffer_load_b128(WS, og2, k * 32768 + ps * 16384, SC1);
          if (__all((g2[0].y == tag) & (g2[0].w == tag) & (g2[1].y == tag) & (g2[1].w == tag))) break;
        }
      }
      const u32 s1 = f2u(u2f(g2[0].x) + u2f(g2[1].x)), s2 = f2u(u2f(g2[0].z) + u2f(g2[1].z));
      const auto h1 = __builtin_amdgcn_permlane32_swap(s1, s1, false, false), h2 = __builtin_amdgcn_permlane32_swap(s2, s2, false, false);
      const float mean = (u2f(h1[0]) + u2f(h1[1])) * (1.f / D);
      const float v2 = __builtin_fmaxf((u2f(h2[0]) + u2f(h2[1])) * (1.f / D) - mean * mean, 0.f) + 1e-5f;
      const u32 e0 = f2u(-mean), e1 = f2u(-1.44269504089f * __builtin_amdgcn_rsqf(v2)), e2 = f2u(v2);
      const auto b0 = __builtin_amdgcn_permlane16_swap(e0, e0, false, false), b1 = __builtin_amdgcn_permlane16_swap(e1, e1, false, false),
                 b2 = __builtin_amdgcn_permlane16_swap(e2, e2, false, false);
      v4u sa[2], sb[2];
#pragma unroll
      for (int pr = 0; pr < 2; ++pr) {
        const bool up = (pr ^ spar) != 0;
        const float nm = u2f(up ? b0[1] : b0[0]), k1 = u2f(up ? b1[1] : b1[0]), vv = u2f(up ? b2[1] : b2[0]);
        u32 d[8];
        f32x2 a[7], bb[7], e[7];
#pragma unroll
        for (int t = 0; t < 7; ++t) a[t] = (f32x2){acc[t][pr][0], acc[t][pr][1]} + nm * (f32x2){cs[t][0], cs[t][1]};
        SB();
#pragma unroll
        for (int t = 0; t < 7; ++t) e[t] = a[t] * k1;
        SB();
#pragma unroll
        for (int t = 0; t < 7; ++t) {
          e[t] = (f32x2){__builtin_amdgcn_exp2f(e[t][0]), __builtin_amdgcn_exp2f(e[t][1])};
          bb[t] = (f32x2){acc[t][pr][2], acc[t][pr][3]} + nm * (f32x2){cs[t][2], cs[t][3]};
          if (ps == 0 && 7 * (1 - pr) + t < W2_EPI) w2l(w2_first(16) - W2_EPI + 7 * (1 - pr) + t);
          if (ps == 1 && t < 3) ald(8 + 3 * pr + t);
          SB();
        }
#pragma unroll
        for (int t = 0; t < 7; ++t) e[t] = e[t] * vv + vv;
        SB();
#pragma unroll
        for (int t = 0; t < 7; ++t) { e[t] = (f32x2){__builtin_amdgcn_rcpf(e[t][0]), __builtin_amdgcn_rcpf(e[t][1])}; a[t] *= bb[t]; SB(); }
#pragma unroll
        for (int t = 0; t < 7; ++t) a[t] *= e[t];
        SB();
#pragma unroll
        for (int t = 0; t < 7; ++t) d[t] = pk(a[t][0], a[t][1]);
        sa[pr] = (v4u){d[0], d[1], d[2], d[3]};
        const auto x0 = __builtin_amdgcn_permlane32_swap(d[4], d[6], false, false), x1 = __builtin_amdgcn_permlane32_swap(d[5], d[5], false, false);
        const auto y0 = __builtin_amdgcn_permlane16_swap(x0[0], x1[0], false, false), y1 = __builtin_amdgcn_permlane16_swap(x0[1], x1[1], false, false);
        sb[pr] = (v4u){y0[0], y0[1], y1[0], y1[1]};
        asm volatile("" : "+v"(sa[pr]), "+v"(sb[pr]));
        SB();
      }
#pragma unroll
      for (int pr = 0; pr < 2; ++pr) {
        const u32 ro = (u32)((pr ^ spar) * (224 * 256) + ps * (64 * 224 * 256));
        __builtin_amdgcn_raw_buffer_store_b128(sa[pr], WS, oact + ro, 0, SC1);
        __builtin_amdgcn_raw_buffer_store_b128(sb[pr], WS, oact2 + ro, 0, SC1);
      }
      SB();
    };
    hdma(0, 0); hdma(0, 1); hdma(1, 0); hdma(1, 1);
    {
      LAS char* c0 = wp0 + w * 1024;
      LAS char* c1 = wp0 + (w == 3 ? 3 : w + 4) * 1024;
      LAS char* c2 = c0 + 57344;
      LAS char* c3 = c1 + 57344;
      asm volatile("" : "+v"(c0), "+v"(c1), "+v"(c2), "+v"(c3));
      auto crd = [&](int k, int hi) { return *(LAS v4u*)((k < 8 ? (hi ? c1 : c0) : (hi ? c3 : c2)) + (k & 7) * 7168); };
      v4u fa[16], fb[16]; f32x4 ca, cb;
#pragma unroll
      for (int k = 0; k < 4; ++k) { fa[k] = crd(k, 0); fb[k] = crd(k, 1); }
      SB();
      sfor<0, 16>([&](auto K_) {
        constexpr int k = decltype(K_)::value;
        mfv<k == 0>(ca, fa[k], ones); SB();
        if constexpr (k + 4 < 16) fa[k + 4] = crd(k + 4, 0);
        SB();
        mfv<k == 0>(cb, fb[k], ones); SB();
        if constexpr (k + 4 < 16) fb[k + 4] = crd(k + 4, 1);
        if constexpr (k >= 2 && k < 8) hdma(2 + (k - 2) / 2, k & 1);
        if constexpr (k == 12) put_h(1);
        SB();
      });
      WAITV(9);
#pragma unroll
      for (int pr = 0; pr < 2; ++pr) { const v2u a = hfrag(0, pr, 0), c = hfrag(0, pr, 1); hf[0][pr] = (v4u){a.x, a.y, c.x, c.y}; }
      asm volatile("s_nop 7" ::: "memory");
      *(LAS f32x4*)pcs = ca; *(LAS f32x4*)(pcs + 64) = cb;
    }
    SB();
    STAMP(20);
    sfor<0, 32>([&](auto G_) {
      constexpr int g = decltype(G_)::value, s = g & 15;
      v4u(&hc)[2] = hf[g & 1]; v4u(&hn)[2] = hf[(g + 1) & 1];
      LAS char* wn = wp;
      if constexpr ((g & 3) == 3 && g < 31) wn = wp0 + sg[((g + 1) >> 2) & 3] * 28672;
      v2u ha;
      sfor<0, 7>([&](auto T_) {
        constexpr int t = decltype(T_)::value, it = g * 7 + t, nx = it + WD, ng = nx / 7, nt = nx % 7;
        if constexpr (t == 0) asm volatile("" : "+v"(wf[it % 8]), "+v"(wf[(it + 1) % 8]), "+v"(wf[(it + 2) % 8]), "+v"(wf[(it + 3) % 8]), "+v"(hc[0]), "+v"(hc[1]));
        if constexpr (t == 4) asm volatile("" : "+v"(wf[it % 8]), "+v"(wf[(it + 1) % 8]), "+v"(wf[(it + 2) % 8]));
        mfv<s == 0>(acc[t][0], wf[it % (WD + 1)], hc[0]);
        SB();
        if constexpr (nx < 224) wf[nx % (WD + 1)] = *(LAS v4u*)((ng > g ? wn : wp) + (ng & 3) * 7168 + nt * 1024);
        SB();
        mfv<s == 0>(acc[t][1], wf[it % (WD + 1)], hc[1]);
        SB();
        if constexpr (s < 15 && t < 4) {
          if constexpr (t == 0) { constexpr int hw = h_wait(g); WAITV(hw); }
          if constexpr (t & 1) { const v2u hb = hfrag(g + 1, t >> 1, 1); hn[t >> 1] = (v4u){ha.x, ha.y, hb.x, hb.y}; } else ha = hfrag(g + 1, t >> 1, 0);
        }
        if constexpr (t >= 4 && t - 4 < w2_n(g)) w2l(w2_first(g) + t - 4);
        if constexpr (s < 11 && t >= 5) hdma(g + 5, t - 5);
        if constexpr (g == 28 && t == 4) {
          constexpr int nw = vm_sum(26, 28);
          asm volatile("s_waitcnt vmcnt(%1)" : "+v"(fl3) : "n"(nw) : "memory");
          if (!__all(fl3 == tag)) spin(of3, tag);
        }
        if constexpr (g >= 28 && (t == 4 || t == 6)) ald(2 * (g - 28) + (t - 4) / 2);
        if constexpr (g == 5 || g == 6) { if constexpr (t >= 3 && 4 * (g - 5) + t - 3 < 7) { constexpr int k = 4 * (g - 5) + t - 3; cs[k] = *(LAS f32x4*)(pcr + (k & 3) * 12288 + (k >> 2) * 64); } }
        SB();
      });
      if constexpr (s < 4) { mfv<s == 0>(sS, ones, hc[0]); mfv<s == 0>(sQ, hc[0], hc[0]); }
      if constexpr (g == 4) {
        constexpr int nw = vm_sum(0, 4) + w2_n(4) + 2;
        WAITV(nw);
        __builtin_amdgcn_s_barrier();
        asm volatile("" ::: "memory");
        if (w == 0) { if (l < 8) __hip_atomic_fetch_add(pc2 + 4096, 1u, RLX); }
        else __builtin_amdgcn_raw_buffer_store_b32(vtag, WS, 0xffffff00u, 0, SC1);
      }
      if constexpr (g == 13) asm volatile("buffer_load_dword %0, %1, %2, 0 offen sc1" : "=&v"(fl2) : "v"(oc2 + 16384), "s"(rWS) : "memory");
      if constexpr (s == 5) {
        float q01, q23, qq;
        asm volatile("v_cndmask_b32 %0, %3, %4, %7\n\tv_cndmask_b32 %1, %5, %6, %7\n\tv_cndmask_b32 %2, %0, %1, %8"
                     : "=&v"(q01), "=&v"(q23), "=&v"(qq) : "v"(sQ[0]), "v"(sQ[1]), "v"(sQ[2]), "v"(sQ[3]), "s"(0xaaaaaaaaaaaaaaaaull), "s"(0xccccccccccccccccull));
        __builtin_amdgcn_raw_buffer_store_b128((v4u){f2u(sS[0]), vtag, f2u(qq), vtag}, WS, ost + (g >> 4) * 16384, 0, SC1);
      }
      if constexpr (s == 12) {
        asm volatile("buffer_load_dwordx4 %0, %2, %3, %4 offen sc1\n\tbuffer_load_dwordx4 %1, %2, %3, %5 offen sc1" : "=&v"(g2[0]), "=&v"(g2[1])
                     : "v"(og2), "s"(rWS), "s"((g >> 4) * 16384), "s"(32768 + (g >> 4) * 16384) : "memory");
      }
      if constexpr (g == 19) {
        constexpr int nw = vm_sum(16, 19) + w2_n(19) + 2;
        WAITV(nw);
        __builtin_amdgcn_raw_buffer_store_b32(vtag, WS, of3w, 0, SC1);
      }
      if constexpr (g == 27) {
        dma<true>(lds0 + ring, 4096, l16, rWS, (32 * (w >> 1) + 4 * x + rbl) * 32768 + (2 * ct + (w & 1)) * 2048, O_HT);
        dma<true>(lds0 + ring, 5120, l16, rWS, (32 * (w >> 1) + 4 * x + rbl) * 32768 + (2 * ct + (w & 1)) * 2048, O_HT + 1024);
      }
      if constexpr (g == 25) asm volatile("buffer_load_dword %0, %1, %2, 0 offen sc1" : "=&v"(fl3) : "v"(of3), "s"(rWS) : "memory");
      SB();
      wp = wn;
      if constexpr ((g & 7) == 7) STAMP(16 + (g >> 3));
      if constexpr (s == 15) {
        constexpr int nw = g == 15 ? vm_sum(14, 16) : vm_sum(29, 32);
        asm volatile("s_waitcnt vmcnt(%3)\n\ts_nop 7" : "+v"(g2[0]), "+v"(g2[1]), "+v"(fl2) : "n"(nw) : "memory");
        SB();
        if constexpr (g == 15) {
          if (!__all(fl2 == c2want)) spin(oc2 + 16384, c2want);
#pragma unroll
          for (int k = 16; k < 21; ++k) { hdma(k, 0); hdma(k, 1); }
        }
        epilogue(IC<(g >> 4)>{});
        if constexpr (g == 15) {
          WAITV(8 + vm_pre(1));
#pragma unroll
          for (int pr = 0; pr < 2; ++pr) { const v2u a = hfrag(16, pr, 0), c = hfrag(16, pr, 1); hf[0][pr] = (v4u){a.x, a.y, c.x, c.y}; }
          SB();
        }
        if constexpr (g == 15) STAMP(9);
      }
    });
  }
  STAMP(10);

  {
    __builtin_amdgcn_s_barrier();
    asm volatile("" ::: "memory");
    v2u hr[4];
#pragma unroll
    for (int i = 0; i < 4; ++i)
      hr[i] = trd(lds, L_R + (u32)(((w & 2) + (i >> 1)) * 12288 + 4096 + (8 * q + 4 * (i & 1) + rr) * 64 + (((2 * (w & 1) + (p4 >> 1)) ^ (2 * (q & 1))) << 4) + (p4 & 1) * 8));
    LAS char* pw = lds + w * 4096 + l * 16;
    u32 f3b;
    f32x4 o[4][2], mine[4];
    SB();
    sfor<0, 28>([&](auto U_) {
      constexpr int u = decltype(U_)::value;
#pragma unroll
      for (int c = 0; c < 2; ++c)
#pragma unroll
        for (int pp = 0; pp < 4; ++pp) mfa<u % 14 == 0>(o[pp][c], W2[4 * (u % 14) + pp], AC[u][c]);
      SB();
      if constexpr (u == 5) {
        WAITV(0);
        __builtin_amdgcn_raw_buffer_store_b32(vtag, WS, of3w + 16384, 0, SC1);
        SB();
      }
      if constexpr (u == UPOLL) { f3b = __builtin_amdgcn_raw_buffer_load_b32(WS, of3 + 16384, 0, SC1); SB(); }
      if constexpr (u == UCHK) {
        if (!__all(f3b == tag)) spin(of3 + 16384, tag);
        STAMP(12);
#pragma unroll
        for (int v = 14; v < 20; ++v) ald(v);
      }
      if constexpr (u > UCHK && u <= UCHK + 8) ald(u - UCHK + 19);
      if constexpr (u % 14 == 13) {
        asm volatile("s_nop 7" ::: "memory");
        SB();
        constexpr int hfb = 2 * (u / 14);
#pragma unroll
        for (int pp = 0; pp < 4; ++pp) *(LAS f32x4*)(pw + ((hfb + ((w & 1) ^ 1)) * 16384) + pp * 1024) = o[pp][1];
        if ((w & 2) == hfb) {
#pragma unroll
          for (int pp = 0; pp < 4; ++pp) { mine[pp] = o[pp][0]; asm volatile("" : "+v"(mine[pp])); }
        } else {
#pragma unroll
          for (int pp = 0; pp < 4; ++pp) *(LAS f32x4*)(pw + ((hfb + (w & 1)) * 16384) + pp * 1024) = o[pp][0];
        }
        if constexpr (u == 13) STAMP(11);
        SB();
      }
    });
    STAMP(13);
    BAR();
#pragma unroll
    for (int j = 1; j < 4; ++j)
#pragma unroll
      for (int pp = 0; pp < 4; ++pp) mine[pp] += *(LAS f32x4*)(lds + w * 16384 + ((w + j) & 3) * 4096 + pp * 1024 + l * 16);
    u32 r[8];
#pragma unroll
    for (int i = 0; i < 4; ++i) {
      r[2 * i] = pk(lo2f(hr[i].x) + mine[0][i], hi2f(hr[i].x) + mine[1][i]);
      r[2 * i + 1] = pk(lo2f(hr[i].y) + mine[2][i], hi2f(hr[i].y) + mine[3][i]);
    }
    u16* op = A.out + (1024 * (w >> 1) + 128 * x + 32 * rbl + 16 * (w & 1) + n16) * D + 64 * ct + 8 * q;
    *(v4u*)op = (v4u){r[0], r[1], r[2], r[3]};
    *(v4u*)(op + 32) = (v4u){r[4], r[5], r[6], r[7]};
    if (b == 0 && w == 0 && l == 0) __builtin_amdgcn_raw_buffer_store_b32(tag, WS, O_EP, 0, SC1);
  }
}
}

extern "C" void kernel_launch(void* const* d_in, const int* in_sizes, int n_in, void* d_out, int out_size, void* d_ws, size_t ws_size, hipStream_t stream) {
  fk::Args a;
  a.q = (const fk::u16*)d_in[0]; a.k = (const fk::u16*)d_in[1]; a.v = (const fk::u16*)d_in[2]; a.shk = (const fk::u16*)d_in[3];
  a.ssk = (const fk::u16*)d_in[4]; a.ssd = (const fk::u16*)d_in[5]; a.wg = (const fk::u16*)d_in[7]; a.wu = (const fk::u16*)d_in[8];
  a.wd = (const fk::u16*)d_in[9]; a.out = (fk::u16*)d_out; a.ws = (char*)d_ws;
  fk::fused<<<256, 256, 0, stream>>>(a);
}
```

```cpp
#include <hip/hip_runtime.h>
#include <stdint.h>
#define NWE 20
#define PFM 0
#define UPOLL 8
#define UCHK 11

namespace fk {
constexpr int WD = 7;
typedef unsigned u32;
typedef unsigned short u16;
typedef unsigned long long u64;
typedef u32 v4u __attribute__((ext_vector_type(4)));
typedef u32 v2u __attribute__((ext_vector_type(2)));
typedef float f32x4 __attribute__((ext_vector_type(4)));
typedef float f32x2 __attribute__((ext_vector_type(2)));
typedef short s16x4 __attribute__((ext_vector_type(4)));
typedef short s16x8 __attribute__((ext_vector_type(8)));
typedef __bf16 bf2 __attribute__((ext_vector_type(2)));
#define LAS __attribute__((address_space(3)))
#define GAS __attribute__((address_space(1)))
#define RLX __ATOMIC_RELAXED, __HIP_MEMORY_SCOPE_AGENT
#define SB() __builtin_amdgcn_sched_barrier(0)
#define WAITV(n) asm volatile("s_waitcnt vmcnt(%0)" ::"n"(n) : "memory")
#define BAR() do { asm volatile("s_waitcnt lgkmcnt(0)" ::: "memory"); __builtin_amdgcn_s_barrier(); asm volatile("" ::: "memory"); } while (0)

constexpr int S = 2048, D = 512, F = 1792;
constexpr u32 O_EP = 0, O_C2 = 16384, O_ST1 = 65536, O_F1 = 98304, O_ST2 = 131072, O_F3 = 262144;
constexpr u32 O_KVT = 1u << 20, O_QT = 3u << 20, O_HT = 5u << 20, O_W2T = 7u << 20, O_ACT = 9u << 20, O_W1F = 17u << 20, WS_BYTES = 21u << 20;
constexpr u32 O_DIAG = 24u << 20;
constexpr u32 L_R = 114688, L_K = L_R, L_V = L_R + 9216, L_Q = L_R + 17408, L_WR = 0, WR_S = 1056, L_TZ = 36864, TZ_S = 4160, TZ_C = 4 * TZ_S, L_XR = L_R + 33280, XR_S = 4608,
              L_S1 = L_R, L_EX = L_R + 16384, LDS_BYTES = 163840;
constexpr u32 SPIN_CAP = 1u << 18;
constexpr int SC1 = 16;

struct Args { const u16 *q, *k, *v, *shk, *ssk, *ssd, *wg, *wu, *wd; u16* out; char* ws; };

__device__ __forceinline__ u32 f2u(float x) { return __builtin_bit_cast(u32, x); }
__device__ __forceinline__ float u2f(u32 x) { return __builtin_bit_cast(float, x); }
__device__ __forceinline__ float lo2f(u32 x) { return u2f(x << 16); }
__device__ __forceinline__ float hi2f(u32 x) { return u2f(x & 0xffff0000u); }
__device__ __forceinline__ u32 pk(float lo, float hi) { bf2 r = {(__bf16)lo, (__bf16)hi}; return __builtin_bit_cast(u32, r); }
__device__ __forceinline__ float rbf(float x) { return lo2f(pk(x, x)); }
__device__ __forceinline__ f32x4 mfma(v4u a, v4u b, f32x4 c) {
  return __builtin_amdgcn_mfma_f32_16x16x32_bf16(__builtin_bit_cast(s16x8, a), __builtin_bit_cast(s16x8, b), c, 0, 0, 0);
}
template <bool FIRST> __device__ __forceinline__ void mfv(f32x4& c, v4u a, v4u b) {
  if (FIRST) asm volatile("v_mfma_f32_16x16x32_bf16 %0, %1, %2, 0" : "=&v"(c) : "v"(a), "v"(b));
  else asm volatile("v_mfma_f32_16x16x32_bf16 %0, %1, %2, %0" : "+v"(c) : "v"(a), "v"(b));
}
template <bool FIRST> __device__ __forceinline__ void mfa(f32x4& c, v4u a, v4u b) {
  if (FIRST) asm volatile("v_mfma_f32_16x16x32_bf16 %0, %1, %2, 0" : "=&v"(c) : "a"(a), "v"(b));
  else asm volatile("v_mfma_f32_16x16x32_bf16 %0, %1, %2, %0" : "+v"(c) : "a"(a), "v"(b));
}
template <bool FIRST> __device__ __forceinline__ void mfb(f32x4& c, v4u a, v4u b) {
  if (FIRST) asm volatile("v_mfma_f32_16x16x32_bf16 %0, %1, %2, 0" : "=&v"(c) : "v"(a), "a"(b));
  else asm volatile("v_mfma_f32_16x16x32_bf16 %0, %1, %2, %0" : "+v"(c) : "v"(a), "a"(b));
}
__device__ __forceinline__ v2u trd(LAS char* lds, u32 off) {
  return __builtin_bit_cast(v2u, __builtin_amdgcn_ds_read_tr16_b64_v4i16((LAS s16x4*)(lds + off)));
}
__device__ __forceinline__ v4u mkrs(const void* p, u32 bytes) { u64 a = (u64)p; return (v4u){(u32)a, (u32)(a >> 32) & 0xffffu, bytes, 0x00020000u}; }
template <bool COH> __device__ __forceinline__ void dma(u32 lb, u32 lo, u32 voff, v4u rs, u32 sb, u32 so) {
  u32 t;
  if (COH) asm volatile("s_add_u32 m0, %1, %6\n\ts_add_u32 %0, %4, %5\n\tbuffer_load_dwordx4 %2, %3, %0 offen sc1 lds" : "=&s"(t) : "s"(lb), "v"(voff), "s"(rs), "s"(sb), "i"(so), "i"(lo) : "memory", "scc");
  else asm volatile("s_add_u32 m0, %1, %6\n\ts_add_u32 %0, %4, %5\n\tbuffer_load_dwordx4 %2, %3, %0 offen lds" : "=&s"(t) : "s"(lb), "v"(voff), "s"(rs), "s"(sb), "i"(so), "i"(lo) : "memory", "scc");
}
template <int CTRL, int RM> __device__ __forceinline__ float dppadd(float v) {
  return v + __builtin_bit_cast(float, __builtin_amdgcn_update_dpp(0, __builtin_bit_cast(int, v), CTRL, RM, 0xf, true));
}
__device__ __forceinline__ float hsum(float v) {
  v = dppadd<0x111, 0xf>(v); v = dppadd<0x112, 0xf>(v); v = dppadd<0x114, 0xf>(v); v = dppadd<0x118, 0xf>(v);
  return dppadd<0x142, 0xa>(v);
}
#define STAMP(i) do { if ((i) == 0) { u64 t0_; asm volatile("s_memrealtime %0\n\ts_waitcnt lgkmcnt(0)" : "=s"(t0_)::"memory"); pt0 = t0_; } if ((i) == PK) { u64 t_; asm volatile("s_memrealtime %0\n\ts_waitcnt lgkmcnt(0)" : "=s"(t_)::"memory"); ptk = t_; } } while (0)
constexpr int PK = 0;
template <int V> struct IC { static constexpr int value = V; };
template <int B, int E, class Fn> __device__ __forceinline__ void sfor(Fn&& f) { if constexpr (B < E) { f(IC<B>{}); sfor<B + 1, E>(f); } }
constexpr int cv_a0(int u) { return u < 16 ? u >> 2 : ((u - 16) >> 2) > 4 ? (u - 16) >> 2 : 4; }
constexpr int cv_first(int u) { int n = 0; for (int j = 0; j < u; ++j) n += (j < 16 ? 4 : 8) - cv_a0(j); return n; }
constexpr int cv_u(int n) { int u = 0; while (cv_first(u + 1) <= n) ++u; return u; }
constexpr int cv_slot(int i) { int s_ = cv_first(i < 16 ? i : i + 16) - 12; if (i < 31) { const int v = cv_slot(i + 1) - 2; if (v < s_) s_ = v; } return s_ & ~1; }
constexpr int cv_at(int n) { for (int i = 0; i < 32; ++i) if (cv_slot(i) == n) return i; return -1; }
constexpr int w2_n(int g) { return g >= 27 ? 0 : (g & 15) < 11 ? ((g & 1) ^ 1) : 3; }
constexpr int W2_EPI = 56 - 27 - NWE;
constexpr int w2_first(int g) { int n = NWE + (g > 15 ? W2_EPI : 0); for (int j = 0; j < g; ++j) n += w2_n(j); return n; }
constexpr int a_n(int g) { return g >= 28 ? 4 : 0; }
constexpr int vm_tail(int g) { return (g == 4) + ((g & 15) == 5) + 2 * ((g & 15) == 12) + (g == 13) + (g == 19) + (g == 25) + 2 * (g == 27); }
constexpr int vm_ops(int g) { return w2_n(g) + a_n(g) + ((g & 15) < 11 ? 2 : 0) + vm_tail(g); }
constexpr int vm_sum(int a, int e) { int n = 0; for (int j = a; j < e; ++j) n += vm_ops(j); return n; }
constexpr int vm_pre(int ps) { return ps ? W2_EPI + 4 : 1; }
constexpr int h_wait(int g) {
  return (g & 15) < 4 ? 2 * (3 - (g & 15)) + vm_pre(g >> 4) + vm_sum(g & 16, g) : vm_tail(g - 4) + vm_sum(g - 3, g);
}
static_assert(w2_first(32) == 56 && W2_EPI >= 0 && W2_EPI <= 14, "all w_down fragments are requested");

__global__ void __launch_bounds__(256, 1) fused(Args A) {
  __shared__ __attribute__((aligned(16))) char smem_[LDS_BYTES];
  LAS char* lds = (LAS char*)smem_;
  const u32 lds0 = (u32)(uintptr_t)lds;
  u64 pt0, ptk;
  STAMP(0);
  ptk = pt0;
  const int b = blockIdx.x, x = b & 7, y = b >> 3;
  const int l = threadIdx.x & 63, w = __builtin_amdgcn_readfirstlane(threadIdx.x >> 6);
  const int n16 = l & 15, q = l >> 4, rr = (l >> 2) & 3, p4 = l & 3;
  u32 pfd;
  {
    u64 pc;
    asm volatile("s_getpc_b64 %0" : "=s"(pc));
    constexpr u32 CODE = 36864;
    const u32 co = PFM == 0 ? (u32)((w == 0 ? 0 : 1 + (y + w) % 4) * 8192 + l * 128) : l < 48 ? (u32)(l * 128) : (u32)(6144 + (((y & 7) * 4 + w) * 8 + (l & 7)) * 128);
    asm volatile("global_load_dword %0, %1, %2" : "=&v"(pfd) : "v"(co < CODE ? co : 0u), "s"(pc & ~3ull) : "memory");
  }
  const __amdgpu_buffer_rsrc_t WS = __builtin_amdgcn_make_buffer_rsrc(A.ws, 0, WS_BYTES, 0x00020000);
  const v4u rWS = mkrs(A.ws, WS_BYTES);
  auto spin = [&](u32 off, u32 want) {
    for (u32 sp = 0; sp < SPIN_CAP; ++sp) {
      asm volatile("" ::: "memory");
      if (__all(__builtin_amdgcn_raw_buffer_load_b32(WS, off, 0, SC1) == want)) break;
    }
  };

  const int ti = 4 * x + (y >> 3), cj = y & 7;
  const int cA = 64 * cj + 16 * w + n16;
  const int srow = 256 * x + 8 * y + 2 * w;
  const int ch = w >> 1, par2 = w & 1, cB = 64 * x + 2 * y + ch;
  v2u shk; v4u sx0, sx1, kz[2];
  v2u kedge; u32 vtag, vdp;
  u32 l16 = l * 16;
  {
    const v4u rK = mkrs(A.k, S * D * 2), rV = mkrs(A.v, S * D * 2), rQ = mkrs(A.q, S * D * 2);
    const u32 c0 = ((l & 7) ^ ((l >> 3) & 2)) * 16, tv = (u32)((64 * ti + (l >> 3)) * 1024 + 128 * cj);
    const u32 va = tv + c0, vb = tv + (c0 ^ 64);
    const u32 vk = ((w & 1) ? vb : va) + (u32)(w * 8192 - 8192);
    dma<false>(lds0 + w * 1024, L_K, vk, rK, 0, 0);
    dma<false>(lds0 + w * 1024, L_K + 4096, vk + 32768, rK, 0, 0);
    if (w == 0) dma<false>(lds0, L_K + 8192, vk + 65536, rK, 0, 0);
    dma<false>(lds0 + w * 2048, L_V, va, rV, w * 16384, 0);
    dma<false>(lds0 + w * 2048, L_V + 1024, vb, rV, w * 16384, 8192);
    asm volatile("global_load_dwordx2 %0, %1, %2" : "=&v"(shk) : "v"(cA * 8), "s"(A.shk) : "memory");
    asm volatile("global_load_dwordx4 %0, %2, %3\n\tglobal_load_dwordx4 %1, %2, %3 offset:1024" : "=&v"(kz[0]), "=&v"(kz[1]) : "v"(l16), "s"(A.ssk + cB * S + 1024 * par2) : "memory");
    asm volatile("global_load_dwordx2 %0, %3, %4 offset:2048\n\tglobal_load_dword %1, %3, %5\n\tglobal_load_dword %2, %3, %6"
                 : "=&v"(kedge), "=&v"(vtag), "=&v"(vdp) : "v"(0), "s"(A.ssk + cB * S), "s"(A.ws + O_EP), "s"(A.ssd + 64 * x + 2 * y) : "memory");
    dma<false>(lds0 + w * 2048, L_Q, va, rQ, w * 16384, 0);
    dma<false>(lds0 + w * 2048, L_Q + 1024, vb, rQ, w * 16384, 8192);
    asm volatile("global_load_dwordx4 %0, %2, %3\n\tglobal_load_dwordx4 %1, %2, %3 offset:512" : "=&v"(sx0), "=&v"(sx1) : "v"((l >> 5) * 1024 + (l & 31) * 16), "s"(A.q + srow * D) : "memory");
  }
  const int rb = 8 * x + 2 * (y >> 3) + ((y >> 2) & 1), fg = y & 3;
  const v4u rW = mkrs(ch ? A.wu : A.wg, D * F * 2);
  u32 wrv = l < 56 ? l16 : 0xffffff00u, w1d[4];
  LAS char* pW = lds + L_WR + ((p4 >> 1) * 8 + rr) * WR_S + (4 * w + q) * 16 + (p4 & 1) * 8;
#pragma unroll
  for (int j = 0; j < 4; ++j) {
    int Z = 16 * j + 4 * w + q;
    asm volatile("" : "+v"(Z));
    const int c = (Z * 37) >> 8;
    w1d[j] = (j < 3 || Z < 56) ? O_W1F + (u32)(((8 * fg + c) * 16 + (rb >> 2)) * 7168 + (Z - 7 * c) * 1024 + (rb & 3) * 256 + (4 * ((n16 & 7) >> 1) + 2 * (n16 >> 3) + (n16 & 1)) * 16) : 0xffffff00u;
  }
  const u32 aAo = (u32)((8 * q + rr) * 128 + (((2 * w + (p4 >> 1)) ^ (rr & 2) ^ ((q & 1) << 2)) << 4) + (p4 & 1) * 8);
  LAS char* pA = lds + L_K + aAo;
  LAS char* pB = lds + L_K + (aAo ^ 64);
  u32 okv = O_KVT + cA * 4096 + (2040 - 32 - 64 * ti - 8 * q) * 2, oqt = O_QT + cA * 4096 + (64 * ti + 8 * q) * 2;
  LAS char* tw = lds + L_TZ + ch * TZ_C + (128 * par2 + l) * 16 + 32;
  u32 og = (l & 31) == 31 ? O_ST1 + (srow + (l >> 5)) * 8 : 0xffffff00u;
  u32 oci = (x == 0 && y < 8 && w == 0 && l < 24) ? O_C2 + l * 2048 + y * 256 : 0xffffff00u;
  asm volatile("" : "+v"(w1d[0]), "+v"(w1d[1]), "+v"(w1d[2]), "+v"(w1d[3]), "+v"(pA), "+v"(pB), "+v"(okv), "+v"(oqt), "+v"(tw), "+v"(og), "+v"(wrv), "+v"(pW), "+v"(oci));
  const int wU = b < 224 ? 2 * b + ch : 0;
  u32 ow[4], w2o = b < 224 ? O_W2T + (u32)(((wU & 7) * 56 + (wU >> 3)) * 4096 + par2 * 2048) + l16 : 0xffff0000u;
  {
    const int UU = 4 * (wU >> 3) + q, f = UU / 7, u = UU - 7 * f;
    const int col = 64 * (wU & 7) + 8 * rr + 4 * (p4 & 1) + 32 * (p4 >> 1) + 2 * par2;
#pragma unroll
    for (int d = 0; d < 4; ++d) ow[d] = (u32)(((56 * f + (u < 4 ? 2 * u + 8 * d : 8 * u + 2 * d)) * D + col) * 2);
  }
  asm volatile("" : "+v"(w2o), "+v"(ow[0]), "+v"(ow[1]), "+v"(ow[2]), "+v"(ow[3]));
  if (w == 0) *(LAS v4u*)(lds + L_XR + (l >> 5) * XR_S + 4096 + (l & 31) * 16) = (v4u){0, 0, 0, 0};
  SB();
  STAMP(23);
  asm volatile("s_waitcnt vmcnt(9)" : "+v"(shk), "+v"(pfd)::"memory");
  BAR();
  STAMP(1);
  u32 tag, dpair;
  {
    v2u ka[2], kb[2], kc[2], va[2], vb[2];
#pragma unroll
    for (int it = 0; it < 2; ++it) { ka[it] = trd(pA, it * 4096 + 512); kb[it] = trd(pB, it * 4096 + 1024); kc[it] = trd(pB, it * 4096 + 1536); }
#pragma unroll
    for (int it = 0; it < 2; ++it) { va[it] = trd(pA, L_V - L_K + it * 4096); vb[it] = trd(pA, L_V - L_K + it * 4096 + 512); }
    SB();
#pragma unroll
    for (int j = 0; j < 4; ++j) dma<false>(lds0 + (8 * ch + 4 * par2) * WR_S, L_WR + j * WR_S, wrv, rW, (8 * rb + 4 * par2) * (F * 2) + fg * 896, j * (F * 2));
    SB();
    auto LO = [](u32 a, u32 c) { return (f32x2){lo2f(a), lo2f(c)}; };
    auto HI = [](u32 a, u32 c) { return (f32x2){hi2f(a), hi2f(c)}; };
    const f32x2 w0 = LO(shk.x, shk.x), w1 = HI(shk.x, shk.x), w2 = LO(shk.y, shk.y), w3 = HI(shk.y, shk.y);
    const f32x2 e[12] = {w0, HI(ka[0].x, ka[1].x), LO(ka[0].y, ka[1].y), HI(ka[0].y, ka[1].y), LO(kb[0].x, kb[1].x), HI(kb[0].x, kb[1].x), LO(kb[0].y, kb[1].y),
                         HI(kb[0].y, kb[1].y), LO(kc[0].x, kc[1].x), HI(kc[0].x, kc[1].x), LO(kc[0].y, kc[1].y), HI(kc[0].y, kc[1].y)};
    const f32x2 vv[8] = {LO(va[0].x, va[1].x), HI(va[0].x, va[1].x), LO(va[0].y, va[1].y), HI(va[0].y, va[1].y), LO(vb[0].x, vb[1].x), HI(vb[0].x, vb[1].x), LO(vb[0].y, vb[1].y), HI(vb[0].y, vb[1].y)};
    f32x2 kv[8];
#pragma unroll
    for (int s = 0; s < 8; ++s) kv[s] = (w3 * e[s + 1] + w2 * e[s + 2] + w1 * e[s + 3] + w0 * e[s + 4]) * vv[s];
#pragma unroll
    for (int it = 0; it < 2; ++it)
      __builtin_amdgcn_raw_buffer_store_b128((v4u){pk(kv[7][it], kv[6][it]), pk(kv[5][it], kv[4][it]), pk(kv[3][it], kv[2][it]), pk(kv[1][it], kv[0][it])}, WS, okv + (1 - it) * 64, 0, SC1);
    SB();
    asm volatile("s_waitcnt vmcnt(8)" : "+v"(kz[0]), "+v"(kz[1]), "+v"(kedge), "+v"(vtag), "+v"(vdp)::"memory");
    BAR();
    vtag += 1u;
    tag = __builtin_amdgcn_readfirstlane(vtag); dpair = __builtin_amdgcn_readfirstlane(vdp);
    v2u qa[2], qb[2];
#pragma unroll
    for (int it = 0; it < 2; ++it) { qa[it] = trd(pA, L_Q - L_K + it * 4096); qb[it] = trd(pA, L_Q - L_K + it * 4096 + 512); }
#pragma unroll
    for (int it = 0; it < 2; ++it) __builtin_amdgcn_raw_buffer_store_b128((v4u){qa[it].x, qa[it].y, qb[it].x, qb[it].y}, WS, oqt + it * 64, 0, SC1);
    __builtin_amdgcn_raw_buffer_store_b32(tag * 64u, WS, oci, 0, SC1);
  }
  SB();
  STAMP(24);
  {
    auto put = [&](LAS char* p, v4u d, u32 d4, u32 d5) {
      const u32 e0 = __builtin_amdgcn_alignbit(d.y, d.x, 16), e1 = __builtin_amdgcn_alignbit(d.z, d.y, 16), e2 = __builtin_amdgcn_alignbit(d.w, d.z, 16),
                e3 = __builtin_amdgcn_alignbit(d4, d.w, 16), e4 = __builtin_amdgcn_alignbit(d5, d4, 16);
      *(LAS v4u*)p = d; *(LAS v4u*)(p + TZ_S) = (v4u){e0, e1, e2, e3}; *(LAS v4u*)(p + 2 * TZ_S) = (v4u){d.y, d.z, d.w, d4}; *(LAS v4u*)(p + 3 * TZ_S) = (v4u){e1, e2, e3, e4};
    };
    auto nxt = [](u32 last, u32 v) { return (u32)__builtin_amdgcn_update_dpp((int)last, (int)v, 0x130, 0xf, 0xf, false); };
    put(tw, kz[0], nxt(__builtin_amdgcn_readlane(kz[1].x, 0), kz[0].x), nxt(__builtin_amdgcn_readlane(kz[1].y, 0), kz[0].y));
    put(tw + 1024, kz[1], nxt(par2 ? 0u : kedge.x, kz[1].x), nxt(par2 ? 0u : kedge.y, kz[1].y));
    const u32 f0 = __builtin_amdgcn_readlane(kz[0].x, 0), f1 = __builtin_amdgcn_readlane(kz[0].y, 0);
    if (l < 2 && !par2) put(tw - 32, (v4u){0, 0, 0, 0}, l ? f0 : 0u, l ? f1 : 0u);
  }
  SB();
  asm volatile("s_waitcnt vmcnt(5)" : "+v"(sx0), "+v"(sx1)::"memory");
  BAR();
  {
    v2u ta[4], tc[4];
#pragma unroll
    for (int j = 0; j < 4; ++j) { ta[j] = trd(pW, j * 256); tc[j] = trd(pW, j * 256 + 4 * WR_S); }
#pragma unroll
    for (int j = 0; j < 4; ++j) __builtin_amdgcn_raw_buffer_store_b128((v4u){ta[j].x, ta[j].y, tc[j].x, tc[j].y}, WS, w1d[j], 0, SC1);
  }
  SB();
  {
    const f32x2 v[8] = {{lo2f(sx0.x), hi2f(sx0.x)}, {lo2f(sx0.y), hi2f(sx0.y)}, {lo2f(sx0.z), hi2f(sx0.z)}, {lo2f(sx0.w), hi2f(sx0.w)},
                        {lo2f(sx1.x), hi2f(sx1.x)}, {lo2f(sx1.y), hi2f(sx1.y)}, {lo2f(sx1.z), hi2f(sx1.z)}, {lo2f(sx1.w), hi2f(sx1.w)}};
    const f32x2 s2 = ((v[0] + v[1]) + (v[2] + v[3])) + ((v[4] + v[5]) + (v[6] + v[7]));
    const float sm = hsum(s2[0] + s2[1]);
    const float m0 = u2f(__builtin_amdgcn_readlane(f2u(sm), 31)), m1 = u2f(__builtin_amdgcn_readlane(f2u(sm), 63));
    const float mean = (l < 32 ? m0 : m1) * (1.f / D);
    f32x2 ss = {0.f, 0.f};
#pragma unroll
    for (int j = 0; j < 8; ++j) { const f32x2 d = v[j] - mean; ss += d * d; }
    const float var = hsum(ss[0] + ss[1]) * (1.f / D);
    const u32 pay = pk(mean, __builtin_amdgcn_sqrtf(rbf(rbf(var) + 1e-5f)));
    __builtin_amdgcn_raw_buffer_store_b64((v2u){pay, vtag}, WS, og, 0, SC1);
  }
  SB();
  WAITV(1);
  BAR();
  STAMP(2);
  if (w == 0 && l == 0) __builtin_amdgcn_raw_buffer_store_b32(tag, WS, O_F1 + (cj * 32 + ti) * 128, 0, SC1);
  u32 w2raw[8];
  asm volatile("global_load_dword %0, %8, %12\n\tglobal_load_dword %1, %8, %12 offset:1024\n\tglobal_load_dword %2, %9, %12\n\tglobal_load_dword %3, %9, %12 offset:1024\n\t"
               "global_load_dword %4, %10, %12\n\tglobal_load_dword %5, %10, %12 offset:1024\n\tglobal_load_dword %6, %11, %12\n\tglobal_load_dword %7, %11, %12 offset:1024"
               : "=&v"(w2raw[0]), "=&v"(w2raw[1]), "=&v"(w2raw[2]), "=&v"(w2raw[3]), "=&v"(w2raw[4]), "=&v"(w2raw[5]), "=&v"(w2raw[6]), "=&v"(w2raw[7])
               : "v"(ow[0]), "v"(ow[1]), "v"(ow[2]), "v"(ow[3]), "s"(A.wd) : "memory");
  v4u T[32];
  {
    const u32 tb = lds0 + L_TZ + ch * TZ_C + ((n16 + 1) & 3) * TZ_S + (u32)((((n16 + 1) >> 2) + 2 * q + 8 * par2) * 8);
#pragma unroll
    for (int i = 0; i < 32; ++i) {
      v2u a, c;
      asm volatile("ds_read_b64 %0, %2 offset:%3\n\tds_read_b64 %1, %2 offset:%4" : "=&v"(a), "=&v"(c) : "v"(tb), "i"(128 * i), "i"(128 * i + 8) : "memory");
      T[i] = (v4u){a.x, a.y, c.x, c.y};
    }
    asm volatile("s_waitcnt lgkmcnt(0)" : "+v"(T[0]), "+v"(T[1]), "+v"(T[2]), "+v"(T[3]), "+v"(T[4]), "+v"(T[5]), "+v"(T[6]), "+v"(T[7]), "+v"(T[8]), "+v"(T[9]), "+v"(T[10]),
                 "+v"(T[11]), "+v"(T[12]), "+v"(T[13]), "+v"(T[14]), "+v"(T[15])::"memory");
    asm volatile("" : "+v"(T[16]), "+v"(T[17]), "+v"(T[18]), "+v"(T[19]), "+v"(T[20]), "+v"(T[21]), "+v"(T[22]), "+v"(T[23]), "+v"(T[24]), "+v"(T[25]), "+v"(T[26]),
                 "+v"(T[27]), "+v"(T[28]), "+v"(T[29]), "+v"(T[30]), "+v"(T[31])::"memory");
  }
  const int tB = 512 * par2 + 16 * n16 + 4 * q;
  LAS char* xp = lds + L_XR + ch * XR_S + (2032 - 16 * n16 + 32 * par2 + 8 * q) * 2 - 3584;
  LAS char* kp = lds + L_XR + ch * XR_S + (2044 - 1280 - tB) * 2;
  LAS char* gp = lds + L_S1 + tB * 8;
  LAS char* ep = lds + L_EX + w * 4096 + l * 16;
  LAS char* er = lds + L_EX + (w ^ 1) * 4096 + l * 16;
  u32 qo = O_QT + cB * 4096 + tB * 2;
  u32 oh = O_HT + (u32)((8 * (2 * par2 + (q & 1)) + (n16 >> 1)) * 32768 + (cB >> 5) * 2048 + (cB & 31) * 64 + (((2 * (n16 & 1) + (q >> 1)) ^ (2 * ((cB >> 3) & 1))) << 4));
  asm volatile("" : "+v"(xp), "+v"(kp), "+v"(gp), "+v"(ep), "+v"(er), "+v"(qo), "+v"(oh), "+v"(l16));
  const int hp = 4 * (x >> 2) + (y >> 3), fch = 8 * (x & 3) + (y & 7);
  const int ct = y & 7, rbl = y >> 3;
  const u32 ring = L_R + w * 12288;
  const int rot = fch & 3, spar = (fch >> 2) & 1;
  u32 w2v = O_W2T + (u32)((ct * 56 + 14 * w) * 4096) + l16;
  u32 ost = (fch < 8 && rr == q) ? O_ST2 + (u32)(rot * 32768 + (128 * hp + 32 * w + 16 * spar + n16) * 16) : 0xffff0000u;
  u32 og2 = O_ST2 + (u32)((l >> 5) * 65536 + (128 * hp + 32 * w + (l & 31)) * 16);
  u32 oact = O_ACT + (u32)(((8 * hp + 2 * w) * 224 + 7 * fch + q) * 256 + n16 * 16), oact2 = q < 3 ? oact + 1024 : 0xff000000u;
  u32 of3 = O_F3 + (u32)(((x * 4 + rbl) * 4 + w) * 128 + (l & 7) * 4), of3w = l == 0 ? O_F3 + (u32)(((hp * 4 + w) * 4 + (fch >> 3)) * 128 + (fch & 7) * 4) : 0xffff0000u;
  u32 av = O_ACT + (u32)(((8 * x + 2 * rbl) * 224 + 56 * w) * 256) + l16;
  LAS char* hx[2];
#pragma unroll
  for (int pr = 0; pr < 2; ++pr) hx[pr] = lds + ring + (8 * q + rr) * 64 + (((2 * (pr ^ spar) + (p4 >> 1)) ^ (2 * (q & 1))) << 4) + (p4 & 1) * 8;
  LAS char* pcs = lds + ring + 10240 + (n16 == 0 ? q * 16 : 128 + l * 16);
  LAS char* pcr = lds + L_R + 10240 + q * 16;
  asm volatile("" : "+v"(av), "+v"(pcs), "+v"(pcr), "+v"(w2v), "+v"(ost), "+v"(og2), "+v"(oact), "+v"(oact2), "+v"(of3), "+v"(of3w), "+v"(hx[0]), "+v"(hx[1]));
  STAMP(21);
  if (w == 0) spin(O_F1 + ((x ^ (4 * (l >> 5))) * 32 + (l & 31)) * 128, tag);
  BAR();
  STAMP(3);

  dma<true>(lds0 + ch * XR_S + par2 * 2048, L_XR, l16, rWS, cB * 4096 + par2 * 2048, O_KVT);
  dma<true>(lds0 + ch * XR_S + par2 * 2048, L_XR + 1024, l16, rWS, cB * 4096 + par2 * 2048, O_KVT + 1024);
#pragma unroll
  for (int i = 0; i < 4; ++i) dma<true>(lds0 + w * 4096, L_S1 + i * 1024, l16, rWS, w * 4096, O_ST1 + i * 1024);
  v2u xq[4];
  asm volatile("buffer_load_dwordx2 %0, %4, %5, 0 offen sc1\n\tbuffer_load_dwordx2 %1, %4, %5, 0 offen offset:512 sc1\n\t"
               "buffer_load_dwordx2 %2, %4, %5, 0 offen offset:2048 sc1\n\tbuffer_load_dwordx2 %3, %4, %5, 0 offen offset:2560 sc1"
               : "=&v"(xq[0]), "=&v"(xq[1]), "=&v"(xq[2]), "=&v"(xq[3]) : "v"(qo), "s"(rWS) : "memory");
  SB();
  asm volatile("s_waitcnt vmcnt(8)" : "+v"(w2raw[0]), "+v"(w2raw[1]), "+v"(w2raw[2]), "+v"(w2raw[3]), "+v"(w2raw[4]), "+v"(w2raw[5]), "+v"(w2raw[6]), "+v"(w2raw[7])::"memory");
  BAR();
  STAMP(4);
  v4u W2[56];
  auto w2l = [&](int i) { W2[i] = __builtin_amdgcn_raw_buffer_load_b128(WS, w2v + (i & 3) * 1024, (i >> 2) * 4096, SC1); };
  u32 hpk[4];
  GAS u32* pc2 = (GAS u32*)(A.ws + O_C2 + (l & 7) * 2048 + x * 256);
  auto put_h = [&](int h) {
    const auto s0 = __builtin_amdgcn_permlane16_swap(hpk[0], hpk[2], false, false);
    const auto s1 = __builtin_amdgcn_permlane16_swap(hpk[1], hpk[3], false, false);
    __builtin_amdgcn_raw_buffer_store_b128((v4u){s0[0], s1[0], s0[1], s1[1]}, WS, oh + h * (32 * 32768), 0, SC1);
  };
  const u32 oc2 = O_C2 + x * 2048 + (l & 7) * 256, c2want = tag * 64u + 32u;
  {
    u32 w1so, flw;
    auto w1l = [&](int i) {
      if ((i & 3) == 0) asm volatile("s_add_u32 m0, %1, %2\n\ts_add_u32 %0, %3, %4\n\tbuffer_load_dwordx4 %5, %6, %0 offen sc1 lds"
                                     : "=&s"(w1so) : "s"(lds0 + w * 28672), "i"(i * 1024), "s"(fch * 114688 + w * 28672), "i"(O_W1F + i * 1024), "v"(l16), "s"(rWS) : "memory", "scc");
      else asm volatile("buffer_load_dwordx4 %0, %1, %2 offen offset:%3 sc1 lds" ::"v"(l16), "s"(rWS), "s"(w1so), "i"((i & 3) * 1024) : "memory");
    };
    const float dsc = ch ? hi2f(dpair) : lo2f(dpair);
    asm volatile("" : "+v"(pc2));
    f32x4 cacc[8], mn[2], ot[2];
    v4u B[32], g1[8], w2f[2]; v2u kr[4];
    float xf[16], ln[16], kd[16], tm[16], hh[8];
    auto fix = [&](int h) {
      u32 df = 0;
#pragma unroll
      for (int i = 4 * h; i < 4 * h + 4; ++i) df |= (g1[i].y ^ vtag) | (g1[i].w ^ vtag);
      if (__builtin_amdgcn_ballot_w64(df != 0)) {
        bool ok;
        for (u32 sp = 0; sp < SPIN_CAP; ++sp) {
          asm volatile("" ::: "memory");
          ok = true;
#pragma unroll
          for (int i = 4 * h; i < 4 * h + 4; ++i) {
            g1[i] = __builtin_amdgcn_raw_buffer_load_b128(WS, O_ST1 + (tB + 256 * ((i >> 1) & 1) + 1024 * h) * 8 + (i & 1) * 16, 0, SC1);
            ok &= (g1[i].y == tag) & (g1[i].w == tag);
          }
          if (__all(ok)) break;
        }
#pragma unroll
        for (int j = 8 * h; j < 8 * h + 8; ++j) { const u32 pay = g1[j >> 1][(j & 1) * 2]; ln[j] = (xf[j] - lo2f(pay)) * __builtin_amdgcn_rcpf(hi2f(pay)); }
      }
    };
    auto give = [&](int h) {
      if (par2) { *(LAS f32x4*)(ep + 2048 * h) = cacc[4 * h]; *(LAS f32x4*)(ep + 2048 * h + 1024) = cacc[4 * h + 1]; mn[0] = cacc[4 * h + 2]; mn[1] = cacc[4 * h + 3]; asm volatile("" : "+v"(mn[0]), "+v"(mn[1])); }
      else { *(LAS f32x4*)(ep + 2048 * h) = cacc[4 * h + 2]; *(LAS f32x4*)(ep + 2048 * h + 1024) = cacc[4 * h + 3]; mn[0] = cacc[4 * h]; mn[1] = cacc[4 * h + 1]; asm volatile("" : "+v"(mn[0]), "+v"(mn[1])); }
    };
    auto pre = [&](auto J_, auto P_) {
      constexpr int j = decltype(J_)::value, ph = decltype(P_)::value;
      const u32 pay = g1[j >> 1][(j & 1) * 2];
      if constexpr (ph == 0) { xf[j] = (j & 1) ? hi2f(xq[j >> 2][(j & 3) >> 1]) : lo2f(xq[j >> 2][(j & 3) >> 1]); asm volatile("" : "+v"(xf[j])); }
      if constexpr (ph == 1) { tm[j] = __builtin_amdgcn_rcpf(hi2f(pay)); asm volatile("" : "+v"(tm[j])); }
      if constexpr (ph == 2) { ln[j] = xf[j] - lo2f(pay); asm volatile("" : "+v"(ln[j])); }
      if constexpr (ph == 3) { ln[j] *= tm[j]; asm volatile("" : "+v"(ln[j])); }
      if constexpr (ph == 4) { const u32 kk = kr[j >> 2][1 - ((j & 3) >> 1)]; kd[j] = (j & 1) ? lo2f(kk) : hi2f(kk); asm volatile("" : "+v"(kd[j])); }
    };
    sfor<0, 32>([&](auto U_) { constexpr int u = decltype(U_)::value; if constexpr (cv_slot(u) < 0) B[u] = *(LAS v4u*)(xp + 128 * (31 - u)); });
    SB();
    sfor<0, 144>([&](auto N_) {
      constexpr int n = decltype(N_)::value, u = cv_u(n), i = u < 16 ? u : u - 16, a = cv_a0(u) + n - cv_first(u), eps = 4 * a - i + 3;
      mfb<i == 0>(cacc[a], T[eps], B[i]);
      SB();
      constexpr int ub = cv_at(n);
      if constexpr (ub >= 0) B[ub] = *(LAS v4u*)(xp + 128 * (31 - ub));
      if constexpr (n < 112 && n % 4 == 1) w1l(n / 4);
      if constexpr (n == 2 || n == 3) {
        constexpr int pp = n - 2;
#pragma unroll
        for (int d = 0; d < 4; ++d) w2f[pp][d] = __builtin_amdgcn_perm(w2raw[2 * d + 1], w2raw[2 * d], pp ? 0x07060302u : 0x05040100u);
        __builtin_amdgcn_raw_buffer_store_b128(w2f[pp], WS, w2o + pp * 1024, 0, SC1);
      }
      if constexpr (n >= 11 && n < 27 && n % 4 == 3) { constexpr int aa = (n - 11) / 4; kr[aa] = *(LAS v2u*)(kp + 2560 - 512 * (aa & 1) - 2048 * (aa >> 1)); }
      if constexpr (n >= 23 && n < 39 && n % 2 == 1) pre(IC<(n - 23) / 2>{}, IC<4>{});
      if constexpr (n == 43) STAMP(25);
      if constexpr (n == 46) STAMP(26);
      if constexpr (n == 67) STAMP(27);
      if constexpr (n == 111) STAMP(28);
      if constexpr (n == 113) STAMP(22);
      if constexpr (n == 44) give(0);
      if constexpr (n == 45) {
        asm volatile("s_waitcnt vmcnt(11) lgkmcnt(0)" : "+v"(xq[0]), "+v"(xq[1]), "+v"(xq[2]), "+v"(xq[3])::"memory");
        __builtin_amdgcn_s_barrier();
        asm volatile("" ::: "memory");
        if (w == 0 && l < 8) __hip_atomic_fetch_add(pc2 + 8192, 1u, RLX);
      }
      if constexpr (n == 46 || n == 47) { g1[2 * (n - 46)] = *(LAS v4u*)(gp + (n - 46) * 2048); g1[2 * (n - 46) + 1] = *(LAS v4u*)(gp + (n - 46) * 2048 + 16); }
      if constexpr (n == 48) { ot[0] = *(LAS f32x4*)er; ot[1] = *(LAS f32x4*)(er + 1024); }
      if constexpr (n >= 50 && n < 58) { pre(IC<n - 50>{}, IC<0>{}); pre(IC<n - 50>{}, IC<1>{}); pre(IC<n - 50>{}, IC<2>{}); if constexpr (n > 50) pre(IC<n - 51>{}, IC<3>{}); }
      if constexpr (n == 58) { pre(IC<7>{}, IC<3>{}); fix(0); }
      if constexpr (n >= 59 && n < 63) {
#pragma unroll
        for (int j = 2 * (n - 59); j < 2 * (n - 59) + 2; ++j) { hh[j] = xf[j] + ln[j] * ((kd[j] * dsc + mn[j >> 2][j & 3]) + ot[j >> 2][j & 3]); asm volatile("" : "+v"(hh[j])); }
      }
      if constexpr (n == 63 || n == 64) { hpk[2 * (n - 63)] = pk(hh[4 * (n - 63)], hh[4 * (n - 63) + 1]); hpk[2 * (n - 63) + 1] = pk(hh[4 * (n - 63) + 2], hh[4 * (n - 63) + 3]); }
      if constexpr (n == 66) put_h(0);
      if constexpr (n == 98) asm volatile("buffer_load_dword %0, %1, %2, 0 offen sc1" : "=&v"(flw) : "v"(oc2 + 32768), "s"(rWS) : "memory");
      if constexpr (n == 112) {
        asm volatile("s_waitcnt vmcnt(3)" : "+v"(flw)::"memory");
        __builtin_amdgcn_s_barrier();
        if (w == 0 && l < 8) __hip_atomic_fetch_add(pc2, 1u, RLX);
        if (!__all(flw == c2want)) spin(oc2 + 32768, c2want);
      }
      if constexpr (n > 112 && n % 2 == 1 && (n - 113) / 2 < NWE) w2l((n - 113) / 2);
      if constexpr (n >= 90 && n < 98 && n % 2 == 0) { constexpr int j = 4 + (n - 90) / 2; g1[j] = *(LAS v4u*)(gp + 8192 + ((j >> 1) & 1) * 2048 + (j & 1) * 16); }
      if constexpr (n >= 104) pre(IC<8 + (n - 104) / 5>{}, IC<(n - 104) % 5>{});
      SB();
    });
    asm volatile("s_nop 7" ::: "memory");
    SB();
    STAMP(5);
    give(1);
    fix(1);
    WAITV(NWE < 16 ? NWE : 16);
    BAR();
#pragma unroll
    for (int i = 16; i < NWE; ++i) w2l(i);
#pragma unroll
    for (int aa = 0; aa < 2; ++aa) {
      const f32x4 o = *(LAS f32x4*)(er + 2048 + aa * 1024);
#pragma unroll
      for (int i = 0; i < 4; ++i) hh[4 * aa + i] = xf[8 + 4 * aa + i] + ln[8 + 4 * aa + i] * ((kd[8 + 4 * aa + i] * dsc + mn[aa][i]) + o[i]);
      hpk[2 * aa] = pk(hh[4 * aa], hh[4 * aa + 1]); hpk[2 * aa + 1] = pk(hh[4 * aa + 2], hh[4 * aa + 3]);
    }
    asm volatile("" : "+v"(hpk[0]), "+v"(hpk[1]), "+v"(hpk[2]), "+v"(hpk[3]));
    STAMP(6);
  }

  v4u ones = {0x3f803f80u, 0x3f803f80u, 0x3f803f80u, 0x3f803f80u};
  LAS char* wp0 = lds + l * 16;
  asm volatile("" : "+v"(ones), "+v"(wp0));
  u32 sg[4];
#pragma unroll
  for (int g = 0; g < 4; ++g) sg[g] = (u32)((g + rot) & 3);
  LAS char* wp = wp0 + sg[0] * 28672;
  v4u wf[WD + 1], hf[2][2], g2[2];
#pragma unroll
  for (int i = 0; i < WD; ++i) wf[i] = *(LAS v4u*)(wp + i * 1024);
  STAMP(7);
  if (w == 0) spin(oc2, c2want);
  BAR();
  STAMP(8);
  f32x4 cs[7];
  u32 fl3 = 0, fl2 = 0;
  v4u AC[28][2];
  const u32 ao[2] = {(u32)((w & 1) * (224 * 256)), (u32)(((w & 1) ^ 1) * (224 * 256))};
  auto ald = [&](int u) {
#pragma unroll
    for (int c = 0; c < 2; ++c) { AC[u][c] = __builtin_amdgcn_raw_buffer_load_b128(WS, av + (u % 14 & 3) * 1024, ao[c] + (u % 14 >> 2) * 4096 + (u / 14) * (64 * 224 * 256), SC1); SB(); }
  };
  {
    u32 hso;
    auto hdma = [&](int g, int n) {
      if (n == 0) asm volatile("s_add_u32 m0, %1, %2\n\ts_add_u32 %0, %3, %4\n\tbuffer_load_dwordx4 %5, %6, %0 offen sc1 lds"
                               : "=&s"(hso) : "s"(lds0 + ring), "i"((g % 5) * 2048), "s"((4 * hp + w) * 32768 + sg[(g >> 2) & 3] * 8192), "i"(O_HT + (g >> 4) * (32 * 32768) + (g & 3) * 2048), "v"(l16), "s"(rWS) : "memory", "scc");
      else asm volatile("buffer_load_dwordx4 %0, %1, %2 offen offset:1024 sc1 lds" ::"v"(l16), "s"(rWS), "s"(hso) : "memory");
    };
    auto hfrag = [&](int g, int pr, int t) { return trd(hx[pr], (g % 5) * 2048 + t * 256); };
    f32x4 acc[7][2], sS, sQ;
    auto epilogue = [&](auto P_) {
      constexpr int ps = decltype(P_)::value;
      if (!__all((g2[0].y == tag) & (g2[0].w == tag) & (g2[1].y == tag) & (g2[1].w == tag))) {
        for (u32 sp = 0; sp < SPIN_CAP; ++sp) {
          asm volatile("" ::: "memory");
#pragma unroll
          for (int k = 0; k < 2; ++k) g2[k] = __builtin_amdgcn_raw_buffer_load_b128(WS, og2, k * 32768 + ps * 16384, SC1);
          if (__all((g2[0].y == tag) & (g2[0].w == tag) & (g2[1].y == tag) & (g2[1].w == tag))) break;
        }
      }
      const u32 s1 = f2u(u2f(g2[0].x) + u2f(g2[1].x)), s2 = f2u(u2f(g2[0].z) + u2f(g2[1].z));
      const auto h1 = __builtin_amdgcn_permlane32_swap(s1, s1, false, false), h2 = __builtin_amdgcn_permlane32_swap(s2, s2, false, false);
      const float mean = (u2f(h1[0]) + u2f(h1[1])) * (1.f / D);
      const float v2 = __builtin_fmaxf((u2f(h2[0]) + u2f(h2[1])) * (1.f / D) - mean * mean, 0.f) + 1e-5f;
      const u32 e0 = f2u(-mean), e1 = f2u(-1.44269504089f * __builtin_amdgcn_rsqf(v2)), e2 = f2u(v2);
      const auto b0 = __builtin_amdgcn_permlane16_swap(e0, e0, false, false), b1 = __builtin_amdgcn_permlane16_swap(e1, e1, false, false),
                 b2 = __builtin_amdgcn_permlane16_swap(e2, e2, false, false);
      v4u sa[2], sb[2];
#pragma unroll
      for (int pr = 0; pr < 2; ++pr) {
        const bool up = (pr ^ spar) != 0;
        const float nm = u2f(up ? b0[1] : b0[0]), k1 = u2f(up ? b1[1] : b1[0]), vv = u2f(up ? b2[1] : b2[0]);
        u32 d[8];
        f32x2 a[7], bb[7], e[7];
#pragma unroll
        for (int t = 0; t < 7; ++t) a[t] = (f32x2){acc[t][pr][0], acc[t][pr][1]} + nm * (f32x2){cs[t][0], cs[t][1]};
        SB();
#pragma unroll
        for (int t = 0; t < 7; ++t) e[t] = a[t] * k1;
        SB();
#pragma unroll
        for (int t = 0; t < 7; ++t) {
          e[t] = (f32x2){__builtin_amdgcn_exp2f(e[t][0]), __builtin_amdgcn_exp2f(e[t][1])};
          bb[t] = (f32x2){acc[t][pr][2], acc[t][pr][3]} + nm * (f32x2){cs[t][2], cs[t][3]};
          if (ps == 0 && 7 * (1 - pr) + t < W2_EPI) w2l(w2_first(16) - W2_EPI + 7 * (1 - pr) + t);
          if (ps == 1 && t < 3) ald(8 + 3 * pr + t);
          SB();
        }
#pragma unroll
        for (int t = 0; t < 7; ++t) e[t] = e[t] * vv + vv;
        SB();
#pragma unroll
        for (int t = 0; t < 7; ++t) { e[t] = (f32x2){__builtin_amdgcn_rcpf(e[t][0]), __builtin_amdgcn_rcpf(e[t][1])}; a[t] *= bb[t]; SB(); }
#pragma unroll
        for (int t = 0; t < 7; ++t) a[t] *= e[t];
        SB();
#pragma unroll
        for (int t = 0; t < 7; ++t) d[t] = pk(a[t][0], a[t][1]);
        sa[pr] = (v4u){d[0], d[1], d[2], d[3]};
        const auto x0 = __builtin_amdgcn_permlane32_swap(d[4], d[6], false, false), x1 = __builtin_amdgcn_permlane32_swap(d[5], d[5], false, false);
        const auto y0 = __builtin_amdgcn_permlane16_swap(x0[0], x1[0], false, false), y1 = __builtin_amdgcn_permlane16_swap(x0[1], x1[1], false, false);
        sb[pr] = (v4u){y0[0], y0[1], y1[0], y1[1]};
        asm volatile("" : "+v"(sa[pr]), "+v"(sb[pr]));
        SB();
      }
#pragma unroll
      for (int pr = 0; pr < 2; ++pr) {
        const u32 ro = (u32)((pr ^ spar) * (224 * 256) + ps * (64 * 224 * 256));
        __builtin_amdgcn_raw_buffer_store_b128(sa[pr], WS, oact + ro, 0, SC1);
        __builtin_amdgcn_raw_buffer_store_b128(sb[pr], WS, oact2 + ro, 0, SC1);
      }
      SB();
    };
    hdma(0, 0); hdma(0, 1); hdma(1, 0); hdma(1, 1);
    {
      LAS char* c0 = wp0 + w * 1024;
      LAS char* c1 = wp0 + (w == 3 ? 3 : w + 4) * 1024;
      LAS char* c2 = c0 + 57344;
      LAS char* c3 = c1 + 57344;
      asm volatile("" : "+v"(c0), "+v"(c1), "+v"(c2), "+v"(c3));
      auto crd = [&](int k, int hi) { return *(LAS v4u*)((k < 8 ? (hi ? c1 : c0) : (hi ? c3 : c2)) + (k & 7) * 7168); };
      v4u fa[16], fb[16]; f32x4 ca, cb;
#pragma unroll
      for (int k = 0; k < 4; ++k) { fa[k] = crd(k, 0); fb[k] = crd(k, 1); }
      SB();
      sfor<0, 16>([&](auto K_) {
        constexpr int k = decltype(K_)::value;
        mfv<k == 0>(ca, fa[k], ones); SB();
        if constexpr (k + 4 < 16) fa[k + 4] = crd(k + 4, 0);
        SB();
        mfv<k == 0>(cb, fb[k], ones); SB();
        if constexpr (k + 4 < 16) fb[k + 4] = crd(k + 4, 1);
        if constexpr (k >= 2 && k < 8) hdma(2 + (k - 2) / 2, k & 1);
        if constexpr (k == 12) put_h(1);
        SB();
      });
      WAITV(9);
#pragma unroll
      for (int pr = 0; pr < 2; ++pr) { const v2u a = hfrag(0, pr, 0), c = hfrag(0, pr, 1); hf[0][pr] = (v4u){a.x, a.y, c.x, c.y}; }
      asm volatile("s_nop 7" ::: "memory");
      *(LAS f32x4*)pcs = ca; *(LAS f32x4*)(pcs + 64) = cb;
    }
    SB();
    STAMP(20);
    sfor<0, 32>([&](auto G_) {
      constexpr int g = decltype(G_)::value, s = g & 15;
      v4u(&hc)[2] = hf[g & 1]; v4u(&hn)[2] = hf[(g + 1) & 1];
      LAS char* wn = wp;
      if constexpr ((g & 3) == 3 && g < 31) wn = wp0 + sg[((g + 1) >> 2) & 3] * 28672;
      v2u ha;
      sfor<0, 7>([&](auto T_) {
        constexpr int t = decltype(T_)::value, it = g * 7 + t, nx = it + WD, ng = nx / 7, nt = nx % 7;
        if constexpr (t == 0) asm volatile("" : "+v"(wf[it % 8]), "+v"(wf[(it + 1) % 8]), "+v"(wf[(it + 2) % 8]), "+v"(wf[(it + 3) % 8]), "+v"(hc[0]), "+v"(hc[1]));
        if constexpr (t == 4) asm volatile("" : "+v"(wf[it % 8]), "+v"(wf[(it + 1) % 8]), "+v"(wf[(it + 2) % 8]));
        mfv<s == 0>(acc[t][0], wf[it % (WD + 1)], hc[0]);
        SB();
        if constexpr (nx < 224) wf[nx % (WD + 1)] = *(LAS v4u*)((ng > g ? wn : wp) + (ng & 3) * 7168 + nt * 1024);
        SB();
        mfv<s == 0>(acc[t][1], wf[it % (WD + 1)], hc[1]);
        SB();
        if constexpr (s < 15 && t < 4) {
          if constexpr (t == 0) { constexpr int hw = h_wait(g); WAITV(hw); }
          if constexpr (t & 1) { const v2u hb = hfrag(g + 1, t >> 1, 1); hn[t >> 1] = (v4u){ha.x, ha.y, hb.x, hb.y}; } else ha = hfrag(g + 1, t >> 1, 0);
        }
        if constexpr (t >= 4 && t - 4 < w2_n(g)) w2l(w2_first(g) + t - 4);
        if constexpr (s < 11 && t >= 5) hdma(g + 5, t - 5);
        if constexpr (g == 28 && t == 4) {
          constexpr int nw = vm_sum(26, 28);
          asm volatile("s_waitcnt vmcnt(%1)" : "+v"(fl3) : "n"(nw) : "memory");
          if (!__all(fl3 == tag)) spin(of3, tag);
        }
        if constexpr (g >= 28 && (t == 4 || t == 6)) ald(2 * (g - 28) + (t - 4) / 2);
        if constexpr (g == 5 || g == 6) { if constexpr (t >= 3 && 4 * (g - 5) + t - 3 < 7) { constexpr int k = 4 * (g - 5) + t - 3; cs[k] = *(LAS f32x4*)(pcr + (k & 3) * 12288 + (k >> 2) * 64); } }
        SB();
      });
      if constexpr (s < 4) { mfv<s == 0>(sS, ones, hc[0]); mfv<s == 0>(sQ, hc[0], hc[0]); }
      if constexpr (g == 4) {
        constexpr int nw = vm_sum(0, 4) + w2_n(4) + 2;
        WAITV(nw);
        __builtin_amdgcn_s_barrier();
        asm volatile("" ::: "memory");
        if (w == 0) { if (l < 8) __hip_atomic_fetch_add(pc2 + 4096, 1u, RLX); }
        else __builtin_amdgcn_raw_buffer_store_b32(vtag, WS, 0xffffff00u, 0, SC1);
      }
      if constexpr (g == 13) asm volatile("buffer_load_dword %0, %1, %2, 0 offen sc1" : "=&v"(fl2) : "v"(oc2 + 16384), "s"(rWS) : "memory");
      if constexpr (s == 5) {
        float q01, q23, qq;
        asm volatile("v_cndmask_b32 %0, %3, %4, %7\n\tv_cndmask_b32 %1, %5, %6, %7\n\tv_cndmask_b32 %2, %0, %1, %8"
                     : "=&v"(q01), "=&v"(q23), "=&v"(qq) : "v"(sQ[0]), "v"(sQ[1]), "v"(sQ[2]), "v"(sQ[3]), "s"(0xaaaaaaaaaaaaaaaaull), "s"(0xccccccccccccccccull));
        __builtin_amdgcn_raw_buffer_store_b128((v4u){f2u(sS[0]), vtag, f2u(qq), vtag}, WS, ost + (g >> 4) * 16384, 0, SC1);
      }
      if constexpr (s == 12) {
        asm volatile("buffer_load_dwordx4 %0, %2, %3, %4 offen sc1\n\tbuffer_load_dwordx4 %1, %2, %3, %5 offen sc1" : "=&v"(g2[0]), "=&v"(g2[1])
                     : "v"(og2), "s"(rWS), "s"((g >> 4) * 16384), "s"(32768 + (g >> 4) * 16384) : "memory");
      }
      if constexpr (g == 19) {
        constexpr int nw = vm_sum(16, 19) + w2_n(19) + 2;
        WAITV(nw);
        __builtin_amdgcn_raw_buffer_store_b32(vtag, WS, of3w, 0, SC1);
      }
      if constexpr (g == 27) {
        dma<true>(lds0 + ring, 4096, l16, rWS, (32 * (w >> 1) + 4 * x + rbl) * 32768 + (2 * ct + (w & 1)) * 2048, O_HT);
        dma<true>(lds0 + ring, 5120, l16, rWS, (32 * (w >> 1) + 4 * x + rbl) * 32768 + (2 * ct + (w & 1)) * 2048, O_HT + 1024);
      }
      if constexpr (g == 25) asm volatile("buffer_load_dword %0, %1, %2, 0 offen sc1" : "=&v"(fl3) : "v"(of3), "s"(rWS) : "memory");
      SB();
      wp = wn;
      if constexpr ((g & 7) == 7) STAMP(16 + (g >> 3));
      if constexpr (s == 15) {
        constexpr int nw = g == 15 ? vm_sum(14, 16) : vm_sum(29, 32);
        asm volatile("s_waitcnt vmcnt(%3)\n\ts_nop 7" : "+v"(g2[0]), "+v"(g2[1]), "+v"(fl2) : "n"(nw) : "memory");
        SB();
        if constexpr (g == 15) {
          if (!__all(fl2 == c2want)) spin(oc2 + 16384, c2want);
#pragma unroll
          for (int k = 16; k < 21; ++k) { hdma(k, 0); hdma(k, 1); }
        }
        epilogue(IC<(g >> 4)>{});
        if constexpr (g == 15) {
          WAITV(8 + vm_pre(1));
#pragma unroll
          for (int pr = 0; pr < 2; ++pr) { const v2u a = hfrag(16, pr, 0), c = hfrag(16, pr, 1); hf[0][pr] = (v4u){a.x, a.y, c.x, c.y}; }
          SB();
        }
        if constexpr (g == 15) STAMP(9);
      }
    });
  }
  STAMP(10);

  {
    __builtin_amdgcn_s_barrier();
    asm volatile("" ::: "memory");
    v2u hr[4];
#pragma unroll
    for (int i = 0; i < 4; ++i)
      hr[i] = trd(lds, L_R + (u32)(((w & 2) + (i >> 1)) * 12288 + 4096 + (8 * q + 4 * (i & 1) + rr) * 64 + (((2 * (w & 1) + (p4 >> 1)) ^ (2 * (q & 1))) << 4) + (p4 & 1) * 8));
    LAS char* pw = lds + w * 4096 + l * 16;
    u32 f3b;
    f32x4 o[4][2], mine[4];
    SB();
    sfor<0, 28>([&](auto U_) {
      constexpr int u = decltype(U_)::value;
#pragma unroll
      for (int c = 0; c < 2; ++c)
#pragma unroll
        for (int pp = 0; pp < 4; ++pp) mfa<u % 14 == 0>(o[pp][c], W2[4 * (u % 14) + pp], AC[u][c]);
      SB();
      if constexpr (u == 5) {
        WAITV(0);
        __builtin_amdgcn_raw_buffer_store_b32(vtag, WS, of3w + 16384, 0, SC1);
        SB();
      }
      if constexpr (u == UPOLL) { f3b = __builtin_amdgcn_raw_buffer_load_b32(WS, of3 + 16384, 0, SC1); SB(); }
      if constexpr (u == UCHK) {
        if (!__all(f3b == tag)) spin(of3 + 16384, tag);
        STAMP(12);
#pragma unroll
        for (int v = 14; v < 20; ++v) ald(v);
      }
      if constexpr (u > UCHK && u <= UCHK + 8) ald(u - UCHK + 19);
      if constexpr (u % 14 == 13) {
        asm volatile("s_nop 7" ::: "memory");
        SB();
        constexpr int hfb = 2 * (u / 14);
#pragma unroll
        for (int pp = 0; pp < 4; ++pp) *(LAS f32x4*)(pw + ((hfb + ((w & 1) ^ 1)) * 16384) + pp * 1024) = o[pp][1];
        if ((w & 2) == hfb) {
#pragma unroll
          for (int pp = 0; pp < 4; ++pp) { mine[pp] = o[pp][0]; asm volatile("" : "+v"(mine[pp])); }
        } else {
#pragma unroll
          for (int pp = 0; pp < 4; ++pp) *(LAS f32x4*)(pw + ((hfb + (w & 1)) * 16384) + pp * 1024) = o[pp][0];
        }
        if constexpr (u == 13) STAMP(11);
        SB();
      }
    });
    STAMP(13);
    BAR();
#pragma unroll
    for (int j = 1; j < 4; ++j)
#pragma unroll
      for (int pp = 0; pp < 4; ++pp) mine[pp] += *(LAS f32x4*)(lds + w * 16384 + ((w + j) & 3) * 4096 + pp * 1024 + l * 16);
    u32 r[8];
#pragma unroll
    for (int i = 0; i < 4; ++i) {
      r[2 * i] = pk(lo2f(hr[i].x) + mine[0][i], hi2f(hr[i].x) + mine[1][i]);
      r[2 * i + 1] = pk(lo2f(hr[i].y) + mine[2][i], hi2f(hr[i].y) + mine[3][i]);
    }
    u16* op = A.out + (1024 * (w >> 1) + 128 * x + 32 * rbl + 16 * (w & 1) + n16) * D + 64 * ct + 8 * q;
    *(v4u*)op = (v4u){r[0], r[1], r[2], r[3]};
    *(v4u*)(op + 32) = (v4u){r[4], r[5], r[6], r[7]};
    if (b == 0 && w == 0 && l == 0) __builtin_amdgcn_raw_buffer_store_b32(tag, WS, O_EP, 0, SC1);
  }
  STAMP(14);
  WAITV(0);
  STAMP(15);
  { const u64 until = pt0 + 5000ull + 100ull * (ptk - pt0); while (__builtin_amdgcn_s_memrealtime() < until) __builtin_amdgcn_s_sleep(8); }
}
}

extern "C" void kernel_launch(void* const* d_in, const int* in_sizes, int n_in, void* d_out, int out_size, void* d_ws, size_t ws_size, hipStream_t stream) {
  fk::Args a;
  a.q = (const fk::u16*)d_in[0]; a.k = (const fk::u16*)d_in[1]; a.v = (const fk::u16*)d_in[2]; a.shk = (const fk::u16*)d_in[3];
  a.ssk = (const fk::u16*)d_in[4]; a.ssd = (const fk::u16*)d_in[5]; a.wg = (const fk::u16*)d_in[7]; a.wu = (const fk::u16*)d_in[8];
  a.wd = (const fk::u16*)d_in[9]; a.out = (fk::u16*)d_out; a.ws = (char*)d_ws;
  fk::fused<<<256, 256, 0, stream>>>(a);
}
```

```cpp
#include <hip/hip_runtime.h>
#include <stdint.h>
#define NWE 20
#define PFM 9
#define UPOLL 8
#define UCHK 11

namespace fk {
constexpr int WD = 7;
typedef unsigned u32;
typedef unsigned short u16;
typedef unsigned long long u64;
typedef u32 v4u __attribute__((ext_vector_type(4)));
typedef u32 v2u __attribute__((ext_vector_type(2)));
typedef float f32x4 __attribute__((ext_vector_type(4)));
typedef float f32x2 __attribute__((ext_vector_type(2)));
typedef short s16x4 __attribute__((ext_vector_type(4)));
typedef short s16x8 __attribute__((ext_vector_type(8)));
typedef __bf16 bf2 __attribute__((ext_vector_type(2)));
#define LAS __attribute__((address_space(3)))
#define GAS __attribute__((address_space(1)))
#define RLX __ATOMIC_RELAXED, __HIP_MEMORY_SCOPE_AGENT
#define SB() __builtin_amdgcn_sched_barrier(0)
#define WAITV(n) asm volatile("s_waitcnt vmcnt(%0)" ::"n"(n) : "memory")
#define BAR() do { asm volatile("s_waitcnt lgkmcnt(0)" ::: "memory"); __builtin_amdgcn_s_barrier(); asm volatile("" ::: "memory"); } while (0)

constexpr int S = 2048, D = 512, F = 1792;
constexpr u32 O_EP = 0, O_C2 = 16384, O_ST1 = 65536, O_F1 = 98304, O_ST2 = 131072, O_F3 = 262144;
constexpr u32 O_KVT = 1u << 20, O_QT = 3u << 20, O_HT = 5u << 20, O_W2T = 7u << 20, O_ACT = 9u << 20, O_W1F = 17u << 20, WS_BYTES = 21u << 20;
constexpr u32 O_DIAG = 24u << 20;
constexpr u32 L_R = 114688, L_K = L_R, L_V = L_R + 9216, L_Q = L_R + 17408, L_WR = 0, WR_S = 1056, L_TZ = 36864, TZ_S = 4160, TZ_C = 4 * TZ_S, L_XR = L_R + 33280, XR_S = 4608,
              L_S1 = L_R, L_EX = L_R + 16384, LDS_BYTES = 163840;
constexpr u32 SPIN_CAP = 1u << 18;
constexpr int SC1 = 16;

struct Args { const u16 *q, *k, *v, *shk, *ssk, *ssd, *wg, *wu, *wd; u16* out; char* ws; };

__device__ __forceinline__ u32 f2u(float x) { return __builtin_bit_cast(u32, x); }
__device__ __forceinline__ float u2f(u32 x) { return __builtin_bit_cast(float, x); }
__device__ __forceinline__ float lo2f(u32 x) { return u2f(x << 16); }
__device__ __forceinline__ float hi2f(u32 x) { return u2f(x & 0xffff0000u); }
__device__ __forceinline__ u32 pk(float lo, float hi) { bf2 r = {(__bf16)lo, (__bf16)hi}; return __builtin_bit_cast(u32, r); }
__device__ __forceinline__ float rbf(float x) { return lo2f(pk(x, x)); }
__device__ __forceinline__ f32x4 mfma(v4u a, v4u b, f32x4 c) {
  return __builtin_amdgcn_mfma_f32_16x16x32_bf16(__builtin_bit_cast(s16x8, a), __builtin_bit_cast(s16x8, b), c, 0, 0, 0);
}
template <bool FIRST> __device__ __forceinline__ void mfv(f32x4& c, v4u a, v4u b) {
  if (FIRST) asm volatile("v_mfma_f32_16x16x32_bf16 %0, %1, %2, 0" : "=&v"(c) : "v"(a), "v"(b));
  else asm volatile("v_mfma_f32_16x16x32_bf16 %0, %1, %2, %0" : "+v"(c) : "v"(a), "v"(b));
}
template <bool FIRST> __device__ __forceinline__ void mfa(f32x4& c, v4u a, v4u b) {
  if (FIRST) asm volatile("v_mfma_f32_16x16x32_bf16 %0, %1, %2, 0" : "=&v"(c) : "a"(a), "v"(b));
  else asm volatile("v_mfma_f32_16x16x32_bf16 %0, %1, %2, %0" : "+v"(c) : "a"(a), "v"(b));
}
template <bool FIRST> __device__ __forceinline__ void mfb(f32x4& c, v4u a, v4u b) {
  if (FIRST) asm volatile("v_mfma_f32_16x16x32_bf16 %0, %1, %2, 0" : "=&v"(c) : "v"(a), "a"(b));
  else asm volatile("v_mfma_f32_16x16x32_bf16 %0, %1, %2, %0" : "+v"(c) : "v"(a), "a"(b));
}
__device__ __forceinline__ v2u trd(LAS char* lds, u32 off) {
  return __builtin_bit_cast(v2u, __builtin_amdgcn_ds_read_tr16_b64_v4i16((LAS s16x4*)(lds + off)));
}
__device__ __forceinline__ v4u mkrs(const void* p, u32 bytes) { u64 a = (u64)p; return (v4u){(u32)a, (u32)(a >> 32) & 0xffffu, bytes, 0x00020000u}; }
template <bool COH> __device__ __forceinline__ void dma(u32 lb, u32 lo, u32 voff, v4u rs, u32 sb, u32 so) {
  u32 t;
  if (COH) asm volatile("s_add_u32 m0, %1, %6\n\ts_add_u32 %0, %4, %5\n\tbuffer_load_dwordx4 %2, %3, %0 offen sc1 lds" : "=&s"(t) : "s"(lb), "v"(voff), "s"(rs), "s"(sb), "i"(so), "i"(lo) : "memory", "scc");
  else asm volatile("s_add_u32 m0, %1, %6\n\ts_add_u32 %0, %4, %5\n\tbuffer_load_dwordx4 %2, %3, %0 offen lds" : "=&s"(t) : "s"(lb), "v"(voff), "s"(rs), "s"(sb), "i"(so), "i"(lo) : "memory", "scc");
}
template <int CTRL, int RM> __device__ __forceinline__ float dppadd(float v) {
  return v + __builtin_bit_cast(float, __builtin_amdgcn_update_dpp(0, __builtin_bit_cast(int, v), CTRL, RM, 0xf, true));
}
__device__ __forceinline__ float hsum(float v) {
  v = dppadd<0x111, 0xf>(v); v = dppadd<0x112, 0xf>(v); v = dppadd<0x114, 0xf>(v); v = dppadd<0x118, 0xf>(v);
  return dppadd<0x142, 0xa>(v);
}
#define STAMP(i)
template <int V> struct IC { static constexpr int value = V; };
template <int B, int E, class Fn> __device__ __forceinline__ void sfor(Fn&& f) { if constexpr (B < E) { f(IC<B>{}); sfor<B + 1, E>(f); } }
constexpr int cv_a0(int u) { return u < 16 ? u >> 2 : ((u - 16) >> 2) > 4 ? (u - 16) >> 2 : 4; }
constexpr int cv_first(int u) { int n = 0; for (int j = 0; j < u; ++j) n += (j < 16 ? 4 : 8) - cv_a0(j); return n; }
constexpr int cv_u(int n) { int u = 0; while (cv_first(u + 1) <= n) ++u; return u; }
constexpr int cv_slot(int i) { int s_ = cv_first(i < 16 ? i : i + 16) - 12; if (i < 31) { const int v = cv_slot(i + 1) - 2; if (v < s_) s_ = v; } return s_ & ~1; }
constexpr int cv_at(int n) { for (int i = 0; i < 32; ++i) if (cv_slot(i) == n) return i; return -1; }
constexpr int w2_n(int g) { return g >= 27 ? 0 : (g & 15) < 11 ? ((g & 1) ^ 1) : 3; }
constexpr int W2_EPI = 56 - 27 - NWE;
constexpr int w2_first(int g) { int n = NWE + (g > 15 ? W2_EPI : 0); for (int j = 0; j < g; ++j) n += w2_n(j); return n; }
constexpr int a_n(int g) { return g >= 28 ? 4 : 0; }
constexpr int vm_tail(int g) { return (g == 4) + ((g & 15) == 5) + 2 * ((g & 15) == 12) + (g == 13) + (g == 19) + (g == 25) + 2 * (g == 27); }
constexpr int vm_ops(int g) { return w2_n(g) + a_n(g) + ((g & 15) < 11 ? 2 : 0) + vm_tail(g); }
constexpr int vm_sum(int a, int e) { int n = 0; for (int j = a; j < e; ++j) n += vm_ops(j); return n; }
constexpr int vm_pre(int ps) { return ps ? W2_EPI + 4 : 1; }
constexpr int h_wait(int g) {
  return (g & 15) < 4 ? 2 * (3 - (g & 15)) + vm_pre(g >> 4) + vm_sum(g & 16, g) : vm_tail(g - 4) + vm_sum(g - 3, g);
}
static_assert(w2_first(32) == 56 && W2_EPI >= 0 && W2_EPI <= 14, "all w_down fragments are requested");

__global__ void __launch_bounds__(256, 1) fused(Args A) {
  __shared__ __attribute__((aligned(16))) char smem_[LDS_BYTES];
  LAS char* lds = (LAS char*)smem_;
  const u32 lds0 = (u32)(uintptr_t)lds;
  STAMP(0);
  const int b = blockIdx.x, x = b & 7, y = b >> 3;
  const int l = threadIdx.x & 63, w = __builtin_amdgcn_readfirstlane(threadIdx.x >> 6);
  const int n16 = l & 15, q = l >> 4, rr = (l >> 2) & 3, p4 = l & 3;
  u32 pfd;
  {
    u64 pc;
    asm volatile("s_getpc_b64 %0" : "=s"(pc));
    constexpr u32 CODE = 36864;
    const u32 co = (u32)((((y & (PFM == 9 ? 7 : 3)) * 4 + w) * PFM + (l < PFM ? l : 0)) * 128);
    asm volatile("global_load_dword %0, %1, %2" : "=&v"(pfd) : "v"(co < CODE ? co : 0u), "s"(pc & ~3ull) : "memory");
  }
  const __amdgpu_buffer_rsrc_t WS = __builtin_amdgcn_make_buffer_rsrc(A.ws, 0, WS_BYTES, 0x00020000);
  const v4u rWS = mkrs(A.ws, WS_BYTES);
  auto spin = [&](u32 off, u32 want) {
    for (u32 sp = 0; sp < SPIN_CAP; ++sp) {
      asm volatile("" ::: "memory");
      if (__all(__builtin_amdgcn_raw_buffer_load_b32(WS, off, 0, SC1) == want)) break;
    }
  };

  const int ti = 4 * x + (y >> 3), cj = y & 7;
  const int cA = 64 * cj + 16 * w + n16;
  const int srow = 256 * x + 8 * y + 2 * w;
  const int ch = w >> 1, par2 = w & 1, cB = 64 * x + 2 * y + ch;
  v2u shk; v4u sx0, sx1, kz[2];
  v2u kedge; u32 vtag, vdp;
  u32 l16 = l * 16;
  {
    const v4u rK = mkrs(A.k, S * D * 2), rV = mkrs(A.v, S * D * 2), rQ = mkrs(A.q, S * D * 2);
    const u32 c0 = ((l & 7) ^ ((l >> 3) & 2)) * 16, tv = (u32)((64 * ti + (l >> 3)) * 1024 + 128 * cj);
    const u32 va = tv + c0, vb = tv + (c0 ^ 64);
    const u32 vk = ((w & 1) ? vb : va) + (u32)(w * 8192 - 8192);
    dma<false>(lds0 + w * 1024, L_K, vk, rK, 0, 0);
    dma<false>(lds0 + w * 1024, L_K + 4096, vk + 32768, rK, 0, 0);
    if (w == 0) dma<false>(lds0, L_K + 8192, vk + 65536, rK, 0, 0);
    dma<false>(lds0 + w * 2048, L_V, va, rV, w * 16384, 0);
    dma<false>(lds0 + w * 2048, L_V + 1024, vb, rV, w * 16384, 8192);
    asm volatile("global_load_dwordx2 %0, %1, %2" : "=&v"(shk) : "v"(cA * 8), "s"(A.shk) : "memory");
    asm volatile("global_load_dwordx4 %0, %2, %3\n\tglobal_load_dwordx4 %1, %2, %3 offset:1024" : "=&v"(kz[0]), "=&v"(kz[1]) : "v"(l16), "s"(A.ssk + cB * S + 1024 * par2) : "memory");
    asm volatile("global_load_dwordx2 %0, %3, %4 offset:2048\n\tglobal_load_dword %1, %3, %5\n\tglobal_load_dword %2, %3, %6"
                 : "=&v"(kedge), "=&v"(vtag), "=&v"(vdp) : "v"(0), "s"(A.ssk + cB * S), "s"(A.ws + O_EP), "s"(A.ssd + 64 * x + 2 * y) : "memory");
    dma<false>(lds0 + w * 2048, L_Q, va, rQ, w * 16384, 0);
    dma<false>(lds0 + w * 2048, L_Q + 1024, vb, rQ, w * 16384, 8192);
    asm volatile("global_load_dwordx4 %0, %2, %3\n\tglobal_load_dwordx4 %1, %2, %3 offset:512" : "=&v"(sx0), "=&v"(sx1) : "v"((l >> 5) * 1024 + (l & 31) * 16), "s"(A.q + srow * D) : "memory");
  }
  const int rb = 8 * x + 2 * (y >> 3) + ((y >> 2) & 1), fg = y & 3;
  const v4u rW = mkrs(ch ? A.wu : A.wg, D * F * 2);
  u32 wrv = l < 56 ? l16 : 0xffffff00u, w1d[4];
  LAS char* pW = lds + L_WR + ((p4 >> 1) * 8 + rr) * WR_S + (4 * w + q) * 16 + (p4 & 1) * 8;
#pragma unroll
  for (int j = 0; j < 4; ++j) {
    int Z = 16 * j + 4 * w + q;
    asm volatile("" : "+v"(Z));
    const int c = (Z * 37) >> 8;
    w1d[j] = (j < 3 || Z < 56) ? O_W1F + (u32)(((8 * fg + c) * 16 + (rb >> 2)) * 7168 + (Z - 7 * c) * 1024 + (rb & 3) * 256 + (4 * ((n16 & 7) >> 1) + 2 * (n16 >> 3) + (n16 & 1)) * 16) : 0xffffff00u;
  }
  const u32 aAo = (u32)((8 * q + rr) * 128 + (((2 * w + (p4 >> 1)) ^ (rr & 2) ^ ((q & 1) << 2)) << 4) + (p4 & 1) * 8);
  LAS char* pA = lds + L_K + aAo;
  LAS char* pB = lds + L_K + (aAo ^ 64);
  u32 okv = O_KVT + cA * 4096 + (2040 - 32 - 64 * ti - 8 * q) * 2, oqt = O_QT + cA * 4096 + (64 * ti + 8 * q) * 2;
  LAS char* tw = lds + L_TZ + ch * TZ_C + (128 * par2 + l) * 16 + 32;
  u32 og = (l & 31) == 31 ? O_ST1 + (srow + (l >> 5)) * 8 : 0xffffff00u;
  u32 oci = (x == 0 && y < 8 && w == 0 && l < 24) ? O_C2 + l * 2048 + y * 256 : 0xffffff00u;
  asm volatile("" : "+v"(w1d[0]), "+v"(w1d[1]), "+v"(w1d[2]), "+v"(w1d[3]), "+v"(pA), "+v"(pB), "+v"(okv), "+v"(oqt), "+v"(tw), "+v"(og), "+v"(wrv), "+v"(pW), "+v"(oci));
  const int wU = b < 224 ? 2 * b + ch : 0;
  u32 ow[4], w2o = b < 224 ? O_W2T + (u32)(((wU & 7) * 56 + (wU >> 3)) * 4096 + par2 * 2048) + l16 : 0xffff0000u;
  {
    const int UU = 4 * (wU >> 3) + q, f = UU / 7, u = UU - 7 * f;
    const int col = 64 * (wU & 7) + 8 * rr + 4 * (p4 & 1) + 32 * (p4 >> 1) + 2 * par2;
#pragma unroll
    for (int d = 0; d < 4; ++d) ow[d] = (u32)(((56 * f + (u < 4 ? 2 * u + 8 * d : 8 * u + 2 * d)) * D + col) * 2);
  }
  asm volatile("" : "+v"(w2o), "+v"(ow[0]), "+v"(ow[1]), "+v"(ow[2]), "+v"(ow[3]));
  if (w == 0) *(LAS v4u*)(lds + L_XR + (l >> 5) * XR_S + 4096 + (l & 31) * 16) = (v4u){0, 0, 0, 0};
  SB();
  STAMP(23);
  asm volatile("s_waitcnt vmcnt(9)" : "+v"(shk), "+v"(pfd)::"memory");
  BAR();
  STAMP(1);
  u32 tag, dpair;
  {
    v2u ka[2], kb[2], kc[2], va[2], vb[2];
#pragma unroll
    for (int it = 0; it < 2; ++it) { ka[it] = trd(pA, it * 4096 + 512); kb[it] = trd(pB, it * 4096 + 1024); kc[it] = trd(pB, it * 4096 + 1536); }
#pragma unroll
    for (int it = 0; it < 2; ++it) { va[it] = trd(pA, L_V - L_K + it * 4096); vb[it] = trd(pA, L_V - L_K + it * 4096 + 512); }
    SB();
#pragma unroll
    for (int j = 0; j < 4; ++j) dma<false>(lds0 + (8 * ch + 4 * par2) * WR_S, L_WR + j * WR_S, wrv, rW, (8 * rb + 4 * par2) * (F * 2) + fg * 896, j * (F * 2));
    SB();
    auto LO = [](u32 a, u32 c) { return (f32x2){lo2f(a), lo2f(c)}; };
    auto HI = [](u32 a, u32 c) { return (f32x2){hi2f(a), hi2f(c)}; };
    const f32x2 w0 = LO(shk.x, shk.x), w1 = HI(shk.x, shk.x), w2 = LO(shk.y, shk.y), w3 = HI(shk.y, shk.y);
    const f32x2 e[12] = {w0, HI(ka[0].x, ka[1].x), LO(ka[0].y, ka[1].y), HI(ka[0].y, ka[1].y), LO(kb[0].x, kb[1].x), HI(kb[0].x, kb[1].x), LO(kb[0].y, kb[1].y),
                         HI(kb[0].y, kb[1].y), LO(kc[0].x, kc[1].x), HI(kc[0].x, kc[1].x), LO(kc[0].y, kc[1].y), HI(kc[0].y, kc[1].y)};
    const f32x2 vv[8] = {LO(va[0].x, va[1].x), HI(va[0].x, va[1].x), LO(va[0].y, va[1].y), HI(va[0].y, va[1].y), LO(vb[0].x, vb[1].x), HI(vb[0].x, vb[1].x), LO(vb[0].y, vb[1].y), HI(vb[0].y, vb[1].y)};
    f32x2 kv[8];
#pragma unroll
    for (int s = 0; s < 8; ++s) kv[s] = (w3 * e[s + 1] + w2 * e[s + 2] + w1 * e[s + 3] + w0 * e[s + 4]) * vv[s];
#pragma unroll
    for (int it = 0; it < 2; ++it)
      __builtin_amdgcn_raw_buffer_store_b128((v4u){pk(kv[7][it], kv[6][it]), pk(kv[5][it], kv[4][it]), pk(kv[3][it], kv[2][it]), pk(kv[1][it], kv[0][it])}, WS, okv + (1 - it) * 64, 0, SC1);
    SB();
    asm volatile("s_waitcnt vmcnt(8)" : "+v"(kz[0]), "+v"(kz[1]), "+v"(kedge), "+v"(vtag), "+v"(vdp)::"memory");
    BAR();
    vtag += 1u;
    tag = __builtin_amdgcn_readfirstlane(vtag); dpair = __builtin_amdgcn_readfirstlane(vdp);
    v2u qa[2], qb[2];
#pragma unroll
    for (int it = 0; it < 2; ++it) { qa[it] = trd(pA, L_Q - L_K + it * 4096); qb[it] = trd(pA, L_Q - L_K + it * 4096 + 512); }
#pragma unroll
    for (int it = 0; it < 2; ++it) __builtin_amdgcn_raw_buffer_store_b128((v4u){qa[it].x, qa[it].y, qb[it].x, qb[it].y}, WS, oqt + it * 64, 0, SC1);
    __builtin_amdgcn_raw_buffer_store_b32(tag * 64u, WS, oci, 0, SC1);
  }
  SB();
  STAMP(24);
  {
    auto put = [&](LAS char* p, v4u d, u32 d4, u32 d5) {
      const u32 e0 = __builtin_amdgcn_alignbit(d.y, d.x, 16), e1 = __builtin_amdgcn_alignbit(d.z, d.y, 16), e2 = __builtin_amdgcn_alignbit(d.w, d.z, 16),
                e3 = __builtin_amdgcn_alignbit(d4, d.w, 16), e4 = __builtin_amdgcn_alignbit(d5, d4, 16);
      *(LAS v4u*)p = d; *(LAS v4u*)(p + TZ_S) = (v4u){e0, e1, e2, e3}; *(LAS v4u*)(p + 2 * TZ_S) = (v4u){d.y, d.z, d.w, d4}; *(LAS v4u*)(p + 3 * TZ_S) = (v4u){e1, e2, e3, e4};
    };
    auto nxt = [](u32 last, u32 v) { return (u32)__builtin_amdgcn_update_dpp((int)last, (int)v, 0x130, 0xf, 0xf, false); };
    put(tw, kz[0], nxt(__builtin_amdgcn_readlane(kz[1].x, 0), kz[0].x), nxt(__builtin_amdgcn_readlane(kz[1].y, 0), kz[0].y));
    put(tw + 1024, kz[1], nxt(par2 ? 0u : kedge.x, kz[1].x), nxt(par2 ? 0u : kedge.y, kz[1].y));
    const u32 f0 = __builtin_amdgcn_readlane(kz[0].x, 0), f1 = __builtin_amdgcn_readlane(kz[0].y, 0);
    if (l < 2 && !par2) put(tw - 32, (v4u){0, 0, 0, 0}, l ? f0 : 0u, l ? f1 : 0u);
  }
  SB();
  asm volatile("s_waitcnt vmcnt(5)" : "+v"(sx0), "+v"(sx1)::"memory");
  BAR();
  {
    v2u ta[4], tc[4];
#pragma unroll
    for (int j = 0; j < 4; ++j) { ta[j] = trd(pW, j * 256); tc[j] = trd(pW, j * 256 + 4 * WR_S); }
#pragma unroll
    for (int j = 0; j < 4; ++j) __builtin_amdgcn_raw_buffer_store_b128((v4u){ta[j].x, ta[j].y, tc[j].x, tc[j].y}, WS, w1d[j], 0, SC1);
  }
  SB();
  {
    const f32x2 v[8] = {{lo2f(sx0.x), hi2f(sx0.x)}, {lo2f(sx0.y), hi2f(sx0.y)}, {lo2f(sx0.z), hi2f(sx0.z)}, {lo2f(sx0.w), hi2f(sx0.w)},
                        {lo2f(sx1.x), hi2f(sx1.x)}, {lo2f(sx1.y), hi2f(sx1.y)}, {lo2f(sx1.z), hi2f(sx1.z)}, {lo2f(sx1.w), hi2f(sx1.w)}};
    const f32x2 s2 = ((v[0] + v[1]) + (v[2] + v[3])) + ((v[4] + v[5]) + (v[6] + v[7]));
    const float sm = hsum(s2[0] + s2[1]);
    const float m0 = u2f(__builtin_amdgcn_readlane(f2u(sm), 31)), m1 = u2f(__builtin_amdgcn_readlane(f2u(sm), 63));
    const float mean = (l < 32 ? m0 : m1) * (1.f / D);
    f32x2 ss = {0.f, 0.f};
#pragma unroll
    for (int j = 0; j < 8; ++j) { const f32x2 d = v[j] - mean; ss += d * d; }
    const float var = hsum(ss[0] + ss[1]) * (1.f / D);
    const u32 pay = pk(mean, __builtin_amdgcn_sqrtf(rbf(rbf(var) + 1e-5f)));
    __builtin_amdgcn_raw_buffer_store_b64((v2u){pay, vtag}, WS, og, 0, SC1);
  }
  SB();
  WAITV(1);
  BAR();
  STAMP(2);
  if (w == 0 && l == 0) __builtin_amdgcn_raw_buffer_store_b32(tag, WS, O_F1 + (cj * 32 + ti) * 128, 0, SC1);
  u32 w2raw[8];
  asm volatile("global_load_dword %0, %8, %12\n\tglobal_load_dword %1, %8, %12 offset:1024\n\tglobal_load_dword %2, %9, %12\n\tglobal_load_dword %3, %9, %12 offset:1024\n\t"
               "global_load_dword %4, %10, %12\n\tglobal_load_dword %5, %10, %12 offset:1024\n\tglobal_load_dword %6, %11, %12\n\tglobal_load_dword %7, %11, %12 offset:1024"
               : "=&v"(w2raw[0]), "=&v"(w2raw[1]), "=&v"(w2raw[2]), "=&v"(w2raw[3]), "=&v"(w2raw[4]), "=&v"(w2raw[5]), "=&v"(w2raw[6]), "=&v"(w2raw[7])
               : "v"(ow[0]), "v"(ow[1]), "v"(ow[2]), "v"(ow[3]), "s"(A.wd) : "memory");
  v4u T[32];
  {
    const u32 tb = lds0 + L_TZ + ch * TZ_C + ((n16 + 1) & 3) * TZ_S + (u32)((((n16 + 1) >> 2) + 2 * q + 8 * par2) * 8);
#pragma unroll
    for (int i = 0; i < 32; ++i) {
      v2u a, c;
      asm volatile("ds_read_b64 %0, %2 offset:%3\n\tds_read_b64 %1, %2 offset:%4" : "=&v"(a), "=&v"(c) : "v"(tb), "i"(128 * i), "i"(128 * i + 8) : "memory");
      T[i] = (v4u){a.x, a.y, c.x, c.y};
    }
    asm volatile("s_waitcnt lgkmcnt(0)" : "+v"(T[0]), "+v"(T[1]), "+v"(T[2]), "+v"(T[3]), "+v"(T[4]), "+v"(T[5]), "+v"(T[6]), "+v"(T[7]), "+v"(T[8]), "+v"(T[9]), "+v"(T[10]),
                 "+v"(T[11]), "+v"(T[12]), "+v"(T[13]), "+v"(T[14]), "+v"(T[15])::"memory");
    asm volatile("" : "+v"(T[16]), "+v"(T[17]), "+v"(T[18]), "+v"(T[19]), "+v"(T[20]), "+v"(T[21]), "+v"(T[22]), "+v"(T[23]), "+v"(T[24]), "+v"(T[25]), "+v"(T[26]),
                 "+v"(T[27]), "+v"(T[28]), "+v"(T[29]), "+v"(T[30]), "+v"(T[31])::"memory");
  }
  const int tB = 512 * par2 + 16 * n16 + 4 * q;
  LAS char* xp = lds + L_XR + ch * XR_S + (2032 - 16 * n16 + 32 * par2 + 8 * q) * 2 - 3584;
  LAS char* kp = lds + L_XR + ch * XR_S + (2044 - 1280 - tB) * 2;
  LAS char* gp = lds + L_S1 + tB * 8;
  LAS char* ep = lds + L_EX + w * 4096 + l * 16;
  LAS char* er = lds + L_EX + (w ^ 1) * 4096 + l * 16;
  u32 qo = O_QT + cB * 4096 + tB * 2;
  u32 oh = O_HT + (u32)((8 * (2 * par2 + (q & 1)) + (n16 >> 1)) * 32768 + (cB >> 5) * 2048 + (cB & 31) * 64 + (((2 * (n16 & 1) + (q >> 1)) ^ (2 * ((cB >> 3) & 1))) << 4));
  asm volatile("" : "+v"(xp), "+v"(kp), "+v"(gp), "+v"(ep), "+v"(er), "+v"(qo), "+v"(oh), "+v"(l16));
  const int hp = 4 * (x >> 2) + (y >> 3), fch = 8 * (x & 3) + (y & 7);
  const int ct = y & 7, rbl = y >> 3;
  const u32 ring = L_R + w * 12288;
  const int rot = fch & 3, spar = (fch >> 2) & 1;
  u32 w2v = O_W2T + (u32)((ct * 56 + 14 * w) * 4096) + l16;
  u32 ost = (fch < 8 && rr == q) ? O_ST2 + (u32)(rot * 32768 + (128 * hp + 32 * w + 16 * spar + n16) * 16) : 0xffff0000u;
  u32 og2 = O_ST2 + (u32)((l >> 5) * 65536 + (128 * hp + 32 * w + (l & 31)) * 16);
  u32 oact = O_ACT + (u32)(((8 * hp + 2 * w) * 224 + 7 * fch + q) * 256 + n16 * 16), oact2 = q < 3 ? oact + 1024 : 0xff000000u;
  u32 of3 = O_F3 + (u32)(((x * 4 + rbl) * 4 + w) * 128 + (l & 7) * 4), of3w = l == 0 ? O_F3 + (u32)(((hp * 4 + w) * 4 + (fch >> 3)) * 128 + (fch & 7) * 4) : 0xffff0000u;
  u32 av = O_ACT + (u32)(((8 * x + 2 * rbl) * 224 + 56 * w) * 256) + l16;
  LAS char* hx[2];
#pragma unroll
  for (int pr = 0; pr < 2; ++pr) hx[pr] = lds + ring + (8 * q + rr) * 64 + (((2 * (pr ^ spar) + (p4 >> 1)) ^ (2 * (q & 1))) << 4) + (p4 & 1) * 8;
  LAS char* pcs = lds + ring + 10240 + (n16 == 0 ? q * 16 : 128 + l * 16);
  LAS char* pcr = lds + L_R + 10240 + q * 16;
  asm volatile("" : "+v"(av), "+v"(pcs), "+v"(pcr), "+v"(w2v), "+v"(ost), "+v"(og2), "+v"(oact), "+v"(oact2), "+v"(of3), "+v"(of3w), "+v"(hx[0]), "+v"(hx[1]));
  STAMP(21);
  if (w == 0) spin(O_F1 + ((x ^ (4 * (l >> 5))) * 32 + (l & 31)) * 128, tag);
  BAR();
  STAMP(3);

  dma<true>(lds0 + ch * XR_S + par2 * 2048, L_XR, l16, rWS, cB * 4096 + par2 * 2048, O_KVT);
  dma<true>(lds0 + ch * XR_S + par2 * 2048, L_XR + 1024, l16, rWS, cB * 4096 + par2 * 2048, O_KVT + 1024);
#pragma unroll
  for (int i = 0; i < 4; ++i) dma<true>(lds0 + w * 4096, L_S1 + i * 1024, l16, rWS, w * 4096, O_ST1 + i * 1024);
  v2u xq[4];
  asm volatile("buffer_load_dwordx2 %0, %4, %5, 0 offen sc1\n\tbuffer_load_dwordx2 %1, %4, %5, 0 offen offset:512 sc1\n\t"
               "buffer_load_dwordx2 %2, %4, %5, 0 offen offset:2048 sc1\n\tbuffer_load_dwordx2 %3, %4, %5, 0 offen offset:2560 sc1"
               : "=&v"(xq[0]), "=&v"(xq[1]), "=&v"(xq[2]), "=&v"(xq[3]) : "v"(qo), "s"(rWS) : "memory");
  SB();
  asm volatile("s_waitcnt vmcnt(8)" : "+v"(w2raw[0]), "+v"(w2raw[1]), "+v"(w2raw[2]), "+v"(w2raw[3]), "+v"(w2raw[4]), "+v"(w2raw[5]), "+v"(w2raw[6]), "+v"(w2raw[7])::"memory");
  BAR();
  STAMP(4);
  v4u W2[56];
  auto w2l = [&](int i) { W2[i] = __builtin_amdgcn_raw_buffer_load_b128(WS, w2v + (i & 3) * 1024, (i >> 2) * 4096, SC1); };
  u32 hpk[4];
  GAS u32* pc2 = (GAS u32*)(A.ws + O_C2 + (l & 7) * 2048 + x * 256);
  auto put_h = [&](int h) {
    const auto s0 = __builtin_amdgcn_permlane16_swap(hpk[0], hpk[2], false, false);
    const auto s1 = __builtin_amdgcn_permlane16_swap(hpk[1], hpk[3], false, false);
    __builtin_amdgcn_raw_buffer_store_b128((v4u){s0[0], s1[0], s0[1], s1[1]}, WS, oh + h * (32 * 32768), 0, SC1);
  };
  const u32 oc2 = O_C2 + x * 2048 + (l & 7) * 256, c2want = tag * 64u + 32u;
  {
    u32 w1so, flw;
    auto w1l = [&](int i) {
      if ((i & 3) == 0) asm volatile("s_add_u32 m0, %1, %2\n\ts_add_u32 %0, %3, %4\n\tbuffer_load_dwordx4 %5, %6, %0 offen sc1 lds"
                                     : "=&s"(w1so) : "s"(lds0 + w * 28672), "i"(i * 1024), "s"(fch * 114688 + w * 28672), "i"(O_W1F + i * 1024), "v"(l16), "s"(rWS) : "memory", "scc");
      else asm volatile("buffer_load_dwordx4 %0, %1, %2 offen offset:%3 sc1 lds" ::"v"(l16), "s"(rWS), "s"(w1so), "i"((i & 3) * 1024) : "memory");
    };
    const float dsc = ch ? hi2f(dpair) : lo2f(dpair);
    asm volatile("" : "+v"(pc2));
    f32x4 cacc[8], mn[2], ot[2];
    v4u B[32], g1[8], w2f[2]; v2u kr[4];
    float xf[16], ln[16], kd[16], tm[16], hh[8];
    auto fix = [&](int h) {
      u32 df = 0;
#pragma unroll
      for (int i = 4 * h; i < 4 * h + 4; ++i) df |= (g1[i].y ^ vtag) | (g1[i].w ^ vtag);
      if (__builtin_amdgcn_ballot_w64(df != 0)) {
        bool ok;
        for (u32 sp = 0; sp < SPIN_CAP; ++sp) {
          asm volatile("" ::: "memory");
          ok = true;
#pragma unroll
          for (int i = 4 * h; i < 4 * h + 4; ++i) {
            g1[i] = __builtin_amdgcn_raw_buffer_load_b128(WS, O_ST1 + (tB + 256 * ((i >> 1) & 1) + 1024 * h) * 8 + (i & 1) * 16, 0, SC1);
            ok &= (g1[i].y == tag) & (g1[i].w == tag);
          }
          if (__all(ok)) break;
        }
#pragma unroll
        for (int j = 8 * h; j < 8 * h + 8; ++j) { const u32 pay = g1[j >> 1][(j & 1) * 2]; ln[j] = (xf[j] - lo2f(pay)) * __builtin_amdgcn_rcpf(hi2f(pay)); }
      }
    };
    auto give = [&](int h) {
      if (par2) { *(LAS f32x4*)(ep + 2048 * h) = cacc[4 * h]; *(LAS f32x4*)(ep + 2048 * h + 1024) = cacc[4 * h + 1]; mn[0] = cacc[4 * h + 2]; mn[1] = cacc[4 * h + 3]; asm volatile("" : "+v"(mn[0]), "+v"(mn[1])); }
      else { *(LAS f32x4*)(ep + 2048 * h) = cacc[4 * h + 2]; *(LAS f32x4*)(ep + 2048 * h + 1024) = cacc[4 * h + 3]; mn[0] = cacc[4 * h]; mn[1] = cacc[4 * h + 1]; asm volatile("" : "+v"(mn[0]), "+v"(mn[1])); }
    };
    auto pre = [&](auto J_, auto P_) {
      constexpr int j = decltype(J_)::value, ph = decltype(P_)::value;
      const u32 pay = g1[j >> 1][(j & 1) * 2];
      if constexpr (ph == 0) { xf[j] = (j & 1) ? hi2f(xq[j >> 2][(j & 3) >> 1]) : lo2f(xq[j >> 2][(j & 3) >> 1]); asm volatile("" : "+v"(xf[j])); }
      if constexpr (ph == 1) { tm[j] = __builtin_amdgcn_rcpf(hi2f(pay)); asm volatile("" : "+v"(tm[j])); }
      if constexpr (ph == 2) { ln[j] = xf[j] - lo2f(pay); asm volatile("" : "+v"(ln[j])); }
      if constexpr (ph == 3) { ln[j] *= tm[j]; asm volatile("" : "+v"(ln[j])); }
      if constexpr (ph == 4) { const u32 kk = kr[j >> 2][1 - ((j & 3) >> 1)]; kd[j] = (j & 1) ? lo2f(kk) : hi2f(kk); asm volatile("" : "+v"(kd[j])); }
    };
    sfor<0, 32>([&](auto U_) { constexpr int u = decltype(U_)::value; if constexpr (cv_slot(u) < 0) B[u] = *(LAS v4u*)(xp + 128 * (31 - u)); });
    SB();
    sfor<0, 144>([&](auto N_) {
      constexpr int n = decltype(N_)::value, u = cv_u(n), i = u < 16 ? u : u - 16, a = cv_a0(u) + n - cv_first(u), eps = 4 * a - i + 3;
      mfb<i == 0>(cacc[a], T[eps], B[i]);
      SB();
      constexpr int ub = cv_at(n);
      if constexpr (ub >= 0) B[ub] = *(LAS v4u*)(xp + 128 * (31 - ub));
      if constexpr (n < 112 && n % 4 == 1) w1l(n / 4);
      if constexpr (n == 2 || n == 3) {
        constexpr int pp = n - 2;
#pragma unroll
        for (int d = 0; d < 4; ++d) w2f[pp][d] = __builtin_amdgcn_perm(w2raw[2 * d + 1], w2raw[2 * d], pp ? 0x07060302u : 0x05040100u);
        __builtin_amdgcn_raw_buffer_store_b128(w2f[pp], WS, w2o + pp * 1024, 0, SC1);
      }
      if constexpr (n >= 11 && n < 27 && n % 4 == 3) { constexpr int aa = (n - 11) / 4; kr[aa] = *(LAS v2u*)(kp + 2560 - 512 * (aa & 1) - 2048 * (aa >> 1)); }
      if constexpr (n >= 23 && n < 39 && n % 2 == 1) pre(IC<(n - 23) / 2>{}, IC<4>{});
      if constexpr (n == 43) STAMP(25);
      if constexpr (n == 46) STAMP(26);
      if constexpr (n == 67) STAMP(27);
      if constexpr (n == 111) STAMP(28);
      if constexpr (n == 113) STAMP(22);
      if constexpr (n == 44) give(0);
      if constexpr (n == 45) {
        asm volatile("s_waitcnt vmcnt(11) lgkmcnt(0)" : "+v"(xq[0]), "+v"(xq[1]), "+v"(xq[2]), "+v"(xq[3])::"memory");
        __builtin_amdgcn_s_barrier();
        asm volatile("" ::: "memory");
        if (w == 0 && l < 8) __hip_atomic_fetch_add(pc2 + 8192, 1u, RLX);
      }
      if constexpr (n == 46 || n == 47) { g1[2 * (n - 46)] = *(LAS v4u*)(gp + (n - 46) * 2048); g1[2 * (n - 46) + 1] = *(LAS v4u*)(gp + (n - 46) * 2048 + 16); }
      if constexpr (n == 48) { ot[0] = *(LAS f32x4*)er; ot[1] = *(LAS f32x4*)(er + 1024); }
      if constexpr (n >= 50 && n < 58) { pre(IC<n - 50>{}, IC<0>{}); pre(IC<n - 50>{}, IC<1>{}); pre(IC<n - 50>{}, IC<2>{}); if constexpr (n > 50) pre(IC<n - 51>{}, IC<3>{}); }
      if constexpr (n == 58) { pre(IC<7>{}, IC<3>{}); fix(0); }
      if constexpr (n >= 59 && n < 63) {
#pragma unroll
        for (int j = 2 * (n - 59); j < 2 * (n - 59) + 2; ++j) { hh[j] = xf[j] + ln[j] * ((kd[j] * dsc + mn[j >> 2][j & 3]) + ot[j >> 2][j & 3]); asm volatile("" : "+v"(hh[j])); }
      }
      if constexpr (n == 63 || n == 64) { hpk[2 * (n - 63)] = pk(hh[4 * (n - 63)], hh[4 * (n - 63) + 1]); hpk[2 * (n - 63) + 1] = pk(hh[4 * (n - 63) + 2], hh[4 * (n - 63) + 3]); }
      if constexpr (n == 66) put_h(0);
      if constexpr (n == 98) asm volatile("buffer_load_dword %0, %1, %2, 0 offen sc1" : "=&v"(flw) : "v"(oc2 + 32768), "s"(rWS) : "memory");
      if constexpr (n == 112) {
        asm volatile("s_waitcnt vmcnt(3)" : "+v"(flw)::"memory");
        __builtin_amdgcn_s_barrier();
        if (w == 0 && l < 8) __hip_atomic_fetch_add(pc2, 1u, RLX);
        if (!__all(flw == c2want)) spin(oc2 + 32768, c2want);
      }
      if constexpr (n > 112 && n % 2 == 1 && (n - 113) / 2 < NWE) w2l((n - 113) / 2);
      if constexpr (n >= 90 && n < 98 && n % 2 == 0) { constexpr int j = 4 + (n - 90) / 2; g1[j] = *(LAS v4u*)(gp + 8192 + ((j >> 1) & 1) * 2048 + (j & 1) * 16); }
      if constexpr (n >= 104) pre(IC<8 + (n - 104) / 5>{}, IC<(n - 104) % 5>{});
      SB();
    });
    asm volatile("s_nop 7" ::: "memory");
    SB();
    STAMP(5);
    give(1);
    fix(1);
    WAITV(NWE < 16 ? NWE : 16);
    BAR();
#pragma unroll
    for (int i = 16; i < NWE; ++i) w2l(i);
#pragma unroll
    for (int aa = 0; aa < 2; ++aa) {
      const f32x4 o = *(LAS f32x4*)(er + 2048 + aa * 1024);
#pragma unroll
      for (int i = 0; i < 4; ++i) hh[4 * aa + i] = xf[8 + 4 * aa + i] + ln[8 + 4 * aa + i] * ((kd[8 + 4 * aa + i] * dsc + mn[aa][i]) + o[i]);
      hpk[2 * aa] = pk(hh[4 * aa], hh[4 * aa + 1]); hpk[2 * aa + 1] = pk(hh[4 * aa + 2], hh[4 * aa + 3]);
    }
    asm volatile("" : "+v"(hpk[0]), "+v"(hpk[1]), "+v"(hpk[2]), "+v"(hpk[3]));
    STAMP(6);
  }

  v4u ones = {0x3f803f80u, 0x3f803f80u, 0x3f803f80u, 0x3f803f80u};
  LAS char* wp0 = lds + l * 16;
  asm volatile("" : "+v"(ones), "+v"(wp0));
  u32 sg[4];
#pragma unroll
  for (int g = 0; g < 4; ++g) sg[g] = (u32)((g + rot) & 3);
  LAS char* wp = wp0 + sg[0] * 28672;
  v4u wf[WD + 1], hf[2][2], g2[2];
#pragma unroll
  for (int i = 0; i < WD; ++i) wf[i] = *(LAS v4u*)(wp + i * 1024);
  STAMP(7);
  if (w == 0) spin(oc2, c2want);
  BAR();
  STAMP(8);
  f32x4 cs[7];
  u32 fl3 = 0, fl2 = 0;
  v4u AC[28][2];
  const u32 ao[2] = {(u32)((w & 1) * (224 * 256)), (u32)(((w & 1) ^ 1) * (224 * 256))};
  auto ald = [&](int u) {
#pragma unroll
    for (int c = 0; c < 2; ++c) { AC[u][c] = __builtin_amdgcn_raw_buffer_load_b128(WS, av + (u % 14 & 3) * 1024, ao[c] + (u % 14 >> 2) * 4096 + (u / 14) * (64 * 224 * 256), SC1); SB(); }
  };
  {
    u32 hso;
    auto hdma = [&](int g, int n) {
      if (n == 0) asm volatile("s_add_u32 m0, %1, %2\n\ts_add_u32 %0, %3, %4\n\tbuffer_load_dwordx4 %5, %6, %0 offen sc1 lds"
                               : "=&s"(hso) : "s"(lds0 + ring), "i"((g % 5) * 2048), "s"((4 * hp + w) * 32768 + sg[(g >> 2) & 3] * 8192), "i"(O_HT + (g >> 4) * (32 * 32768) + (g & 3) * 2048), "v"(l16), "s"(rWS) : "memory", "scc");
      else asm volatile("buffer_load_dwordx4 %0, %1, %2 offen offset:1024 sc1 lds" ::"v"(l16), "s"(rWS), "s"(hso) : "memory");
    };
    auto hfrag = [&](int g, int pr, int t) { return trd(hx[pr], (g % 5) * 2048 + t * 256); };
    f32x4 acc[7][2], sS, sQ;
    auto epilogue = [&](auto P_) {
      constexpr int ps = decltype(P_)::value;
      if (!__all((g2[0].y == tag) & (g2[0].w == tag) & (g2[1].y == tag) & (g2[1].w == tag))) {
        for (u32 sp = 0; sp < SPIN_CAP; ++sp) {
          asm volatile("" ::: "memory");
#pragma unroll
          for (int k = 0; k < 2; ++k) g2[k] = __builtin_amdgcn_raw_buffer_load_b128(WS, og2, k * 32768 + ps * 16384, SC1);
          if (__all((g2[0].y == tag) & (g2[0].w == tag) & (g2[1].y == tag) & (g2[1].w == tag))) break;
        }
      }
      const u32 s1 = f2u(u2f(g2[0].x) + u2f(g2[1].x)), s2 = f2u(u2f(g2[0].z) + u2f(g2[1].z));
      const auto h1 = __builtin_amdgcn_permlane32_swap(s1, s1, false, false), h2 = __builtin_amdgcn_permlane32_swap(s2, s2, false, false);
      const float mean = (u2f(h1[0]) + u2f(h1[1])) * (1.f / D);
      const float v2 = __builtin_fmaxf((u2f(h2[0]) + u2f(h2[1])) * (1.f / D) - mean * mean, 0.f) + 1e-5f;
      const u32 e0 = f2u(-mean), e1 = f2u(-1.44269504089f * __builtin_amdgcn_rsqf(v2)), e2 = f2u(v2);
      const auto b0 = __builtin_amdgcn_permlane16_swap(e0, e0, false, false), b1 = __builtin_amdgcn_permlane16_swap(e1, e1, false, false),
                 b2 = __builtin_amdgcn_permlane16_swap(e2, e2, false, false);
      v4u sa[2], sb[2];
#pragma unroll
      for (int pr = 0; pr < 2; ++pr) {
        const bool up = (pr ^ spar) != 0;
        const float nm = u2f(up ? b0[1] : b0[0]), k1 = u2f(up ? b1[1] : b1[0]), vv = u2f(up ? b2[1] : b2[0]);
        u32 d[8];
        f32x2 a[7], bb[7], e[7];
#pragma unroll
        for (int t = 0; t < 7; ++t) a[t] = (f32x2){acc[t][pr][0], acc[t][pr][1]} + nm * (f32x2){cs[t][0], cs[t][1]};
        SB();
#pragma unroll
        for (int t = 0; t < 7; ++t) e[t] = a[t] * k1;
        SB();
#pragma unroll
        for (int t = 0; t < 7; ++t) {
          e[t] = (f32x2){__builtin_amdgcn_exp2f(e[t][0]), __builtin_amdgcn_exp2f(e[t][1])};
          bb[t] = (f32x2){acc[t][pr][2], acc[t][pr][3]} + nm * (f32x2){cs[t][2], cs[t][3]};
          if (ps == 0 && 7 * (1 - pr) + t < W2_EPI) w2l(w2_first(16) - W2_EPI + 7 * (1 - pr) + t);
          if (ps == 1 && t < 3) ald(8 + 3 * pr + t);
          SB();
        }
#pragma unroll
        for (int t = 0; t < 7; ++t) e[t] = e[t] * vv + vv;
        SB();
#pragma unroll
        for (int t = 0; t < 7; ++t) { e[t] = (f32x2){__builtin_amdgcn_rcpf(e[t][0]), __builtin_amdgcn_rcpf(e[t][1])}; a[t] *= bb[t]; SB(); }
#pragma unroll
        for (int t = 0; t < 7; ++t) a[t] *= e[t];
        SB();
#pragma unroll
        for (int t = 0; t < 7; ++t) d[t] = pk(a[t][0], a[t][1]);
        sa[pr] = (v4u){d[0], d[1], d[2], d[3]};
        const auto x0 = __builtin_amdgcn_permlane32_swap(d[4], d[6], false, false), x1 = __builtin_amdgcn_permlane32_swap(d[5], d[5], false, false);
        const auto y0 = __builtin_amdgcn_permlane16_swap(x0[0], x1[0], false, false), y1 = __builtin_amdgcn_permlane16_swap(x0[1], x1[1], false, false);
        sb[pr] = (v4u){y0[0], y0[1], y1[0], y1[1]};
        asm volatile("" : "+v"(sa[pr]), "+v"(sb[pr]));
        SB();
      }
#pragma unroll
      for (int pr = 0; pr < 2; ++pr) {
        const u32 ro = (u32)((pr ^ spar) * (224 * 256) + ps * (64 * 224 * 256));
        __builtin_amdgcn_raw_buffer_store_b128(sa[pr], WS, oact + ro, 0, SC1);
        __builtin_amdgcn_raw_buffer_store_b128(sb[pr], WS, oact2 + ro, 0, SC1);
      }
      SB();
    };
    hdma(0, 0); hdma(0, 1); hdma(1, 0); hdma(1, 1);
    {
      LAS char* c0 = wp0 + w * 1024;
      LAS char* c1 = wp0 + (w == 3 ? 3 : w + 4) * 1024;
      LAS char* c2 = c0 + 57344;
      LAS char* c3 = c1 + 57344;
      asm volatile("" : "+v"(c0), "+v"(c1), "+v"(c2), "+v"(c3));
      auto crd = [&](int k, int hi) { return *(LAS v4u*)((k < 8 ? (hi ? c1 : c0) : (hi ? c3 : c2)) + (k & 7) * 7168); };
      v4u fa[16], fb[16]; f32x4 ca, cb;
#pragma unroll
      for (int k = 0; k < 4; ++k) { fa[k] = crd(k, 0); fb[k] = crd(k, 1); }
      SB();
      sfor<0, 16>([&](auto K_) {
        constexpr int k = decltype(K_)::value;
        mfv<k == 0>(ca, fa[k], ones); SB();
        if constexpr (k + 4 < 16) fa[k + 4] = crd(k + 4, 0);
        SB();
        mfv<k == 0>(cb, fb[k], ones); SB();
        if constexpr (k + 4 < 16) fb[k + 4] = crd(k + 4, 1);
        if constexpr (k >= 2 && k < 8) hdma(2 + (k - 2) / 2, k & 1);
        if constexpr (k == 12) put_h(1);
        SB();
      });
      WAITV(9);
#pragma unroll
      for (int pr = 0; pr < 2; ++pr) { const v2u a = hfrag(0, pr, 0), c = hfrag(0, pr, 1); hf[0][pr] = (v4u){a.x, a.y, c.x, c.y}; }
      asm volatile("s_nop 7" ::: "memory");
      *(LAS f32x4*)pcs = ca; *(LAS f32x4*)(pcs + 64) = cb;
    }
    SB();
    STAMP(20);
    sfor<0, 32>([&](auto G_) {
      constexpr int g = decltype(G_)::value, s = g & 15;
      v4u(&hc)[2] = hf[g & 1]; v4u(&hn)[2] = hf[(g + 1) & 1];
      LAS char* wn = wp;
      if constexpr ((g & 3) == 3 && g < 31) wn = wp0 + sg[((g + 1) >> 2) & 3] * 28672;
      v2u ha;
      sfor<0, 7>([&](auto T_) {
        constexpr int t = decltype(T_)::value, it = g * 7 + t, nx = it + WD, ng = nx / 7, nt = nx % 7;
        if constexpr (t == 0) asm volatile("" : "+v"(wf[it % 8]), "+v"(wf[(it + 1) % 8]), "+v"(wf[(it + 2) % 8]), "+v"(wf[(it + 3) % 8]), "+v"(hc[0]), "+v"(hc[1]));
        if constexpr (t == 4) asm volatile("" : "+v"(wf[it % 8]), "+v"(wf[(it + 1) % 8]), "+v"(wf[(it + 2) % 8]));
        mfv<s == 0>(acc[t][0], wf[it % (WD + 1)], hc[0]);
        SB();
        if constexpr (nx < 224) wf[nx % (WD + 1)] = *(LAS v4u*)((ng > g ? wn : wp) + (ng & 3) * 7168 + nt * 1024);
        SB();
        mfv<s == 0>(acc[t][1], wf[it % (WD + 1)], hc[1]);
        SB();
        if constexpr (s < 15 && t < 4) {
          if constexpr (t == 0) { constexpr int hw = h_wait(g); WAITV(hw); }
          if constexpr (t & 1) { const v2u hb = hfrag(g + 1, t >> 1, 1); hn[t >> 1] = (v4u){ha.x, ha.y, hb.x, hb.y}; } else ha = hfrag(g + 1, t >> 1, 0);
        }
        if constexpr (t >= 4 && t - 4 < w2_n(g)) w2l(w2_first(g) + t - 4);
        if constexpr (s < 11 && t >= 5) hdma(g + 5, t - 5);
        if constexpr (g == 28 && t == 4) {
          constexpr int nw = vm_sum(26, 28);
          asm volatile("s_waitcnt vmcnt(%1)" : "+v"(fl3) : "n"(nw) : "memory");
          if (!__all(fl3 == tag)) spin(of3, tag);
        }
        if constexpr (g >= 28 && (t == 4 || t == 6)) ald(2 * (g - 28) + (t - 4) / 2);
        if constexpr (g == 5 || g == 6) { if constexpr (t >= 3 && 4 * (g - 5) + t - 3 < 7) { constexpr int k = 4 * (g - 5) + t - 3; cs[k] = *(LAS f32x4*)(pcr + (k & 3) * 12288 + (k >> 2) * 64); } }
        SB();
      });
      if constexpr (s < 4) { mfv<s == 0>(sS, ones, hc[0]); mfv<s == 0>(sQ, hc[0], hc[0]); }
      if constexpr (g == 4) {
        constexpr int nw = vm_sum(0, 4) + w2_n(4) + 2;
        WAITV(nw);
        __builtin_amdgcn_s_barrier();
        asm volatile("" ::: "memory");
        if (w == 0) { if (l < 8) __hip_atomic_fetch_add(pc2 + 4096, 1u, RLX); }
        else __builtin_amdgcn_raw_buffer_store_b32(vtag, WS, 0xffffff00u, 0, SC1);
      }
      if constexpr (g == 13) asm volatile("buffer_load_dword %0, %1, %2, 0 offen sc1" : "=&v"(fl2) : "v"(oc2 + 16384), "s"(rWS) : "memory");
      if constexpr (s == 5) {
        float q01, q23, qq;
        asm volatile("v_cndmask_b32 %0, %3, %4, %7\n\tv_cndmask_b32 %1, %5, %6, %7\n\tv_cndmask_b32 %2, %0, %1, %8"
                     : "=&v"(q01), "=&v"(q23), "=&v"(qq) : "v"(sQ[0]), "v"(sQ[1]), "v"(sQ[2]), "v"(sQ[3]), "s"(0xaaaaaaaaaaaaaaaaull), "s"(0xccccccccccccccccull));
        __builtin_amdgcn_raw_buffer_store_b128((v4u){f2u(sS[0]), vtag, f2u(qq), vtag}, WS, ost + (g >> 4) * 16384, 0, SC1);
      }
      if constexpr (s == 12) {
        asm volatile("buffer_load_dwordx4 %0, %2, %3, %4 offen sc1\n\tbuffer_load_dwordx4 %1, %2, %3, %5 offen sc1" : "=&v"(g2[0]), "=&v"(g2[1])
                     : "v"(og2), "s"(rWS), "s"((g >> 4) * 16384), "s"(32768 + (g >> 4) * 16384) : "memory");
      }
      if constexpr (g == 19) {
        constexpr int nw = vm_sum(16, 19) + w2_n(19) + 2;
        WAITV(nw);
        __builtin_amdgcn_raw_buffer_store_b32(vtag, WS, of3w, 0, SC1);
      }
      if constexpr (g == 27) {
        dma<true>(lds0 + ring, 4096, l16, rWS, (32 * (w >> 1) + 4 * x + rbl) * 32768 + (2 * ct + (w & 1)) * 2048, O_HT);
        dma<true>(lds0 + ring, 5120, l16, rWS, (32 * (w >> 1) + 4 * x + rbl) * 32768 + (2 * ct + (w & 1)) * 2048, O_HT + 1024);
      }
      if constexpr (g == 25) asm volatile("buffer_load_dword %0, %1, %2, 0 offen sc1" : "=&v"(fl3) : "v"(of3), "s"(rWS) : "memory");
      SB();
      wp = wn;
      if constexpr ((g & 7) == 7) STAMP(16 + (g >> 3));
      if constexpr (s == 15) {
        constexpr int nw = g == 15 ? vm_sum(14, 16) : vm_sum(29, 32);
        asm volatile("s_waitcnt vmcnt(%3)\n\ts_nop 7" : "+v"(g2[0]), "+v"(g2[1]), "+v"(fl2) : "n"(nw) : "memory");
        SB();
        if constexpr (g == 15) {
          if (!__all(fl2 == c2want)) spin(oc2 + 16384, c2want);
#pragma unroll
          for (int k = 16; k < 21; ++k) { hdma(k, 0); hdma(k, 1); }
        }
        epilogue(IC<(g >> 4)>{});
        if constexpr (g == 15) {
          WAITV(8 + vm_pre(1));
#pragma unroll
          for (int pr = 0; pr < 2; ++pr) { const v2u a = hfrag(16, pr, 0), c = hfrag(16, pr, 1); hf[0][pr] = (v4u){a.x, a.y, c.x, c.y}; }
          SB();
        }
        if constexpr (g == 15) STAMP(9);
      }
    });
  }
  STAMP(10);

  {
    __builtin_amdgcn_s_barrier();
    asm volatile("" ::: "memory");
    v2u hr[4];
#pragma unroll
    for (int i = 0; i < 4; ++i)
      hr[i] = trd(lds, L_R + (u32)(((w & 2) + (i >> 1)) * 12288 + 4096 + (8 * q + 4 * (i & 1) + rr) * 64 + (((2 * (w & 1) + (p4 >> 1)) ^ (2 * (q & 1))) << 4) + (p4 & 1) * 8));
    LAS char* pw = lds + w * 4096 + l * 16;
    u32 f3b;
    f32x4 o[4][2], mine[4];
    SB();
    sfor<0, 28>([&](auto U_) {
      constexpr int u = decltype(U_)::value;
#pragma unroll
      for (int c = 0; c < 2; ++c)
#pragma unroll
        for (int pp = 0; pp < 4; ++pp) mfa<u % 14 == 0>(o[pp][c], W2[4 * (u % 14) + pp], AC[u][c]);
      SB();
      if constexpr (u == 5) {
        WAITV(0);
        __builtin_amdgcn_raw_buffer_store_b32(vtag, WS, of3w + 16384, 0, SC1);
        SB();
      }
      if constexpr (u == UPOLL) { f3b = __builtin_amdgcn_raw_buffer_load_b32(WS, of3 + 16384, 0, SC1); SB(); }
      if constexpr (u == UCHK) {
        if (!__all(f3b == tag)) spin(of3 + 16384, tag);
        STAMP(12);
#pragma unroll
        for (int v = 14; v < 20; ++v) ald(v);
      }
      if constexpr (u > UCHK && u <= UCHK + 8) ald(u - UCHK + 19);
      if constexpr (u % 14 == 13) {
        asm volatile("s_nop 7" ::: "memory");
        SB();
        constexpr int hfb = 2 * (u / 14);
#pragma unroll
        for (int pp = 0; pp < 4; ++pp) *(LAS f32x4*)(pw + ((hfb + ((w & 1) ^ 1)) * 16384) + pp * 1024) = o[pp][1];
        if ((w & 2) == hfb) {
#pragma unroll
          for (int pp = 0; pp < 4; ++pp) { mine[pp] = o[pp][0]; asm volatile("" : "+v"(mine[pp])); }
        } else {
#pragma unroll
          for (int pp = 0; pp < 4; ++pp) *(LAS f32x4*)(pw + ((hfb + (w & 1)) * 16384) + pp * 1024) = o[pp][0];
        }
        if constexpr (u == 13) STAMP(11);
        SB();
      }
    });
    STAMP(13);
    BAR();
#pragma unroll
    for (int j = 1; j < 4; ++j)
#pragma unroll
      for (int pp = 0; pp < 4; ++pp) mine[pp] += *(LAS f32x4*)(lds + w * 16384 + ((w + j) & 3) * 4096 + pp * 1024 + l * 16);
    u32 r[8];
#pragma unroll
    for (int i = 0; i < 4; ++i) {
      r[2 * i] = pk(lo2f(hr[i].x) + mine[0][i], hi2f(hr[i].x) + mine[1][i]);
      r[2 * i + 1] = pk(lo2f(hr[i].y) + mine[2][i], hi2f(hr[i].y) + mine[3][i]);
    }
    u16* op = A.out + (1024 * (w >> 1) + 128 * x + 32 * rbl + 16 * (w & 1) + n16) * D + 64 * ct + 8 * q;
    *(v4u*)op = (v4u){r[0], r[1], r[2], r[3]};
    *(v4u*)(op + 32) = (v4u){r[4], r[5], r[6], r[7]};
    if (b == 0 && w == 0 && l == 0) __builtin_amdgcn_raw_buffer_store_b32(tag, WS, O_EP, 0, SC1);
  }
}
}

extern "C" void kernel_launch(void* const* d_in, const int* in_sizes, int n_in, void* d_out, int out_size, void* d_ws, size_t ws_size, hipStream_t stream) {
  fk::Args a;
  a.q = (const fk::u16*)d_in[0]; a.k = (const fk::u16*)d_in[1]; a.v = (const fk::u16*)d_in[2]; a.shk = (const fk::u16*)d_in[3];
  a.ssk = (const fk::u16*)d_in[4]; a.ssd = (const fk::u16*)d_in[5]; a.wg = (const fk::u16*)d_in[7]; a.wu = (const fk::u16*)d_in[8];
  a.wd = (const fk::u16*)d_in[9]; a.out = (fk::u16*)d_out; a.ws = (char*)d_ws;
  fk::fused<<<256, 256, 0, stream>>>(a);
}
```

```cpp
#include <hip/hip_runtime.h>
#include <stdint.h>
#define SIGA 104
#define W1P 5
#define W1A 6
#define N0 45
#define GC 14
#define GS 12
#define PFM 9
#define UPOLL 7
#define UCHK 11

namespace fk {
constexpr int WD = 7;
typedef unsigned u32;
typedef unsigned short u16;
typedef unsigned long long u64;
typedef u32 v4u __attribute__((ext_vector_type(4)));
typedef u32 v2u __attribute__((ext_vector_type(2)));
typedef float f32x4 __attribute__((ext_vector_type(4)));
typedef float f32x2 __attribute__((ext_vector_type(2)));
typedef short s16x4 __attribute__((ext_vector_type(4)));
typedef short s16x8 __attribute__((ext_vector_type(8)));
typedef __bf16 bf2 __attribute__((ext_vector_type(2)));
#define LAS __attribute__((address_space(3)))
#define GAS __attribute__((address_space(1)))
#define RLX __ATOMIC_RELAXED, __HIP_MEMORY_SCOPE_AGENT
#define SB() __builtin_amdgcn_sched_barrier(0)
#define WAITV(n) asm volatile("s_waitcnt vmcnt(%0)" ::"n"(n) : "memory")
#define BAR() do { asm volatile("s_waitcnt lgkmcnt(0)" ::: "memory"); __builtin_amdgcn_s_barrier(); asm volatile("" ::: "memory"); } while (0)

constexpr int S = 2048, D = 512, F = 1792;
constexpr u32 O_EP = 0, O_C2 = 16384, O_F2 = 294912, O_ST1 = 65536, O_F1 = 98304, O_ST2 = 131072, O_F3 = 262144;
constexpr u32 O_KVT = 1u << 20, O_QT = 3u << 20, O_HT = 5u << 20, O_W2T = 7u << 20, O_ACT = 9u << 20, O_W1F = 17u << 20, WS_BYTES = 21u << 20;
constexpr u32 O_DIAG = 24u << 20;
constexpr u32 L_R = 114688, L_K = L_R, L_V = L_R + 9216, L_Q = L_R + 17408, L_WR = 0, WR_S = 1056, L_TZ = 36864, TZ_S = 4160, TZ_C = 4 * TZ_S, L_XR = L_R + 33280, XR_S = 4608,
              L_S1 = L_R, L_EX = L_R + 16384, LDS_BYTES = 163840;
constexpr u32 SPIN_CAP = 1u << 18;
constexpr int SC1 = 16;

struct Args { const u16 *q, *k, *v, *shk, *ssk, *ssd, *wg, *wu, *wd; u16* out; char* ws; };

__device__ __forceinline__ u32 f2u(float x) { return __builtin_bit_cast(u32, x); }
__device__ __forceinline__ float u2f(u32 x) { return __builtin_bit_cast(float, x); }
__device__ __forceinline__ float lo2f(u32 x) { return u2f(x << 16); }
__device__ __forceinline__ float hi2f(u32 x) { return u2f(x & 0xffff0000u); }
__device__ __forceinline__ u32 pk(float lo, float hi) { bf2 r = {(__bf16)lo, (__bf16)hi}; return __builtin_bit_cast(u32, r); }
__device__ __forceinline__ float rbf(float x) { return lo2f(pk(x, x)); }
__device__ __forceinline__ f32x4 mfma(v4u a, v4u b, f32x4 c) {
  return __builtin_amdgcn_mfma_f32_16x16x32_bf16(__builtin_bit_cast(s16x8, a), __builtin_bit_cast(s16x8, b), c, 0, 0, 0);
}
template <bool FIRST> __device__ __forceinline__ void mfv(f32x4& c, v4u a, v4u b) {
  if (FIRST) asm volatile("v_mfma_f32_16x16x32_bf16 %0, %1, %2, 0" : "=&v"(c) : "v"(a), "v"(b));
  else asm volatile("v_mfma_f32_16x16x32_bf16 %0, %1, %2, %0" : "+v"(c) : "v"(a), "v"(b));
}
template <bool FIRST> __device__ __forceinline__ void mfa(f32x4& c, v4u a, v4u b) {
  if (FIRST) asm volatile("v_mfma_f32_16x16x32_bf16 %0, %1, %2, 0" : "=&v"(c) : "a"(a), "v"(b));
  else asm volatile("v_mfma_f32_16x16x32_bf16 %0, %1, %2, %0" : "+v"(c) : "a"(a), "v"(b));
}
template <bool FIRST> __device__ __forceinline__ void mfb(f32x4& c, v4u a, v4u b) {
  if (FIRST) asm volatile("v_mfma_f32_16x16x32_bf16 %0, %1, %2, 0" : "=&v"(c) : "v"(a), "a"(b));
  else asm volatile("v_mfma_f32_16x16x32_bf16 %0, %1, %2, %0" : "+v"(c) : "v"(a), "a"(b));
}
__device__ __forceinline__ v2u trd(LAS char* lds, u32 off) {
  return __builtin_bit_cast(v2u, __builtin_amdgcn_ds_read_tr16_b64_v4i16((LAS s16x4*)(lds + off)));
}
__device__ __forceinline__ v4u mkrs(const void* p, u32 bytes) { u64 a = (u64)p; return (v4u){(u32)a, (u32)(a >> 32) & 0xffffu, bytes, 0x00020000u}; }
template <bool COH> __device__ __forceinline__ void dma(u32 lb, u32 lo, u32 voff, v4u rs, u32 sb, u32 so) {
  u32 t;
  if (COH) asm volatile("s_add_u32 m0, %1, %6\n\ts_add_u32 %0, %4, %5\n\tbuffer_load_dwordx4 %2, %3, %0 offen sc1 lds" : "=&s"(t) : "s"(lb), "v"(voff), "s"(rs), "s"(sb), "i"(so), "i"(lo) : "memory", "scc");
  else asm volatile("s_add_u32 m0, %1, %6\n\ts_add_u32 %0, %4, %5\n\tbuffer_load_dwordx4 %2, %3, %0 offen lds" : "=&s"(t) : "s"(lb), "v"(voff), "s"(rs), "s"(sb), "i"(so), "i"(lo) : "memory", "scc");
}
template <int CTRL, int RM> __device__ __forceinline__ float dppadd(float v) {
  return v + __builtin_bit_cast(float, __builtin_amdgcn_update_dpp(0, __builtin_bit_cast(int, v), CTRL, RM, 0xf, true));
}
__device__ __forceinline__ float hsum(float v) {
  v = dppadd<0x111, 0xf>(v); v = dppadd<0x112, 0xf>(v); v = dppadd<0x114, 0xf>(v); v = dppadd<0x118, 0xf>(v);
  return dppadd<0x142, 0xa>(v);
}
#define STAMP(i)
template <int V> struct IC { static constexpr int value = V; };
template <int B, int E, class Fn> __device__ __forceinline__ void sfor(Fn&& f) { if constexpr (B < E) { f(IC<B>{}); sfor<B + 1, E>(f); } }
constexpr int cv_a0(int u) { return u < 16 ? u >> 2 : ((u - 16) >> 2) > 4 ? (u - 16) >> 2 : 4; }
constexpr int cv_first(int u) { int n = 0; for (int j = 0; j < u; ++j) n += (j < 16 ? 4 : 8) - cv_a0(j); return n; }
constexpr int cv_u(int n) { int u = 0; while (cv_first(u + 1) <= n) ++u; return u; }
constexpr int cv_slot(int i) { int s_ = cv_first(i < 16 ? i : i + 16) - 12; if (i < 31) { const int v = cv_slot(i + 1) - 2; if (v < s_) s_ = v; } return s_ & ~1; }
constexpr int cv_at(int n) { for (int i = 0; i < 32; ++i) if (cv_slot(i) == n) return i; return -1; }
constexpr int w1_upto(int n) {
  int c = n < 5 ? 0 : (n - 5) / 3 + 1;
  if (c > W1A) c = W1A;
  if (n >= N0 + 22) c += (n - N0 - 22) / W1P + 1;
  return c < 28 ? c : 28;
}
constexpr int w2_n(int g) { return g < 3 ? 0 : g >= 27 ? 3 : g % 4 != 2; }
constexpr int W2_EPI = 14, W2_EPB = 9;
constexpr int w2_first(int g) { int n = g > 15 ? W2_EPI : 0; for (int j = 0; j < g; ++j) n += w2_n(j); return n; }
constexpr int vm_tail(int g) { return (g == 4) + (g == GS) + ((g & 15) == 5) + 2 * ((g & 15) == 12) + (g == 15 ? W2_EPI + 4 : 0) + (g == 19) + 2 * (g == 27) + (g == 30); }
constexpr int d_n(int g) { return g >= 27 || (g >= 11 && g < GC) ? 0 : g == GC ? GC - 10 : 1; }
constexpr int vm_ops(int g) { return w2_n(g) + 2 * d_n(g) + vm_tail(g); }
constexpr int vm_sum(int a, int e) { int n = 0; for (int j = a; j < e; ++j) n += vm_ops(j); return n; }
constexpr int h_wait(int g) {
  if (g < 4) return 2 * (3 - g) + 2 + vm_sum(0, g);
  if (g >= 15 && g - 4 <= GC) return 2 * (GC - (g - 4)) + vm_tail(GC) + vm_sum(GC + 1, g + (g == 15));
  return vm_tail(g - 4) + vm_sum(g - 3, g);
}
static_assert(w2_first(32) + W2_EPB == 56, "all w_down fragments are requested");

__global__ void __launch_bounds__(256, 1) fused(Args A) {
  __shared__ __attribute__((aligned(16))) char smem_[LDS_BYTES];
  LAS char* lds = (LAS char*)smem_;
  const u32 lds0 = (u32)(uintptr_t)lds;
  STAMP(0);
  const int b = blockIdx.x, x = b & 7, y = b >> 3;
  const int l = threadIdx.x & 63, w = __builtin_amdgcn_readfirstlane(threadIdx.x >> 6);
  const int n16 = l & 15, q = l >> 4, rr = (l >> 2) & 3, p4 = l & 3;
  u32 pfd;
  {
    u64 pc;
    asm volatile("s_getpc_b64 %0" : "=s"(pc));
    constexpr u32 CODE = 36864;
    const u32 co = (u32)((((y & (PFM == 9 ? 7 : 3)) * 4 + w) * PFM + (l < PFM ? l : 0)) * 128);
    asm volatile("global_load_dword %0, %1, %2" : "=&v"(pfd) : "v"(co < CODE ? co : 0u), "s"(pc & ~3ull) : "memory");
  }
  const __amdgpu_buffer_rsrc_t WS = __builtin_amdgcn_make_buffer_rsrc(A.ws, 0, WS_BYTES, 0x00020000);
  const v4u rWS = mkrs(A.ws, WS_BYTES);
  auto spin = [&](u32 off, u32 want) {
    for (u32 sp = 0; sp < SPIN_CAP; ++sp) {
      asm volatile("" ::: "memory");
      if (__builtin_amdgcn_raw_buffer_load_b32(WS, off, 0, SC1) == want) off = 0xffffff00u;
      if (__all(off == 0xffffff00u)) break;
    }
  };

  const int ti = 4 * x + (y >> 3), cj = y & 7;
  const int cA = 64 * cj + 16 * w + n16;
  const int srow = 256 * x + 8 * y + 2 * w;
  const int ch = w >> 1, par2 = w & 1, cB = 64 * x + 2 * y + ch;
  v2u shk; v4u sx0, sx1, kz[2];
  v2u kedge; u32 vtag, vdp;
  u32 l16 = l * 16;
  {
    const v4u rK = mkrs(A.k, S * D * 2), rV = mkrs(A.v, S * D * 2), rQ = mkrs(A.q, S * D * 2);
    const u32 c0 = ((l & 7) ^ ((l >> 3) & 2)) * 16, tv = (u32)((64 * ti + (l >> 3)) * 1024 + 128 * cj);
    const u32 va = tv + c0, vb = tv + (c0 ^ 64);
    const u32 vk = ((w & 1) ? vb : va) + (u32)(w * 8192 - 8192);
    dma<false>(lds0 + w * 1024, L_K, vk, rK, 0, 0);
    dma<false>(lds0 + w * 1024, L_K + 4096, vk + 32768, rK, 0, 0);
    if (w == 0) dma<false>(lds0, L_K + 8192, vk + 65536, rK, 0, 0);
    dma<false>(lds0 + w * 2048, L_V, va, rV, w * 16384, 0);
    dma<false>(lds0 + w * 2048, L_V + 1024, vb, rV, w * 16384, 8192);
    asm volatile("global_load_dwordx2 %0, %1, %2" : "=&v"(shk) : "v"(cA * 8), "s"(A.shk) : "memory");
    asm volatile("global_load_dwordx4 %0, %2, %3\n\tglobal_load_dwordx4 %1, %2, %3 offset:1024" : "=&v"(kz[0]), "=&v"(kz[1]) : "v"(l16), "s"(A.ssk + cB * S + 1024 * par2) : "memory");
    asm volatile("global_load_dwordx2 %0, %3, %4 offset:2048\n\tglobal_load_dword %1, %3, %5\n\tglobal_load_dword %2, %3, %6"
                 : "=&v"(kedge), "=&v"(vtag), "=&v"(vdp) : "v"(0), "s"(A.ssk + cB * S), "s"(A.ws + O_EP), "s"(A.ssd + 64 * x + 2 * y) : "memory");
    dma<false>(lds0 + w * 2048, L_Q, va, rQ, w * 16384, 0);
    dma<false>(lds0 + w * 2048, L_Q + 1024, vb, rQ, w * 16384, 8192);
    asm volatile("global_load_dwordx4 %0, %2, %3\n\tglobal_load_dwordx4 %1, %2, %3 offset:512" : "=&v"(sx0), "=&v"(sx1) : "v"((l >> 5) * 1024 + (l & 31) * 16), "s"(A.q + srow * D) : "memory");
  }
  const int rb = 8 * x + 2 * (y >> 3) + ((y >> 2) & 1), fg = y & 3;
  const v4u rW = mkrs(ch ? A.wu : A.wg, D * F * 2);
  u32 wrv = l < 56 ? l16 : 0xffffff00u, w1d[4];
  LAS char* pW = lds + L_WR + ((p4 >> 1) * 8 + rr) * WR_S + (4 * w + q) * 16 + (p4 & 1) * 8;
#pragma unroll
  for (int j = 0; j < 4; ++j) {
    int Z = 16 * j + 4 * w + q;
    asm volatile("" : "+v"(Z));
    const int c = (Z * 37) >> 8;
    w1d[j] = (j < 3 || Z < 56) ? O_W1F + (u32)(((8 * fg + c) * 16 + (rb >> 2)) * 7168 + (Z - 7 * c) * 1024 + (rb & 3) * 256 + (4 * ((n16 & 7) >> 1) + 2 * (n16 >> 3) + (n16 & 1)) * 16) : 0xffffff00u;
  }
  const u32 aAo = (u32)((8 * q + rr) * 128 + (((2 * w + (p4 >> 1)) ^ (rr & 2) ^ ((q & 1) << 2)) << 4) + (p4 & 1) * 8);
  LAS char* pA = lds + L_K + aAo;
  LAS char* pB = lds + L_K + (aAo ^ 64);
  u32 okv = O_KVT + cA * 4096 + (2040 - 32 - 64 * ti - 8 * q) * 2, oqt = O_QT + cA * 4096 + (64 * ti + 8 * q) * 2;
  LAS char* tw = lds + L_TZ + ch * TZ_C + (128 * par2 + l) * 16 + 32;
  u32 og = (l & 31) == 31 ? O_ST1 + (srow + (l >> 5)) * 8 : 0xffffff00u;
  u32 oci = (x == 0 && y < 8 && w == 0 && l < 8) ? O_C2 + l * 2048 + y * 256 : 0xffffff00u;
  asm volatile("" : "+v"(oci), "+v"(w1d[0]), "+v"(w1d[1]), "+v"(w1d[2]), "+v"(w1d[3]), "+v"(pA), "+v"(pB), "+v"(okv), "+v"(oqt), "+v"(tw), "+v"(og), "+v"(wrv), "+v"(pW));
  const int wU = b < 224 ? 2 * b + ch : 0;
  u32 ow[4], w2o = b < 224 ? O_W2T + (u32)(((wU & 7) * 56 + (wU >> 3)) * 4096 + par2 * 2048) + l16 : 0xffff0000u;
  {
    const int UU = 4 * (wU >> 3) + q, f = UU / 7, u = UU - 7 * f;
    const int col = 64 * (wU & 7) + 8 * rr + 4 * (p4 & 1) + 32 * (p4 >> 1) + 2 * par2;
#pragma unroll
    for (int d = 0; d < 4; ++d) ow[d] = (u32)(((56 * f + (u < 4 ? 2 * u + 8 * d : 8 * u + 2 * d)) * D + col) * 2);
  }
  asm volatile("" : "+v"(w2o), "+v"(ow[0]), "+v"(ow[1]), "+v"(ow[2]), "+v"(ow[3]));
  if (w == 0) *(LAS v4u*)(lds + L_XR + (l >> 5) * XR_S + 4096 + (l & 31) * 16) = (v4u){0, 0, 0, 0};
  SB();
  STAMP(23);
  asm volatile("s_waitcnt vmcnt(9)" : "+v"(shk), "+v"(pfd)::"memory");
  BAR();
  STAMP(1);
  u32 tag, dpair;
  {
    v2u ka[2], kb[2], kc[2], va[2], vb[2];
#pragma unroll
    for (int it = 0; it < 2; ++it) { ka[it] = trd(pA, it * 4096 + 512); kb[it] = trd(pB, it * 4096 + 1024); kc[it] = trd(pB, it * 4096 + 1536); }
#pragma unroll
    for (int it = 0; it < 2; ++it) { va[it] = trd(pA, L_V - L_K + it * 4096); vb[it] = trd(pA, L_V - L_K + it * 4096 + 512); }
    SB();
#pragma unroll
    for (int j = 0; j < 4; ++j) dma<false>(lds0 + (8 * ch + 4 * par2) * WR_S, L_WR + j * WR_S, wrv, rW, (8 * rb + 4 * par2) * (F * 2) + fg * 896, j * (F * 2));
    SB();
    auto LO = [](u32 a, u32 c) { return (f32x2){lo2f(a), lo2f(c)}; };
    auto HI = [](u32 a, u32 c) { return (f32x2){hi2f(a), hi2f(c)}; };
    const f32x2 w0 = LO(shk.x, shk.x), w1 = HI(shk.x, shk.x), w2 = LO(shk.y, shk.y), w3 = HI(shk.y, shk.y);
    const f32x2 e[12] = {w0, HI(ka[0].x, ka[1].x), LO(ka[0].y, ka[1].y), HI(ka[0].y, ka[1].y), LO(kb[0].x, kb[1].x), HI(kb[0].x, kb[1].x), LO(kb[0].y, kb[1].y),
                         HI(kb[0].y, kb[1].y), LO(kc[0].x, kc[1].x), HI(kc[0].x, kc[1].x), LO(kc[0].y, kc[1].y), HI(kc[0].y, kc[1].y)};
    const f32x2 vv[8] = {LO(va[0].x, va[1].x), HI(va[0].x, va[1].x), LO(va[0].y, va[1].y), HI(va[0].y, va[1].y), LO(vb[0].x, vb[1].x), HI(vb[0].x, vb[1].x), LO(vb[0].y, vb[1].y), HI(vb[0].y, vb[1].y)};
    f32x2 kv[8];
#pragma unroll
    for (int s = 0; s < 8; ++s) kv[s] = (w3 * e[s + 1] + w2 * e[s + 2] + w1 * e[s + 3] + w0 * e[s + 4]) * vv[s];
#pragma unroll
    for (int it = 0; it < 2; ++it)
      __builtin_amdgcn_raw_buffer_store_b128((v4u){pk(kv[7][it], kv[6][it]), pk(kv[5][it], kv[4][it]), pk(kv[3][it], kv[2][it]), pk(kv[1][it], kv[0][it])}, WS, okv + (1 - it) * 64, 0, SC1);
    SB();
    asm volatile("s_waitcnt vmcnt(8)" : "+v"(kz[0]), "+v"(kz[1]), "+v"(kedge), "+v"(vtag), "+v"(vdp)::"memory");
    BAR();
    vtag += 1u;
    tag = __builtin_amdgcn_readfirstlane(vtag); dpair = __builtin_amdgcn_readfirstlane(vdp);
    v2u qa[2], qb[2];
#pragma unroll
    for (int it = 0; it < 2; ++it) { qa[it] = trd(pA, L_Q - L_K + it * 4096); qb[it] = trd(pA, L_Q - L_K + it * 4096 + 512); }
#pragma unroll
    for (int it = 0; it < 2; ++it) __builtin_amdgcn_raw_buffer_store_b128((v4u){qa[it].x, qa[it].y, qb[it].x, qb[it].y}, WS, oqt + it * 64, 0, SC1);
    __builtin_amdgcn_raw_buffer_store_b32(tag * 64u, WS, oci, 0, SC1);
  }
  SB();
  STAMP(24);
  {
    auto put = [&](LAS char* p, v4u d, u32 d4, u32 d5) {
      const u32 e0 = __builtin_amdgcn_alignbit(d.y, d.x, 16), e1 = __builtin_amdgcn_alignbit(d.z, d.y, 16), e2 = __builtin_amdgcn_alignbit(d.w, d.z, 16),
                e3 = __builtin_amdgcn_alignbit(d4, d.w, 16), e4 = __builtin_amdgcn_alignbit(d5, d4, 16);
      *(LAS v4u*)p = d; *(LAS v4u*)(p + TZ_S) = (v4u){e0, e1, e2, e3}; *(LAS v4u*)(p + 2 * TZ_S) = (v4u){d.y, d.z, d.w, d4}; *(LAS v4u*)(p + 3 * TZ_S) = (v4u){e1, e2, e3, e4};
    };
    auto nxt = [](u32 last, u32 v) { return (u32)__builtin_amdgcn_update_dpp((int)last, (int)v, 0x130, 0xf, 0xf, false); };
    put(tw, kz[0], nxt(__builtin_amdgcn_readlane(kz[1].x, 0), kz[0].x), nxt(__builtin_amdgcn_readlane(kz[1].y, 0), kz[0].y));
    put(tw + 1024, kz[1], nxt(par2 ? 0u : kedge.x, kz[1].x), nxt(par2 ? 0u : kedge.y, kz[1].y));
    const u32 f0 = __builtin_amdgcn_readlane(kz[0].x, 0), f1 = __builtin_amdgcn_readlane(kz[0].y, 0);
    if (l < 2 && !par2) put(tw - 32, (v4u){0, 0, 0, 0}, l ? f0 : 0u, l ? f1 : 0u);
  }
  SB();
  v2u st1;
  asm volatile("s_waitcnt vmcnt(9)" : "+v"(sx0), "+v"(sx1)::"memory");
  {
    const f32x2 v[8] = {{lo2f(sx0.x), hi2f(sx0.x)}, {lo2f(sx0.y), hi2f(sx0.y)}, {lo2f(sx0.z), hi2f(sx0.z)}, {lo2f(sx0.w), hi2f(sx0.w)},
                        {lo2f(sx1.x), hi2f(sx1.x)}, {lo2f(sx1.y), hi2f(sx1.y)}, {lo2f(sx1.z), hi2f(sx1.z)}, {lo2f(sx1.w), hi2f(sx1.w)}};
    const f32x2 s2 = ((v[0] + v[1]) + (v[2] + v[3])) + ((v[4] + v[5]) + (v[6] + v[7]));
    const float sm = hsum(s2[0] + s2[1]);
    const float m0 = u2f(__builtin_amdgcn_readlane(f2u(sm), 31)), m1 = u2f(__builtin_amdgcn_readlane(f2u(sm), 63));
    const float mean = (l < 32 ? m0 : m1) * (1.f / D);
    f32x2 ss = {0.f, 0.f};
#pragma unroll
    for (int j = 0; j < 8; ++j) { const f32x2 d = v[j] - mean; ss += d * d; }
    const float var = hsum(ss[0] + ss[1]) * (1.f / D);
    st1 = (v2u){pk(mean, __builtin_amdgcn_sqrtf(rbf(rbf(var) + 1e-5f))), vtag};
    __builtin_amdgcn_raw_buffer_store_b64(st1, WS, og, 0, SC1);
  }
  SB();
  WAITV(6);
  BAR();
  {
    v2u ta[4], tc[4];
#pragma unroll
    for (int j = 0; j < 4; ++j) { ta[j] = trd(pW, j * 256); tc[j] = trd(pW, j * 256 + 4 * WR_S); }
#pragma unroll
    for (int j = 0; j < 4; ++j) __builtin_amdgcn_raw_buffer_store_b128((v4u){ta[j].x, ta[j].y, tc[j].x, tc[j].y}, WS, w1d[j], 0, SC1);
  }
  SB();
  WAITV(0);
  BAR();
  STAMP(2);
  if (w == 0 && l == 0) __builtin_amdgcn_raw_buffer_store_b32(tag, WS, O_F1 + b * 128, 0, SC1);
  u32 w2raw[8];
  asm volatile("global_load_dword %0, %8, %12\n\tglobal_load_dword %1, %8, %12 offset:1024\n\tglobal_load_dword %2, %9, %12\n\tglobal_load_dword %3, %9, %12 offset:1024\n\t"
               "global_load_dword %4, %10, %12\n\tglobal_load_dword %5, %10, %12 offset:1024\n\tglobal_load_dword %6, %11, %12\n\tglobal_load_dword %7, %11, %12 offset:1024"
               : "=&v"(w2raw[0]), "=&v"(w2raw[1]), "=&v"(w2raw[2]), "=&v"(w2raw[3]), "=&v"(w2raw[4]), "=&v"(w2raw[5]), "=&v"(w2raw[6]), "=&v"(w2raw[7])
               : "v"(ow[0]), "v"(ow[1]), "v"(ow[2]), "v"(ow[3]), "s"(A.wd) : "memory");
  v4u T[32];
  {
    const u32 tb = lds0 + L_TZ + ch * TZ_C + ((n16 + 1) & 3) * TZ_S + (u32)((((n16 + 1) >> 2) + 2 * q + 8 * par2) * 8);
#pragma unroll
    for (int i = 0; i < 32; ++i) {
      v2u a, c;
      asm volatile("ds_read_b64 %0, %2 offset:%3\n\tds_read_b64 %1, %2 offset:%4" : "=&v"(a), "=&v"(c) : "v"(tb), "i"(128 * i), "i"(128 * i + 8) : "memory");
      T[i] = (v4u){a.x, a.y, c.x, c.y};
    }
    asm volatile("s_waitcnt lgkmcnt(0)" : "+v"(T[0]), "+v"(T[1]), "+v"(T[2]), "+v"(T[3]), "+v"(T[4]), "+v"(T[5]), "+v"(T[6]), "+v"(T[7]), "+v"(T[8]), "+v"(T[9]), "+v"(T[10]),
                 "+v"(T[11]), "+v"(T[12]), "+v"(T[13]), "+v"(T[14]), "+v"(T[15])::"memory");
    asm volatile("" : "+v"(T[16]), "+v"(T[17]), "+v"(T[18]), "+v"(T[19]), "+v"(T[20]), "+v"(T[21]), "+v"(T[22]), "+v"(T[23]), "+v"(T[24]), "+v"(T[25]), "+v"(T[26]),
                 "+v"(T[27]), "+v"(T[28]), "+v"(T[29]), "+v"(T[30]), "+v"(T[31])::"memory");
  }
  const int tB = 512 * par2 + 16 * n16 + 4 * q;
  LAS char* xp = lds + L_XR + ch * XR_S + (2032 - 16 * n16 + 32 * par2 + 8 * q) * 2 - 3584;
  LAS char* kp = lds + L_XR + ch * XR_S + (2044 - 1280 - tB) * 2;
  LAS char* gp = lds + L_S1 + tB * 8;
  LAS char* ep = lds + L_EX + w * 4096 + l * 16;
  LAS char* er = lds + L_EX + (w ^ 1) * 4096 + l * 16;
  u32 qo = O_QT + cB * 4096 + tB * 2;
  u32 oh = O_HT + (u32)((8 * (2 * par2 + (q & 1)) + (n16 >> 1)) * 32768 + (cB >> 5) * 2048 + (cB & 31) * 64 + (((2 * (n16 & 1) + (q >> 1)) ^ (2 * ((cB >> 3) & 1))) << 4));
  asm volatile("" : "+v"(xp), "+v"(kp), "+v"(gp), "+v"(ep), "+v"(er), "+v"(qo), "+v"(oh), "+v"(l16));
  const int hp = 4 * (x >> 2) + (y >> 3), fch = 8 * (x & 3) + (y & 7);
  const int ct = y & 7, rbl = y >> 3;
  const u32 ring = L_R + w * 12288;
  const int rot = fch & 3, spar = (fch >> 2) & 1;
  u32 w2v = O_W2T + (u32)((ct * 56 + 14 * w) * 4096) + l16;
  u32 ost = (fch < 8 && rr == q) ? O_ST2 + (u32)(rot * 32768 + (128 * hp + 32 * w + 16 * spar + n16) * 16) : 0xffff0000u;
  u32 og2 = O_ST2 + (u32)((l >> 5) * 65536 + (128 * hp + 32 * w + (l & 31)) * 16);
  u32 oact = O_ACT + (u32)(((8 * hp + 2 * w) * 224 + 7 * fch + q) * 256 + n16 * 16), oact2 = q < 3 ? oact + 1024 : 0xff000000u;
  u32 of3 = O_F3 + (u32)(((x * 4 + rbl) * 4 + w) * 128 + (l & 7) * 4), of3w = l == 0 ? O_F3 + (u32)(((hp * 4 + w) * 4 + (fch >> 3)) * 128 + (fch & 7) * 4) : 0xffff0000u;
  u32 av = O_ACT + (u32)(((8 * x + 2 * rbl) * 224 + 56 * w) * 256) + l16;
  LAS char* hx[2];
#pragma unroll
  for (int pr = 0; pr < 2; ++pr) hx[pr] = lds + ring + (8 * q + rr) * 64 + (((2 * (pr ^ spar) + (p4 >> 1)) ^ (2 * (q & 1))) << 4) + (p4 & 1) * 8;
  LAS char* pcs = lds + ring + 10240 + (n16 == 0 ? q * 16 : 128 + l * 16);
  LAS char* pcr = lds + L_R + 10240 + q * 16;
  asm volatile("" : "+v"(av), "+v"(pcs), "+v"(pcr), "+v"(w2v), "+v"(ost), "+v"(og2), "+v"(oact), "+v"(oact2), "+v"(of3), "+v"(of3w), "+v"(hx[0]), "+v"(hx[1]));
  u32 of1 = O_F1 + (u32)((l & 7) + 8 * ((x & 3) + 4 * (l >> 3))) * 128, of2 = O_F2 + (u32)(2 * ((rot + w) & 3) + (l & 1) + 8 * (l >> 1)) * 128, of2w = (w == 0 && l == 0) ? O_F2 + (u32)b * 128 : 0xffff0000u;
  asm volatile("" : "+v"(of1), "+v"(of2), "+v"(of2w));
  STAMP(21);
  if (w == 0) spin(of1, tag);
  BAR();
  STAMP(3);

  dma<true>(lds0 + ch * XR_S + par2 * 2048, L_XR, l16, rWS, cB * 4096 + par2 * 2048, O_KVT);
  dma<true>(lds0 + ch * XR_S + par2 * 2048, L_XR + 1024, l16, rWS, cB * 4096 + par2 * 2048, O_KVT + 1024);
#pragma unroll
  for (int i = 0; i < 4; ++i) dma<true>(lds0 + w * 4096, L_S1 + i * 1024, l16, rWS, w * 4096, O_ST1 + i * 1024);
  v2u xq[4];
  asm volatile("buffer_load_dwordx2 %0, %4, %5, 0 offen sc1\n\tbuffer_load_dwordx2 %1, %4, %5, 0 offen offset:512 sc1\n\t"
               "buffer_load_dwordx2 %2, %4, %5, 0 offen offset:2048 sc1\n\tbuffer_load_dwordx2 %3, %4, %5, 0 offen offset:2560 sc1"
               : "=&v"(xq[0]), "=&v"(xq[1]), "=&v"(xq[2]), "=&v"(xq[3]) : "v"(qo), "s"(rWS) : "memory");
  SB();
  asm volatile("s_waitcnt vmcnt(8)" : "+v"(w2raw[0]), "+v"(w2raw[1]), "+v"(w2raw[2]), "+v"(w2raw[3]), "+v"(w2raw[4]), "+v"(w2raw[5]), "+v"(w2raw[6]), "+v"(w2raw[7])::"memory");
  BAR();
  STAMP(4);
  GAS u32* pc2 = (GAS u32*)(A.ws + O_C2 + (l & 7) * 2048 + x * 256);
  const u32 oc2 = O_C2 + x * 2048 + (l & 7) * 256, c2want = tag * 64u + 32u;
  v4u W2[56];
  auto w2l = [&](int i) { W2[i] = __builtin_amdgcn_raw_buffer_load_b128(WS, w2v + (i & 3) * 1024, (i >> 2) * 4096, SC1); };
  u32 hpk[4];
  auto put_h = [&](int h) {
    const auto s0 = __builtin_amdgcn_permlane16_swap(hpk[0], hpk[2], false, false);
    const auto s1 = __builtin_amdgcn_permlane16_swap(hpk[1], hpk[3], false, false);
    __builtin_amdgcn_raw_buffer_store_b128((v4u){s0[0], s1[0], s0[1], s1[1]}, WS, oh + h * (32 * 32768), 0, SC1);
  };
  {
    u32 w1so;
    auto w1l = [&](int i) {
      if ((i & 3) == 0) asm volatile("s_add_u32 m0, %1, %2\n\ts_add_u32 %0, %3, %4\n\tbuffer_load_dwordx4 %5, %6, %0 offen sc1 lds"
                                     : "=&s"(w1so) : "s"(lds0 + w * 28672), "i"(i * 1024), "s"(fch * 114688 + w * 28672), "i"(O_W1F + i * 1024), "v"(l16), "s"(rWS) : "memory", "scc");
      else asm volatile("buffer_load_dwordx4 %0, %1, %2 offen offset:%3 sc1 lds" ::"v"(l16), "s"(rWS), "s"(w1so), "i"((i & 3) * 1024) : "memory");
    };
    const float dsc = ch ? hi2f(dpair) : lo2f(dpair);
    f32x4 cacc[8], mn[2], ot[2];
    v4u B[32], g1[8], w2f[2]; v2u kr[4];
    float xf[16], ln[16], kd[16], tm[16], hh[8];
    auto fix = [&](int h) {
      u32 df = 0;
#pragma unroll
      for (int i = 4 * h; i < 4 * h + 4; ++i) df |= (g1[i].y ^ vtag) | (g1[i].w ^ vtag);
      if (__builtin_amdgcn_ballot_w64(df != 0)) {
        bool ok;
        for (u32 sp = 0; sp < SPIN_CAP; ++sp) {
          asm volatile("" ::: "memory");
          ok = true;
#pragma unroll
          for (int i = 4 * h; i < 4 * h + 4; ++i) {
            g1[i] = __builtin_amdgcn_raw_buffer_load_b128(WS, O_ST1 + (tB + 256 * ((i >> 1) & 1) + 1024 * h) * 8 + (i & 1) * 16, 0, SC1);
            ok &= (g1[i].y == tag) & (g1[i].w == tag);
          }
          if (__all(ok)) break;
        }
#pragma unroll
        for (int j = 8 * h; j < 8 * h + 8; ++j) { const u32 pay = g1[j >> 1][(j & 1) * 2]; ln[j] = (xf[j] - lo2f(pay)) * __builtin_amdgcn_rcpf(hi2f(pay)); }
      }
    };
    auto give = [&](int h) {
      if (par2) { *(LAS f32x4*)(ep + 2048 * h) = cacc[4 * h]; *(LAS f32x4*)(ep + 2048 * h + 1024) = cacc[4 * h + 1]; mn[0] = cacc[4 * h + 2]; mn[1] = cacc[4 * h + 3]; asm volatile("" : "+v"(mn[0]), "+v"(mn[1])); }
      else { *(LAS f32x4*)(ep + 2048 * h) = cacc[4 * h + 2]; *(LAS f32x4*)(ep + 2048 * h + 1024) = cacc[4 * h + 3]; mn[0] = cacc[4 * h]; mn[1] = cacc[4 * h + 1]; asm volatile("" : "+v"(mn[0]), "+v"(mn[1])); }
    };
    auto pre = [&](auto J_, auto P_) {
      constexpr int j = decltype(J_)::value, ph = decltype(P_)::value;
      const u32 pay = g1[j >> 1][(j & 1) * 2];
      if constexpr (ph == 0) { xf[j] = (j & 1) ? hi2f(xq[j >> 2][(j & 3) >> 1]) : lo2f(xq[j >> 2][(j & 3) >> 1]); asm volatile("" : "+v"(xf[j])); }
      if constexpr (ph == 1) { tm[j] = __builtin_amdgcn_rcpf(hi2f(pay)); asm volatile("" : "+v"(tm[j])); }
      if constexpr (ph == 2) { ln[j] = xf[j] - lo2f(pay); asm volatile("" : "+v"(ln[j])); }
      if constexpr (ph == 3) { ln[j] *= tm[j]; asm volatile("" : "+v"(ln[j])); }
      if constexpr (ph == 4) { const u32 kk = kr[j >> 2][1 - ((j & 3) >> 1)]; kd[j] = (j & 1) ? lo2f(kk) : hi2f(kk); asm volatile("" : "+v"(kd[j])); }
    };
    sfor<0, 32>([&](auto U_) { constexpr int u = decltype(U_)::value; if constexpr (cv_slot(u) < 0) B[u] = *(LAS v4u*)(xp + 128 * (31 - u)); });
    SB();
    sfor<0, 144>([&](auto N_) {
      constexpr int n = decltype(N_)::value, u = cv_u(n), i = u < 16 ? u : u - 16, a = cv_a0(u) + n - cv_first(u), eps = 4 * a - i + 3;
      mfb<i == 0>(cacc[a], T[eps], B[i]);
      SB();
      constexpr int ub = cv_at(n);
      if constexpr (ub >= 0) B[ub] = *(LAS v4u*)(xp + 128 * (31 - ub));
      if constexpr (w1_upto(n) > w1_upto(n - 1)) w1l(w1_upto(n) - 1);
      if constexpr (n < 4) {
        constexpr int pp = n / 2, d = 2 * (n & 1);
        w2f[pp][d] = __builtin_amdgcn_perm(w2raw[2 * d + 1], w2raw[2 * d], pp ? 0x07060302u : 0x05040100u);
        w2f[pp][d + 1] = __builtin_amdgcn_perm(w2raw[2 * d + 3], w2raw[2 * d + 2], pp ? 0x07060302u : 0x05040100u);
        if constexpr (n & 1) __builtin_amdgcn_raw_buffer_store_b128(w2f[pp], WS, w2o + pp * 1024, 0, SC1); else asm volatile("" : "+v"(w2f[pp]));
      }
      if constexpr (n == 4 || n == 6) kr[(n - 4) / 2] = *(LAS v2u*)(kp + 2560 - 512 * ((n - 4) / 2));
      if constexpr (n >= 23 && n < 39 && n % 2 == 1) pre(IC<(n - 23) / 2>{}, IC<4>{});
      if constexpr (n == N0 - 2) STAMP(25);
      if constexpr (n == N0 + 1) STAMP(26);
      if constexpr (n == N0 + 22) STAMP(27);
      if constexpr (n == SIGA - 1) STAMP(28);
      if constexpr (n == SIGA + 1) STAMP(22);
      if constexpr (n == 44) give(0);
      if constexpr (n == N0) {
        constexpr int nw = 2 + w1_upto(N0);
        asm volatile("s_waitcnt vmcnt(%4) lgkmcnt(0)" : "+v"(xq[0]), "+v"(xq[1]), "+v"(xq[2]), "+v"(xq[3]) : "n"(nw) : "memory");
        __builtin_amdgcn_s_barrier();
        asm volatile("" ::: "memory");
      }
      if constexpr (n == N0 + 1 || n == N0 + 2) { constexpr int k = n - N0 - 1; g1[2 * k] = *(LAS v4u*)(gp + k * 2048); g1[2 * k + 1] = *(LAS v4u*)(gp + k * 2048 + 16); ot[k] = *(LAS f32x4*)(er + k * 1024); }
      if constexpr (n == N0 + 3 || n == N0 + 4) { constexpr int k = 4 * (n - N0 - 3); pre(IC<k>{}, IC<0>{}); pre(IC<k + 1>{}, IC<0>{}); pre(IC<k + 2>{}, IC<0>{}); pre(IC<k + 3>{}, IC<0>{}); }
      if constexpr (n >= N0 + 5 && n < N0 + 13) {
        constexpr int j = n - N0 - 5;
        pre(IC<j>{}, IC<1>{}); pre(IC<j>{}, IC<2>{});
        if constexpr (j > 0) pre(IC<j - 1>{}, IC<3>{});
      }
      if constexpr (n == N0 + 13) { pre(IC<7>{}, IC<3>{}); fix(0); }
      if constexpr (n >= N0 + 14 && n < N0 + 18) {
#pragma unroll
        for (int j = 2 * (n - N0 - 14); j < 2 * (n - N0 - 14) + 2; ++j) { hh[j] = xf[j] + ln[j] * ((kd[j] * dsc + mn[j >> 2][j & 3]) + ot[j >> 2][j & 3]); asm volatile("" : "+v"(hh[j])); }
      }
      if constexpr (n == N0 + 18 || n == N0 + 19) { constexpr int k = n - N0 - 18; hpk[2 * k] = pk(hh[4 * k], hh[4 * k + 1]); hpk[2 * k + 1] = pk(hh[4 * k + 2], hh[4 * k + 3]); asm volatile("" : "+v"(hpk[2 * k]), "+v"(hpk[2 * k + 1])); }
      if constexpr (n == N0 + 20) put_h(0);
      if constexpr (n == 70 || n == 74) kr[2 + (n - 70) / 4] = *(LAS v2u*)(kp + 512 - 512 * ((n - 70) / 4));
      if constexpr (n >= 78 && n < 94 && n % 4 == 2) { constexpr int j = 4 + (n - 78) / 4; g1[j] = *(LAS v4u*)(gp + 8192 + ((j >> 1) & 1) * 2048 + (j & 1) * 16); }
      if constexpr (n == SIGA) {
        constexpr int nw = w1_upto(SIGA) - w1_upto(N0 + 20);
        WAITV(nw);
        __builtin_amdgcn_s_barrier();
        __builtin_amdgcn_raw_buffer_store_b32(vtag, WS, of2w, 0, SC1);
      }
      if constexpr (n >= 96 && n < 136) pre(IC<8 + (n - 96) / 5>{}, IC<(n - 96) % 5>{});
      SB();
    });
    asm volatile("s_nop 7" ::: "memory");
    SB();
    STAMP(5);
    sfor<w1_upto(143), 28>([&](auto I_) { w1l(decltype(I_)::value); });
    give(1);
    fix(1);
    BAR();
#pragma unroll
    for (int aa = 0; aa < 2; ++aa) {
      const f32x4 o = *(LAS f32x4*)(er + 2048 + aa * 1024);
#pragma unroll
      for (int i = 0; i < 4; ++i) hh[4 * aa + i] = xf[8 + 4 * aa + i] + ln[8 + 4 * aa + i] * ((kd[8 + 4 * aa + i] * dsc + mn[aa][i]) + o[i]);
      hpk[2 * aa] = pk(hh[4 * aa], hh[4 * aa + 1]); hpk[2 * aa + 1] = pk(hh[4 * aa + 2], hh[4 * aa + 3]);
    }
    asm volatile("" : "+v"(hpk[0]), "+v"(hpk[1]), "+v"(hpk[2]), "+v"(hpk[3]));
    STAMP(6);
  }

  v4u ones = {0x3f803f80u, 0x3f803f80u, 0x3f803f80u, 0x3f803f80u};
  LAS char* wp0 = lds + l * 16;
  asm volatile("" : "+v"(ones), "+v"(wp0));
  u32 sg[4];
#pragma unroll
  for (int g = 0; g < 4; ++g) sg[g] = (u32)((g + rot) & 3);
  LAS char* wp = wp0 + sg[0] * 28672;
  v4u wf[WD + 1], hf[2][2], g2[2];
  WAITV(0);
  STAMP(7);
  if (w < 2) spin(of2, tag);
  BAR();
  STAMP(8);
  f32x4 cs[7];
  u32 fl3 = 0, fl2 = 0;
  v4u AC[28][2];
  const u32 ao[2] = {(u32)((w & 1) * (224 * 256)), (u32)(((w & 1) ^ 1) * (224 * 256))};
  auto ald = [&](int u) {
#pragma unroll
    for (int c = 0; c < 2; ++c) { AC[u][c] = __builtin_amdgcn_raw_buffer_load_b128(WS, av + (u % 14 & 3) * 1024, ao[c] + (u % 14 >> 2) * 4096 + (u / 14) * (64 * 224 * 256), SC1); SB(); }
  };
  {
    u32 hso;
    auto hdma = [&](int g, int n) {
      if (n == 0) asm volatile("s_add_u32 m0, %1, %2\n\ts_add_u32 %0, %3, %4\n\tbuffer_load_dwordx4 %5, %6, %0 offen sc1 lds"
                               : "=&s"(hso) : "s"(lds0 + ring), "i"((g % 5) * 2048), "s"((4 * hp + w) * 32768 + sg[(g >> 2) & 3] * 8192), "i"(O_HT + (g >> 4) * (32 * 32768) + (g & 3) * 2048), "v"(l16), "s"(rWS) : "memory", "scc");
      else asm volatile("buffer_load_dwordx4 %0, %1, %2 offen offset:1024 sc1 lds" ::"v"(l16), "s"(rWS), "s"(hso) : "memory");
    };
    auto hfrag = [&](int g, int pr, int t) { return trd(hx[pr], (g % 5) * 2048 + t * 256); };
    f32x4 acc[7][2], sS, sQ;
    auto epilogue = [&](auto P_) {
      constexpr int ps = decltype(P_)::value;
      if (!__all((g2[0].y == tag) & (g2[0].w == tag) & (g2[1].y == tag) & (g2[1].w == tag))) {
        for (u32 sp = 0; sp < SPIN_CAP; ++sp) {
          asm volatile("" ::: "memory");
#pragma unroll
          for (int k = 0; k < 2; ++k) g2[k] = __builtin_amdgcn_raw_buffer_load_b128(WS, og2, k * 32768 + ps * 16384, SC1);
          if (__all((g2[0].y == tag) & (g2[0].w == tag) & (g2[1].y == tag) & (g2[1].w == tag))) break;
        }
      }
      const u32 s1 = f2u(u2f(g2[0].x) + u2f(g2[1].x)), s2 = f2u(u2f(g2[0].z) + u2f(g2[1].z));
      const auto h1 = __builtin_amdgcn_permlane32_swap(s1, s1, false, false), h2 = __builtin_amdgcn_permlane32_swap(s2, s2, false, false);
      const float mean = (u2f(h1[0]) + u2f(h1[1])) * (1.f / D);
      const float v2 = __builtin_fmaxf((u2f(h2[0]) + u2f(h2[1])) * (1.f / D) - mean * mean, 0.f) + 1e-5f;
      const u32 e0 = f2u(-mean), e1 = f2u(-1.44269504089f * __builtin_amdgcn_rsqf(v2)), e2 = f2u(v2);
      const auto b0 = __builtin_amdgcn_permlane16_swap(e0, e0, false, false), b1 = __builtin_amdgcn_permlane16_swap(e1, e1, false, false),
                 b2 = __builtin_amdgcn_permlane16_swap(e2, e2, false, false);
      v4u sa[2], sb[2];
#pragma unroll
      for (int pr = 0; pr < 2; ++pr) {
        const bool up = (pr ^ spar) != 0;
        const float nm = u2f(up ? b0[1] : b0[0]), k1 = u2f(up ? b1[1] : b1[0]), vv = u2f(up ? b2[1] : b2[0]);
        u32 d[8];
        f32x2 a[7], bb[7], e[7];
#pragma unroll
        for (int t = 0; t < 7; ++t) a[t] = (f32x2){acc[t][pr][0], acc[t][pr][1]} + nm * (f32x2){cs[t][0], cs[t][1]};
        SB();
#pragma unroll
        for (int t = 0; t < 7; ++t) e[t] = a[t] * k1;
        SB();
#pragma unroll
        for (int t = 0; t < 7; ++t) {
          e[t] = (f32x2){__builtin_amdgcn_exp2f(e[t][0]), __builtin_amdgcn_exp2f(e[t][1])};
          bb[t] = (f32x2){acc[t][pr][2], acc[t][pr][3]} + nm * (f32x2){cs[t][2], cs[t][3]};
          if (ps == 0) w2l(w2_first(16) - W2_EPI + 7 * pr + t);
          if (ps == 1 && 7 * pr + t < W2_EPB) w2l(56 - W2_EPB + 7 * pr + t);
          SB();
        }
#pragma unroll
        for (int t = 0; t < 7; ++t) e[t] = e[t] * vv + vv;
        SB();
#pragma unroll
        for (int t = 0; t < 7; ++t) { e[t] = (f32x2){__builtin_amdgcn_rcpf(e[t][0]), __builtin_amdgcn_rcpf(e[t][1])}; a[t] *= bb[t]; SB(); }
#pragma unroll
        for (int t = 0; t < 7; ++t) a[t] *= e[t];
        SB();
#pragma unroll
        for (int t = 0; t < 7; ++t) d[t] = pk(a[t][0], a[t][1]);
        sa[pr] = (v4u){d[0], d[1], d[2], d[3]};
        const auto x0 = __builtin_amdgcn_permlane32_swap(d[4], d[6], false, false), x1 = __builtin_amdgcn_permlane32_swap(d[5], d[5], false, false);
        const auto y0 = __builtin_amdgcn_permlane16_swap(x0[0], x1[0], false, false), y1 = __builtin_amdgcn_permlane16_swap(x0[1], x1[1], false, false);
        sb[pr] = (v4u){y0[0], y0[1], y1[0], y1[1]};
        asm volatile("" : "+v"(sa[pr]), "+v"(sb[pr]));
        SB();
      }
#pragma unroll
      for (int pr = 0; pr < 2; ++pr) {
        const u32 ro = (u32)((pr ^ spar) * (224 * 256) + ps * (64 * 224 * 256));
        __builtin_amdgcn_raw_buffer_store_b128(sa[pr], WS, oact + ro, 0, SC1);
        __builtin_amdgcn_raw_buffer_store_b128(sb[pr], WS, oact2 + ro, 0, SC1);
      }
      SB();
    };
    hdma(0, 0); hdma(0, 1); hdma(1, 0); hdma(1, 1);
    {
      LAS char* c0 = wp0 + w * 1024;
      LAS char* c1 = wp0 + (w == 3 ? 3 : w + 4) * 1024;
      LAS char* c2 = c0 + 57344;
      LAS char* c3 = c1 + 57344;
      asm volatile("" : "+v"(c0), "+v"(c1), "+v"(c2), "+v"(c3));
      auto crd = [&](int k, int hi) { return *(LAS v4u*)((k < 8 ? (hi ? c1 : c0) : (hi ? c3 : c2)) + (k & 7) * 7168); };
      v4u fa[16], fb[16]; f32x4 ca, cb;
#pragma unroll
      for (int k = 0; k < 4; ++k) { fa[k] = crd(k, 0); fb[k] = crd(k, 1); }
      SB();
      sfor<0, 16>([&](auto K_) {
        constexpr int k = decltype(K_)::value;
        mfv<k == 0>(ca, fa[k], ones); SB();
        if constexpr (k + 4 < 16) fa[k + 4] = crd(k + 4, 0);
        SB();
        mfv<k == 0>(cb, fb[k], ones); SB();
        if constexpr (k + 4 < 16) fb[k + 4] = crd(k + 4, 1);
        if constexpr (k >= 2 && k < 8) hdma(2 + (k - 2) / 2, k & 1);
        if constexpr (k == 8) asm volatile("buffer_load_dword %0, %1, %2, 0 offen sc1" : "=&v"(fl2) : "v"(w < 2 ? 0xffffff00u : of2), "s"(rWS) : "memory");
        if constexpr (k == 9) put_h(1);
        if constexpr (k >= 9) wf[k - 9] = *(LAS v4u*)(wp + (k - 9) * 1024);
        SB();
      });
      WAITV(10);
#pragma unroll
      for (int pr = 0; pr < 2; ++pr) { const v2u a = hfrag(0, pr, 0), c = hfrag(0, pr, 1); hf[0][pr] = (v4u){a.x, a.y, c.x, c.y}; }
      asm volatile("s_nop 7" ::: "memory");
      *(LAS f32x4*)pcs = ca; *(LAS f32x4*)(pcs + 64) = cb;
    }
    SB();
    STAMP(20);
    sfor<0, 32>([&](auto G_) {
      constexpr int g = decltype(G_)::value, s = g & 15;
      v4u(&hc)[2] = hf[g & 1]; v4u(&hn)[2] = hf[(g + 1) & 1];
      LAS char* wn = wp;
      if constexpr ((g & 3) == 3 && g < 31) wn = wp0 + sg[((g + 1) >> 2) & 3] * 28672;
      v2u ha;
      sfor<0, 7>([&](auto T_) {
        constexpr int t = decltype(T_)::value, it = g * 7 + t, nx = it + WD, ng = nx / 7, nt = nx % 7;
        if constexpr (t == 0) asm volatile("" : "+v"(wf[it % 8]), "+v"(wf[(it + 1) % 8]), "+v"(wf[(it + 2) % 8]), "+v"(wf[(it + 3) % 8]), "+v"(hc[0]), "+v"(hc[1]));
        if constexpr (t == 4) asm volatile("" : "+v"(wf[it % 8]), "+v"(wf[(it + 1) % 8]), "+v"(wf[(it + 2) % 8]));
        mfv<s == 0>(acc[t][0], wf[it % (WD + 1)], hc[0]);
        SB();
        if constexpr (nx < 224) wf[nx % (WD + 1)] = *(LAS v4u*)((ng > g ? wn : wp) + (ng & 3) * 7168 + nt * 1024);
        SB();
        mfv<s == 0>(acc[t][1], wf[it % (WD + 1)], hc[1]);
        SB();
        if constexpr (g < 31 && g != 15 && t < 4) {
          if constexpr (t == 0) { constexpr int hw = h_wait(g); WAITV(hw); }
          if constexpr (t & 1) { const v2u hb = hfrag(g + 1, t >> 1, 1); hn[t >> 1] = (v4u){ha.x, ha.y, hb.x, hb.y}; } else ha = hfrag(g + 1, t >> 1, 0);
        }
        if constexpr (t >= 4 && t - 4 < w2_n(g)) w2l(w2_first(g) + t - 4);
        if constexpr (g == GC && t == 5) {
          constexpr int nw = vm_sum(GS + 1, GC) + w2_n(GC);
          asm volatile("s_waitcnt vmcnt(%1)" : "+v"(fl2) : "n"(nw) : "memory");
          if (!__all(fl2 == c2want)) spin(oc2, c2want);
          sfor<16, GC + 5>([&](auto K_) { hdma(decltype(K_)::value, 0); hdma(decltype(K_)::value, 1); });
        }
        if constexpr (d_n(g) > 0 && t >= 5) hdma(g + 5, t - 5);
        if constexpr (g == 5 || g == 6) { if constexpr (t >= 3 && 4 * (g - 5) + t - 3 < 7) { constexpr int k = 4 * (g - 5) + t - 3; cs[k] = *(LAS f32x4*)(pcr + (k & 3) * 12288 + (k >> 2) * 64); } }
        SB();
      });
      if constexpr (s < 4) { mfv<s == 0>(sS, ones, hc[0]); mfv<s == 0>(sQ, hc[0], hc[0]); }
      if constexpr (g == 2) {
        constexpr int nw = 1 + vm_sum(0, 3);
        asm volatile("s_waitcnt vmcnt(%1)" : "+v"(fl2) : "n"(nw) : "memory");
        if (w >= 2 && !__all(fl2 == tag)) spin(of2, tag);
        __builtin_amdgcn_s_barrier();
        asm volatile("" ::: "memory");
      }
      if constexpr (g == 4) {
        constexpr int nw = vm_sum(0, 4) + w2_n(4) + 2;
        WAITV(nw);
        __builtin_amdgcn_s_barrier();
        asm volatile("" ::: "memory");
        if (w == 0) { if (l < 8) __hip_atomic_fetch_add(pc2, 1u, RLX); }
        else __builtin_amdgcn_raw_buffer_store_b32(vtag, WS, 0xffffff00u, 0, SC1);
      }
      if constexpr (s == 5) {
        float q01, q23, qq;
        asm volatile("v_cndmask_b32 %0, %3, %4, %7\n\tv_cndmask_b32 %1, %5, %6, %7\n\tv_cndmask_b32 %2, %0, %1, %8"
                     : "=&v"(q01), "=&v"(q23), "=&v"(qq) : "v"(sQ[0]), "v"(sQ[1]), "v"(sQ[2]), "v"(sQ[3]), "s"(0xaaaaaaaaaaaaaaaaull), "s"(0xccccccccccccccccull));
        __builtin_amdgcn_raw_buffer_store_b128((v4u){f2u(sS[0]), vtag, f2u(qq), vtag}, WS, ost + (g >> 4) * 16384, 0, SC1);
      }
      if constexpr (s == 12) {
        asm volatile("buffer_load_dwordx4 %0, %2, %3, %4 offen sc1\n\tbuffer_load_dwordx4 %1, %2, %3, %5 offen sc1" : "=&v"(g2[0]), "=&v"(g2[1])
                     : "v"(og2), "s"(rWS), "s"((g >> 4) * 16384), "s"(32768 + (g >> 4) * 16384) : "memory");
      }
      if constexpr (g == GS) asm volatile("buffer_load_dword %0, %1, %2, 0 offen sc1" : "=&v"(fl2) : "v"(oc2), "s"(rWS) : "memory");
      if constexpr (g == 19) {
        constexpr int nw = vm_sum(16, 19) + w2_n(19) + 2;
        WAITV(nw);
        __builtin_amdgcn_raw_buffer_store_b32(vtag, WS, of3w, 0, SC1);
      }
      if constexpr (g == 27) {
        dma<true>(lds0 + ring, 4096, l16, rWS, (32 * (w >> 1) + 4 * x + rbl) * 32768 + (2 * ct + (w & 1)) * 2048, O_HT);
        dma<true>(lds0 + ring, 5120, l16, rWS, (32 * (w >> 1) + 4 * x + rbl) * 32768 + (2 * ct + (w & 1)) * 2048, O_HT + 1024);
      }
      if constexpr (g == 30) asm volatile("buffer_load_dword %0, %1, %2, 0 offen sc1" : "=&v"(fl3) : "v"(of3), "s"(rWS) : "memory");
      SB();
      wp = wn;
      if constexpr ((g & 7) == 7) STAMP(16 + (g >> 3));
      if constexpr (s == 15) {
        constexpr int nw = (GS == 12) + (g == 15 ? vm_sum(13, 15) + w2_n(15) + 2 * d_n(15) : vm_sum(29, 32) - (GS == 12));
        asm volatile("s_waitcnt vmcnt(%2)\n\ts_nop 7" : "+v"(g2[0]), "+v"(g2[1]) : "n"(nw) : "memory");
        SB();
        epilogue(IC<(g >> 4)>{});
        if constexpr (g == 15) {
          constexpr int hw = h_wait(15);
          WAITV(hw);
#pragma unroll
          for (int pr = 0; pr < 2; ++pr) { const v2u a = hfrag(16, pr, 0), c = hfrag(16, pr, 1); hf[0][pr] = (v4u){a.x, a.y, c.x, c.y}; }
          SB();
        }
        if constexpr (g == 15) STAMP(9);
      }
    });
  }
  STAMP(10);

  {
    asm volatile("s_waitcnt vmcnt(4)" : "+v"(fl3)::"memory");
    if (!__all(fl3 == tag)) spin(of3, tag);
#pragma unroll
    for (int u = 0; u < 6; ++u) ald(u);
    WAITV(12);
    __builtin_amdgcn_raw_buffer_store_b32(vtag, WS, of3w + 16384, 0, SC1);
    SB();
#pragma unroll
    for (int u = 6; u < 12; ++u) ald(u);
    __builtin_amdgcn_s_barrier();
    asm volatile("" ::: "memory");
    v2u hr[4];
#pragma unroll
    for (int i = 0; i < 4; ++i)
      hr[i] = trd(lds, L_R + (u32)(((w & 2) + (i >> 1)) * 12288 + 4096 + (8 * q + 4 * (i & 1) + rr) * 64 + (((2 * (w & 1) + (p4 >> 1)) ^ (2 * (q & 1))) << 4) + (p4 & 1) * 8));
    LAS char* pw = lds + w * 4096 + l * 16;
    u32 f3b;
    f32x4 o[4][2], mine[4];
    SB();
    sfor<0, 28>([&](auto U_) {
      constexpr int u = decltype(U_)::value;
#pragma unroll
      for (int c = 0; c < 2; ++c)
#pragma unroll
        for (int pp = 0; pp < 4; ++pp) mfa<u % 14 == 0>(o[pp][c], W2[4 * (u % 14) + pp], AC[u][c]);
      SB();
      if constexpr (u == UPOLL) { f3b = __builtin_amdgcn_raw_buffer_load_b32(WS, of3 + 16384, 0, SC1); SB(); ald(12); ald(13); }
      if constexpr (u == UCHK) {
        if (!__all(f3b == tag)) spin(of3 + 16384, tag);
        STAMP(12);
#pragma unroll
        for (int v = 14; v < 20; ++v) ald(v);
      }
      if constexpr (u > UCHK && u <= UCHK + 8) ald(u - UCHK + 19);
      if constexpr (u % 14 == 13) {
        asm volatile("s_nop 7" ::: "memory");
        SB();
        constexpr int hfb = 2 * (u / 14);
#pragma unroll
        for (int pp = 0; pp < 4; ++pp) *(LAS f32x4*)(pw + ((hfb + ((w & 1) ^ 1)) * 16384) + pp * 1024) = o[pp][1];
        if ((w & 2) == hfb) {
#pragma unroll
          for (int pp = 0; pp < 4; ++pp) { mine[pp] = o[pp][0]; asm volatile("" : "+v"(mine[pp])); }
        } else {
#pragma unroll
          for (int pp = 0; pp < 4; ++pp) *(LAS f32x4*)(pw + ((hfb + (w & 1)) * 16384) + pp * 1024) = o[pp][0];
        }
        if constexpr (u == 13) STAMP(11);
        SB();
      }
    });
    STAMP(13);
    BAR();
#pragma unroll
    for (int j = 1; j < 4; ++j)
#pragma unroll
      for (int pp = 0; pp < 4; ++pp) mine[pp] += *(LAS f32x4*)(lds + w * 16384 + ((w + j) & 3) * 4096 + pp * 1024 + l * 16);
    u32 r[8];
#pragma unroll
    for (int i = 0; i < 4; ++i) {
      r[2 * i] = pk(lo2f(hr[i].x) + mine[0][i], hi2f(hr[i].x) + mine[1][i]);
      r[2 * i + 1] = pk(lo2f(hr[i].y) + mine[2][i], hi2f(hr[i].y) + mine[3][i]);
    }
    u16* op = A.out + (1024 * (w >> 1) + 128 * x + 32 * rbl + 16 * (w & 1) + n16) * D + 64 * ct + 8 * q;
    *(v4u*)op = (v4u){r[0], r[1], r[2], r[3]};
    *(v4u*)(op + 32) = (v4u){r[4], r[5], r[6], r[7]};
    if (b == 0 && w == 0 && l == 0) __builtin_amdgcn_raw_buffer_store_b32(tag, WS, O_EP, 0, SC1);
  }
}
}

extern "C" void kernel_launch(void* const* d_in, const int* in_sizes, int n_in, void* d_out, int out_size, void* d_ws, size_t ws_size, hipStream_t stream) {
  fk::Args a;
  a.q = (const fk::u16*)d_in[0]; a.k = (const fk::u16*)d_in[1]; a.v = (const fk::u16*)d_in[2]; a.shk = (const fk::u16*)d_in[3];
  a.ssk = (const fk::u16*)d_in[4]; a.ssd = (const fk::u16*)d_in[5]; a.wg = (const fk::u16*)d_in[7]; a.wu = (const fk::u16*)d_in[8];
  a.wd = (const fk::u16*)d_in[9]; a.out = (fk::u16*)d_out; a.ws = (char*)d_ws;
  fk::fused<<<256, 256, 0, stream>>>(a);
}
```

```cpp
#include <hip/hip_runtime.h>
#include <stdint.h>
#define SIGA 112
#define W1P 5
#define W1A 6
#define N0 45
#define GC 14
#define GS 12
#define PFM 9
#define UFL 3
#define UPOLL 9
#define UCHK 12

namespace fk {
constexpr int WD = 7;
typedef unsigned u32;
typedef unsigned short u16;
typedef unsigned long long u64;
typedef u32 v4u __attribute__((ext_vector_type(4)));
typedef u32 v2u __attribute__((ext_vector_type(2)));
typedef float f32x4 __attribute__((ext_vector_type(4)));
typedef float f32x2 __attribute__((ext_vector_type(2)));
typedef short s16x4 __attribute__((ext_vector_type(4)));
typedef short s16x8 __attribute__((ext_vector_type(8)));
typedef __bf16 bf2 __attribute__((ext_vector_type(2)));
#define LAS __attribute__((address_space(3)))
#define GAS __attribute__((address_space(1)))
#define RLX __ATOMIC_RELAXED, __HIP_MEMORY_SCOPE_AGENT
#define SB() __builtin_amdgcn_sched_barrier(0)
#define WAITV(n) asm volatile("s_waitcnt vmcnt(%0)" ::"n"(n) : "memory")
#define BAR() do { asm volatile("s_waitcnt lgkmcnt(0)" ::: "memory"); __builtin_amdgcn_s_barrier(); asm volatile("" ::: "memory"); } while (0)

constexpr int S = 2048, D = 512, F = 1792;
constexpr u32 O_EP = 0, O_C2 = 16384, O_F2 = 294912, O_ST1 = 65536, O_F1 = 98304, O_ST2 = 131072, O_F3 = 262144;
constexpr u32 O_KVT = 1u << 20, O_QT = 3u << 20, O_HT = 5u << 20, O_W2T = 7u << 20, O_ACT = 9u << 20, O_W1F = 17u << 20, WS_BYTES = 21u << 20;
constexpr u32 O_DIAG = 24u << 20;
constexpr u32 L_R = 114688, L_K = L_R, L_V = L_R + 9216, L_Q = L_R + 17408, L_WR = 0, WR_S = 1056, L_TZ = 36864, TZ_S = 4160, TZ_C = 4 * TZ_S, L_XR = L_R + 33280, XR_S = 4608,
              L_S1 = L_R, L_EX = L_R + 16384, LDS_BYTES = 163840;
constexpr u32 SPIN_CAP = 1u << 18;
constexpr int SC1 = 16;

struct Args { const u16 *q, *k, *v, *shk, *ssk, *ssd, *wg, *wu, *wd; u16* out; char* ws; };

__device__ __forceinline__ u32 f2u(float x) { return __builtin_bit_cast(u32, x); }
__device__ __forceinline__ float u2f(u32 x) { return __builtin_bit_cast(float, x); }
__device__ __forceinline__ float lo2f(u32 x) { return u2f(x << 16); }
__device__ __forceinline__ float hi2f(u32 x) { return u2f(x & 0xffff0000u); }
__device__ __forceinline__ u32 pk(float lo, float hi) { bf2 r = {(__bf16)lo, (__bf16)hi}; return __builtin_bit_cast(u32, r); }
__device__ __forceinline__ float rbf(float x) { return lo2f(pk(x, x)); }
__device__ __forceinline__ f32x4 mfma(v4u a, v4u b, f32x4 c) {
  return __builtin_amdgcn_mfma_f32_16x16x32_bf16(__builtin_bit_cast(s16x8, a), __builtin_bit_cast(s16x8, b), c, 0, 0, 0);
}
template <bool FIRST> __device__ __forceinline__ void mfv(f32x4& c, v4u a, v4u b) {
  if (FIRST) asm volatile("v_mfma_f32_16x16x32_bf16 %0, %1, %2, 0" : "=&v"(c) : "v"(a), "v"(b));
  else asm volatile("v_mfma_f32_16x16x32_bf16 %0, %1, %2, %0" : "+v"(c) : "v"(a), "v"(b));
}
template <bool FIRST> __device__ __forceinline__ void mfa(f32x4& c, v4u a, v4u b) {
  if (FIRST) asm volatile("v_mfma_f32_16x16x32_bf16 %0, %1, %2, 0" : "=&v"(c) : "a"(a), "v"(b));
  else asm volatile("v_mfma_f32_16x16x32_bf16 %0, %1, %2, %0" : "+v"(c) : "a"(a), "v"(b));
}
template <bool FIRST> __device__ __forceinline__ void mfb(f32x4& c, v4u a, v4u b) {
  if (FIRST) asm volatile("v_mfma_f32_16x16x32_bf16 %0, %1, %2, 0" : "=&v"(c) : "v"(a), "a"(b));
  else asm volatile("v_mfma_f32_16x16x32_bf16 %0, %1, %2, %0" : "+v"(c) : "v"(a), "a"(b));
}
__device__ __forceinline__ v2u trd(LAS char* lds, u32 off) {
  return __builtin_bit_cast(v2u, __builtin_amdgcn_ds_read_tr16_b64_v4i16((LAS s16x4*)(lds + off)));
}
__device__ __forceinline__ v4u mkrs(const void* p, u32 bytes) { u64 a = (u64)p; return (v4u){(u32)a, (u32)(a >> 32) & 0xffffu, bytes, 0x00020000u}; }
template <bool COH> __device__ __forceinline__ void dma(u32 lb, u32 lo, u32 voff, v4u rs, u32 sb, u32 so) {
  u32 t;
  if (COH) asm volatile("s_add_u32 m0, %1, %6\n\ts_add_u32 %0, %4, %5\n\tbuffer_load_dwordx4 %2, %3, %0 offen sc1 lds" : "=&s"(t) : "s"(lb), "v"(voff), "s"(rs), "s"(sb), "i"(so), "i"(lo) : "memory", "scc");
  else asm volatile("s_add_u32 m0, %1, %6\n\ts_add_u32 %0, %4, %5\n\tbuffer_load_dwordx4 %2, %3, %0 offen lds" : "=&s"(t) : "s"(lb), "v"(voff), "s"(rs), "s"(sb), "i"(so), "i"(lo) : "memory", "scc");
}
template <int CTRL, int RM> __device__ __forceinline__ float dppadd(float v) {
  return v + __builtin_bit_cast(float, __builtin_amdgcn_update_dpp(0, __builtin_bit_cast(int, v), CTRL, RM, 0xf, true));
}
__device__ __forceinline__ float hsum(float v) {
  v = dppadd<0x111, 0xf>(v); v = dppadd<0x112, 0xf>(v); v = dppadd<0x114, 0xf>(v); v = dppadd<0x118, 0xf>(v);
  return dppadd<0x142, 0xa>(v);
}
#define STAMP(i)
template <int V> struct IC { static constexpr int value = V; };
template <int B, int E, class Fn> __device__ __forceinline__ void sfor(Fn&& f) { if constexpr (B < E) { f(IC<B>{}); sfor<B + 1, E>(f); } }
constexpr int cv_a0(int u) { return u < 16 ? u >> 2 : ((u - 16) >> 2) > 4 ? (u - 16) >> 2 : 4; }
constexpr int cv_first(int u) { int n = 0; for (int j = 0; j < u; ++j) n += (j < 16 ? 4 : 8) - cv_a0(j); return n; }
constexpr int cv_u(int n) { int u = 0; while (cv_first(u + 1) <= n) ++u; return u; }
constexpr int cv_slot(int i) { int s_ = cv_first(i < 16 ? i : i + 16) - 12; if (i < 31) { const int v = cv_slot(i + 1) - 2; if (v < s_) s_ = v; } return s_ & ~1; }
constexpr int cv_at(int n) { for (int i = 0; i < 32; ++i) if (cv_slot(i) == n) return i; return -1; }
constexpr int w1_upto(int n) {
  int c = n < 5 ? 0 : (n - 5) / 3 + 1;
  if (c > W1A) c = W1A;
  if (n >= N0 + 22) c += (n - N0 - 22) / W1P + 1;
  return c < 28 ? c : 28;
}
constexpr int w2_n(int g) { return g < 3 ? 0 : g >= 27 ? 0 : g % 4 != 2; }
constexpr int W2_EPI = 14, W2_EPB = 24;
constexpr int a_n(int g) { return g >= 28 ? 4 : 0; }
constexpr int w2_first(int g) { int n = g > 15 ? W2_EPI : 0; for (int j = 0; j < g; ++j) n += w2_n(j); return n; }
constexpr int vm_tail(int g) { return (g == 4) + (g == GS) + ((g & 15) == 5) + 2 * ((g & 15) == 12) + (g == 15 ? W2_EPI + 4 : 0) + (g == 19) + (g == 25) + 2 * (g == 27); }
constexpr int d_n(int g) { return g >= 27 || (g >= 11 && g < GC) ? 0 : g == GC ? GC - 10 : 1; }
constexpr int vm_ops(int g) { return w2_n(g) + a_n(g) + 2 * d_n(g) + vm_tail(g); }
constexpr int vm_sum(int a, int e) { int n = 0; for (int j = a; j < e; ++j) n += vm_ops(j); return n; }
constexpr int h_wait(int g) {
  if (g < 4) return 2 * (3 - g) + 2 + vm_sum(0, g);
  if (g >= 15 && g - 4 <= GC) return 2 * (GC - (g - 4)) + vm_tail(GC) + vm_sum(GC + 1, g + (g == 15));
  return vm_tail(g - 4) + vm_sum(g - 3, g);
}
static_assert(w2_first(32) + W2_EPB == 56, "all w_down fragments are requested");

__global__ void __launch_bounds__(256, 1) fused(Args A) {
  __shared__ __attribute__((aligned(16))) char smem_[LDS_BYTES];
  LAS char* lds = (LAS char*)smem_;
  const u32 lds0 = (u32)(uintptr_t)lds;
  STAMP(0);
  const int b = blockIdx.x, x = b & 7, y = b >> 3;
  const int l = threadIdx.x & 63, w = __builtin_amdgcn_readfirstlane(threadIdx.x >> 6);
  const int n16 = l & 15, q = l >> 4, rr = (l >> 2) & 3, p4 = l & 3;
  u32 pfd;
  {
    u64 pc;
    asm volatile("s_getpc_b64 %0" : "=s"(pc));
    constexpr u32 CODE = 36864;
    const u32 co = (u32)((((y & (PFM == 9 ? 7 : 3)) * 4 + w) * PFM + (l < PFM ? l : 0)) * 128);
    asm volatile("global_load_dword %0, %1, %2" : "=&v"(pfd) : "v"(co < CODE ? co : 0u), "s"(pc & ~3ull) : "memory");
  }
  const __amdgpu_buffer_rsrc_t WS = __builtin_amdgcn_make_buffer_rsrc(A.ws, 0, WS_BYTES, 0x00020000);
  const v4u rWS = mkrs(A.ws, WS_BYTES);
  auto spin = [&](u32 off, u32 want) {
    for (u32 sp = 0; sp < SPIN_CAP; ++sp) {
      asm volatile("" ::: "memory");
      if (__builtin_amdgcn_raw_buffer_load_b32(WS, off, 0, SC1) == want) off = 0xffffff00u;
      if (__all(off == 0xffffff00u)) break;
    }
  };

  const int ti = 4 * x + (y >> 3), cj = y & 7;
  const int cA = 64 * cj + 16 * w + n16;
  const int srow = 256 * x + 8 * y + 2 * w;
  const int ch = w >> 1, par2 = w & 1, cB = 64 * x + 2 * y + ch;
  v2u shk; v4u sx0, sx1, kz[2];
  v2u kedge; u32 vtag, vdp;
  u32 l16 = l * 16;
  {
    const v4u rK = mkrs(A.k, S * D * 2), rV = mkrs(A.v, S * D * 2), rQ = mkrs(A.q, S * D * 2);
    const u32 c0 = ((l & 7) ^ ((l >> 3) & 2)) * 16, tv = (u32)((64 * ti + (l >> 3)) * 1024 + 128 * cj);
    const u32 va = tv + c0, vb = tv + (c0 ^ 64);
    const u32 vk = ((w & 1) ? vb : va) + (u32)(w * 8192 - 8192);
    dma<false>(lds0 + w * 1024, L_K, vk, rK, 0, 0);
    dma<false>(lds0 + w * 1024, L_K + 4096, vk + 32768, rK, 0, 0);
    if (w == 0) dma<false>(lds0, L_K + 8192, vk + 65536, rK, 0, 0);
    dma<false>(lds0 + w * 2048, L_V, va, rV, w * 16384, 0);
    dma<false>(lds0 + w * 2048, L_V + 1024, vb, rV, w * 16384, 8192);
    asm volatile("global_load_dwordx2 %0, %1, %2" : "=&v"(shk) : "v"(cA * 8), "s"(A.shk) : "memory");
    asm volatile("global_load_dwordx4 %0, %2, %3\n\tglobal_load_dwordx4 %1, %2, %3 offset:1024" : "=&v"(kz[0]), "=&v"(kz[1]) : "v"(l16), "s"(A.ssk + cB * S + 1024 * par2) : "memory");
    asm volatile("global_load_dwordx2 %0, %3, %4 offset:2048\n\tglobal_load_dword %1, %3, %5\n\tglobal_load_dword %2, %3, %6"
                 : "=&v"(kedge), "=&v"(vtag), "=&v"(vdp) : "v"(0), "s"(A.ssk + cB * S), "s"(A.ws + O_EP), "s"(A.ssd + 64 * x + 2 * y) : "memory");
    dma<false>(lds0 + w * 2048, L_Q, va, rQ, w * 16384, 0);
    dma<false>(lds0 + w * 2048, L_Q + 1024, vb, rQ, w * 16384, 8192);
    asm volatile("global_load_dwordx4 %0, %2, %3\n\tglobal_load_dwordx4 %1, %2, %3 offset:512" : "=&v"(sx0), "=&v"(sx1) : "v"((l >> 5) * 1024 + (l & 31) * 16), "s"(A.q + srow * D) : "memory");
  }
  const int rb = 8 * x + 2 * (y >> 3) + ((y >> 2) & 1), fg = y & 3;
  const v4u rW = mkrs(ch ? A.wu : A.wg, D * F * 2);
  u32 wrv = l < 56 ? l16 : 0xffffff00u, w1d[4];
  LAS char* pW = lds + L_WR + ((p4 >> 1) * 8 + rr) * WR_S + (4 * w + q) * 16 + (p4 & 1) * 8;
#pragma unroll
  for (int j = 0; j < 4; ++j) {
    int Z = 16 * j + 4 * w + q;
    asm volatile("" : "+v"(Z));
    const int c = (Z * 37) >> 8;
    w1d[j] = (j < 3 || Z < 56) ? O_W1F + (u32)(((8 * fg + c) * 16 + (rb >> 2)) * 7168 + (Z - 7 * c) * 1024 + (rb & 3) * 256 + (4 * ((n16 & 7) >> 1) + 2 * (n16 >> 3) + (n16 & 1)) * 16) : 0xffffff00u;
  }
  const u32 aAo = (u32)((8 * q + rr) * 128 + (((2 * w + (p4 >> 1)) ^ (rr & 2) ^ ((q & 1) << 2)) << 4) + (p4 & 1) * 8);
  LAS char* pA = lds + L_K + aAo;
  LAS char* pB = lds + L_K + (aAo ^ 64);
  u32 okv = O_KVT + cA * 4096 + (2040 - 32 - 64 * ti - 8 * q) * 2, oqt = O_QT + cA * 4096 + (64 * ti + 8 * q) * 2;
  LAS char* tw = lds + L_TZ + ch * TZ_C + (128 * par2 + l) * 16 + 32;
  u32 og = (l & 31) == 31 ? O_ST1 + (srow + (l >> 5)) * 8 : 0xffffff00u;
  u32 oci = (x == 0 && y < 8 && w == 0 && l < 8) ? O_C2 + l * 2048 + y * 256 : 0xffffff00u;
  asm volatile("" : "+v"(oci), "+v"(w1d[0]), "+v"(w1d[1]), "+v"(w1d[2]), "+v"(w1d[3]), "+v"(pA), "+v"(pB), "+v"(okv), "+v"(oqt), "+v"(tw), "+v"(og), "+v"(wrv), "+v"(pW));
  const int wU = b < 224 ? 2 * b + ch : 0;
  u32 ow[4], w2o = b < 224 ? O_W2T + (u32)(((wU & 7) * 56 + (wU >> 3)) * 4096 + par2 * 2048) + l16 : 0xffff0000u;
  {
    const int UU = 4 * (wU >> 3) + q, f = UU / 7, u = UU - 7 * f;
    const int col = 64 * (wU & 7) + 8 * rr + 4 * (p4 & 1) + 32 * (p4 >> 1) + 2 * par2;
#pragma unroll
    for (int d = 0; d < 4; ++d) ow[d] = (u32)(((56 * f + (u < 4 ? 2 * u + 8 * d : 8 * u + 2 * d)) * D + col) * 2);
  }
  asm volatile("" : "+v"(w2o), "+v"(ow[0]), "+v"(ow[1]), "+v"(ow[2]), "+v"(ow[3]));
  if (w == 0) *(LAS v4u*)(lds + L_XR + (l >> 5) * XR_S + 4096 + (l & 31) * 16) = (v4u){0, 0, 0, 0};
  SB();
  STAMP(23);
  asm volatile("s_waitcnt vmcnt(9)" : "+v"(shk), "+v"(pfd)::"memory");
  BAR();
  STAMP(1);
  u32 tag, dpair;
  {
    v2u ka[2], kb[2], kc[2], va[2], vb[2];
#pragma unroll
    for (int it = 0; it < 2; ++it) { ka[it] = trd(pA, it * 4096 + 512); kb[it] = trd(pB, it * 4096 + 1024); kc[it] = trd(pB, it * 4096 + 1536); }
#pragma unroll
    for (int it = 0; it < 2; ++it) { va[it] = trd(pA, L_V - L_K + it * 4096); vb[it] = trd(pA, L_V - L_K + it * 4096 + 512); }
    SB();
#pragma unroll
    for (int j = 0; j < 4; ++j) dma<false>(lds0 + (8 * ch + 4 * par2) * WR_S, L_WR + j * WR_S, wrv, rW, (8 * rb + 4 * par2) * (F * 2) + fg * 896, j * (F * 2));
    SB();
    auto LO = [](u32 a, u32 c) { return (f32x2){lo2f(a), lo2f(c)}; };
    auto HI = [](u32 a, u32 c) { return (f32x2){hi2f(a), hi2f(c)}; };
    const f32x2 w0 = LO(shk.x, shk.x), w1 = HI(shk.x, shk.x), w2 = LO(shk.y, shk.y), w3 = HI(shk.y, shk.y);
    const f32x2 e[12] = {w0, HI(ka[0].x, ka[1].x), LO(ka[0].y, ka[1].y), HI(ka[0].y, ka[1].y), LO(kb[0].x, kb[1].x), HI(kb[0].x, kb[1].x), LO(kb[0].y, kb[1].y),
                         HI(kb[0].y, kb[1].y), LO(kc[0].x, kc[1].x), HI(kc[0].x, kc[1].x), LO(kc[0].y, kc[1].y), HI(kc[0].y, kc[1].y)};
    const f32x2 vv[8] = {LO(va[0].x, va[1].x), HI(va[0].x, va[1].x), LO(va[0].y, va[1].y), HI(va[0].y, va[1].y), LO(vb[0].x, vb[1].x), HI(vb[0].x, vb[1].x), LO(vb[0].y, vb[1].y), HI(vb[0].y, vb[1].y)};
    f32x2 kv[8];
#pragma unroll
    for (int s = 0; s < 8; ++s) kv[s] = (w3 * e[s + 1] + w2 * e[s + 2] + w1 * e[s + 3] + w0 * e[s + 4]) * vv[s];
#pragma unroll
    for (int it = 0; it < 2; ++it)
      __builtin_amdgcn_raw_buffer_store_b128((v4u){pk(kv[7][it], kv[6][it]), pk(kv[5][it], kv[4][it]), pk(kv[3][it], kv[2][it]), pk(kv[1][it], kv[0][it])}, WS, okv + (1 - it) * 64, 0, SC1);
    SB();
    asm volatile("s_waitcnt vmcnt(8)" : "+v"(kz[0]), "+v"(kz[1]), "+v"(kedge), "+v"(vtag), "+v"(vdp)::"memory");
    BAR();
    vtag += 1u;
    tag = __builtin_amdgcn_readfirstlane(vtag); dpair = __builtin_amdgcn_readfirstlane(vdp);
    v2u qa[2], qb[2];
#pragma unroll
    for (int it = 0; it < 2; ++it) { qa[it] = trd(pA, L_Q - L_K + it * 4096); qb[it] = trd(pA, L_Q - L_K + it * 4096 + 512); }
#pragma unroll
    for (int it = 0; it < 2; ++it) __builtin_amdgcn_raw_buffer_store_b128((v4u){qa[it].x, qa[it].y, qb[it].x, qb[it].y}, WS, oqt + it * 64, 0, SC1);
    __builtin_amdgcn_raw_buffer_store_b32(tag * 64u, WS, oci, 0, SC1);
  }
  SB();
  STAMP(24);
  {
    auto put = [&](LAS char* p, v4u d, u32 d4, u32 d5) {
      const u32 e0 = __builtin_amdgcn_alignbit(d.y, d.x, 16), e1 = __builtin_amdgcn_alignbit(d.z, d.y, 16), e2 = __builtin_amdgcn_alignbit(d.w, d.z, 16),
                e3 = __builtin_amdgcn_alignbit(d4, d.w, 16), e4 = __builtin_amdgcn_alignbit(d5, d4, 16);
      *(LAS v4u*)p = d; *(LAS v4u*)(p + TZ_S) = (v4u){e0, e1, e2, e3}; *(LAS v4u*)(p + 2 * TZ_S) = (v4u){d.y, d.z, d.w, d4}; *(LAS v4u*)(p + 3 * TZ_S) = (v4u){e1, e2, e3, e4};
    };
    auto nxt = [](u32 last, u32 v) { return (u32)__builtin_amdgcn_update_dpp((int)last, (int)v, 0x130, 0xf, 0xf, false); };
    put(tw, kz[0], nxt(__builtin_amdgcn_readlane(kz[1].x, 0), kz[0].x), nxt(__builtin_amdgcn_readlane(kz[1].y, 0), kz[0].y));
    put(tw + 1024, kz[1], nxt(par2 ? 0u : kedge.x, kz[1].x), nxt(par2 ? 0u : kedge.y, kz[1].y));
    const u32 f0 = __builtin_amdgcn_readlane(kz[0].x, 0), f1 = __builtin_amdgcn_readlane(kz[0].y, 0);
    if (l < 2 && !par2) put(tw - 32, (v4u){0, 0, 0, 0}, l ? f0 : 0u, l ? f1 : 0u);
  }
  SB();
  v2u st1;
  asm volatile("s_waitcnt vmcnt(9)" : "+v"(sx0), "+v"(sx1)::"memory");
  {
    const f32x2 v[8] = {{lo2f(sx0.x), hi2f(sx0.x)}, {lo2f(sx0.y), hi2f(sx0.y)}, {lo2f(sx0.z), hi2f(sx0.z)}, {lo2f(sx0.w), hi2f(sx0.w)},
                        {lo2f(sx1.x), hi2f(sx1.x)}, {lo2f(sx1.y), hi2f(sx1.y)}, {lo2f(sx1.z), hi2f(sx1.z)}, {lo2f(sx1.w), hi2f(sx1.w)}};
    const f32x2 s2 = ((v[0] + v[1]) + (v[2] + v[3])) + ((v[4] + v[5]) + (v[6] + v[7]));
    const float sm = hsum(s2[0] + s2[1]);
    const float m0 = u2f(__builtin_amdgcn_readlane(f2u(sm), 31)), m1 = u2f(__builtin_amdgcn_readlane(f2u(sm), 63));
    const float mean = (l < 32 ? m0 : m1) * (1.f / D);
    f32x2 ss = {0.f, 0.f};
#pragma unroll
    for (int j = 0; j < 8; ++j) { const f32x2 d = v[j] - mean; ss += d * d; }
    const float var = hsum(ss[0] + ss[1]) * (1.f / D);
    st1 = (v2u){pk(mean, __builtin_amdgcn_sqrtf(rbf(rbf(var) + 1e-5f))), vtag};
    __builtin_amdgcn_raw_buffer_store_b64(st1, WS, og, 0, SC1);
  }
  SB();
  WAITV(6);
  BAR();
  {
    v2u ta[4], tc[4];
#pragma unroll
    for (int j = 0; j < 4; ++j) { ta[j] = trd(pW, j * 256); tc[j] = trd(pW, j * 256 + 4 * WR_S); }
#pragma unroll
    for (int j = 0; j < 4; ++j) __builtin_amdgcn_raw_buffer_store_b128((v4u){ta[j].x, ta[j].y, tc[j].x, tc[j].y}, WS, w1d[j], 0, SC1);
  }
  SB();
  WAITV(0);
  BAR();
  STAMP(2);
  if (w == 0 && l == 0) __builtin_amdgcn_raw_buffer_store_b32(tag, WS, O_F1 + b * 128, 0, SC1);
  u32 w2raw[8];
  asm volatile("global_load_dword %0, %8, %12\n\tglobal_load_dword %1, %8, %12 offset:1024\n\tglobal_load_dword %2, %9, %12\n\tglobal_load_dword %3, %9, %12 offset:1024\n\t"
               "global_load_dword %4, %10, %12\n\tglobal_load_dword %5, %10, %12 offset:1024\n\tglobal_load_dword %6, %11, %12\n\tglobal_load_dword %7, %11, %12 offset:1024"
               : "=&v"(w2raw[0]), "=&v"(w2raw[1]), "=&v"(w2raw[2]), "=&v"(w2raw[3]), "=&v"(w2raw[4]), "=&v"(w2raw[5]), "=&v"(w2raw[6]), "=&v"(w2raw[7])
               : "v"(ow[0]), "v"(ow[1]), "v"(ow[2]), "v"(ow[3]), "s"(A.wd) : "memory");
  v4u T[32];
  {
    const u32 tb = lds0 + L_TZ + ch * TZ_C + ((n16 + 1) & 3) * TZ_S + (u32)((((n16 + 1) >> 2) + 2 * q + 8 * par2) * 8);
#pragma unroll
    for (int i = 0; i < 32; ++i) {
      v2u a, c;
      asm volatile("ds_read_b64 %0, %2 offset:%3\n\tds_read_b64 %1, %2 offset:%4" : "=&v"(a), "=&v"(c) : "v"(tb), "i"(128 * i), "i"(128 * i + 8) : "memory");
      T[i] = (v4u){a.x, a.y, c.x, c.y};
    }
    asm volatile("s_waitcnt lgkmcnt(0)" : "+v"(T[0]), "+v"(T[1]), "+v"(T[2]), "+v"(T[3]), "+v"(T[4]), "+v"(T[5]), "+v"(T[6]), "+v"(T[7]), "+v"(T[8]), "+v"(T[9]), "+v"(T[10]),
                 "+v"(T[11]), "+v"(T[12]), "+v"(T[13]), "+v"(T[14]), "+v"(T[15])::"memory");
    asm volatile("" : "+v"(T[16]), "+v"(T[17]), "+v"(T[18]), "+v"(T[19]), "+v"(T[20]), "+v"(T[21]), "+v"(T[22]), "+v"(T[23]), "+v"(T[24]), "+v"(T[25]), "+v"(T[26]),
                 "+v"(T[27]), "+v"(T[28]), "+v"(T[29]), "+v"(T[30]), "+v"(T[31])::"memory");
  }
  const int tB = 512 * par2 + 16 * n16 + 4 * q;
  LAS char* xp = lds + L_XR + ch * XR_S + (2032 - 16 * n16 + 32 * par2 + 8 * q) * 2 - 3584;
  LAS char* kp = lds + L_XR + ch * XR_S + (2044 - 1280 - tB) * 2;
  LAS char* gp = lds + L_S1 + tB * 8;
  LAS char* ep = lds + L_EX + w * 4096 + l * 16;
  LAS char* er = lds + L_EX + (w ^ 1) * 4096 + l * 16;
  u32 qo = O_QT + cB * 4096 + tB * 2;
  u32 oh = O_HT + (u32)((8 * (2 * par2 + (q & 1)) + (n16 >> 1)) * 32768 + (cB >> 5) * 2048 + (cB & 31) * 64 + (((2 * (n16 & 1) + (q >> 1)) ^ (2 * ((cB >> 3) & 1))) << 4));
  asm volatile("" : "+v"(xp), "+v"(kp), "+v"(gp), "+v"(ep), "+v"(er), "+v"(qo), "+v"(oh), "+v"(l16));
  const int hp = 4 * (x >> 2) + (y >> 3), fch = 8 * (x & 3) + (y & 7);
  const int ct = y & 7, rbl = y >> 3;
  const u32 ring = L_R + w * 12288;
  const int rot = fch & 3, spar = (fch >> 2) & 1;
  u32 w2v = O_W2T + (u32)((ct * 56 + 14 * w) * 4096) + l16;
  u32 ost = (fch < 8 && rr == q) ? O_ST2 + (u32)(rot * 32768 + (128 * hp + 32 * w + 16 * spar + n16) * 16) : 0xffff0000u;
  u32 og2 = O_ST2 + (u32)((l >> 5) * 65536 + (128 * hp + 32 * w + (l & 31)) * 16);
  u32 oact = O_ACT + (u32)(((8 * hp + 2 * w) * 224 + 7 * fch + q) * 256 + n16 * 16), oact2 = q < 3 ? oact + 1024 : 0xff000000u;
  u32 of3 = O_F3 + (u32)(((x * 4 + rbl) * 4 + w) * 128 + (l & 7) * 4), of3w = l == 0 ? O_F3 + (u32)(((hp * 4 + w) * 4 + (fch >> 3)) * 128 + (fch & 7) * 4) : 0xffff0000u;
  u32 av = O_ACT + (u32)(((8 * x + 2 * rbl) * 224 + 56 * w) * 256) + l16;
  LAS char* hx[2];
#pragma unroll
  for (int pr = 0; pr < 2; ++pr) hx[pr] = lds + ring + (8 * q + rr) * 64 + (((2 * (pr ^ spar) + (p4 >> 1)) ^ (2 * (q & 1))) << 4) + (p4 & 1) * 8;
  LAS char* pcs = lds + ring + 10240 + (n16 == 0 ? q * 16 : 128 + l * 16);
  LAS char* pcr = lds + L_R + 10240 + q * 16;
  asm volatile("" : "+v"(av), "+v"(pcs), "+v"(pcr), "+v"(w2v), "+v"(ost), "+v"(og2), "+v"(oact), "+v"(oact2), "+v"(of3), "+v"(of3w), "+v"(hx[0]), "+v"(hx[1]));
  u32 of1 = O_F1 + (u32)((l & 7) + 8 * ((x & 3) + 4 * (l >> 3))) * 128, of2 = O_F2 + (u32)(2 * ((rot + w) & 3) + (l & 1) + 8 * (l >> 1)) * 128, of2w = (w == 0 && l == 0) ? O_F2 + (u32)b * 128 : 0xffff0000u;
  asm volatile("" : "+v"(of1), "+v"(of2), "+v"(of2w));
  STAMP(21);
  if (w == 0) spin(of1, tag);
  BAR();
  STAMP(3);

  dma<true>(lds0 + ch * XR_S + par2 * 2048, L_XR, l16, rWS, cB * 4096 + par2 * 2048, O_KVT);
  dma<true>(lds0 + ch * XR_S + par2 * 2048, L_XR + 1024, l16, rWS, cB * 4096 + par2 * 2048, O_KVT + 1024);
#pragma unroll
  for (int i = 0; i < 4; ++i) dma<true>(lds0 + w * 4096, L_S1 + i * 1024, l16, rWS, w * 4096, O_ST1 + i * 1024);
  v2u xq[4];
  asm volatile("buffer_load_dwordx2 %0, %4, %5, 0 offen sc1\n\tbuffer_load_dwordx2 %1, %4, %5, 0 offen offset:512 sc1\n\t"
               "buffer_load_dwordx2 %2, %4, %5, 0 offen offset:2048 sc1\n\tbuffer_load_dwordx2 %3, %4, %5, 0 offen offset:2560 sc1"
               : "=&v"(xq[0]), "=&v"(xq[1]), "=&v"(xq[2]), "=&v"(xq[3]) : "v"(qo), "s"(rWS) : "memory");
  SB();
  asm volatile("s_waitcnt vmcnt(8)" : "+v"(w2raw[0]), "+v"(w2raw[1]), "+v"(w2raw[2]), "+v"(w2raw[3]), "+v"(w2raw[4]), "+v"(w2raw[5]), "+v"(w2raw[6]), "+v"(w2raw[7])::"memory");
  BAR();
  STAMP(4);
  GAS u32* pc2 = (GAS u32*)(A.ws + O_C2 + (l & 7) * 2048 + x * 256);
  const u32 oc2 = O_C2 + x * 2048 + (l & 7) * 256, c2want = tag * 64u + 32u;
  v4u W2[56];
  auto w2l = [&](int i) { W2[i] = __builtin_amdgcn_raw_buffer_load_b128(WS, w2v + (i & 3) * 1024, (i >> 2) * 4096, SC1); };
  u32 hpk[4];
  auto put_h = [&](int h) {
    const auto s0 = __builtin_amdgcn_permlane16_swap(hpk[0], hpk[2], false, false);
    const auto s1 = __builtin_amdgcn_permlane16_swap(hpk[1], hpk[3], false, false);
    __builtin_amdgcn_raw_buffer_store_b128((v4u){s0[0], s1[0], s0[1], s1[1]}, WS, oh + h * (32 * 32768), 0, SC1);
  };
  {
    u32 w1so;
    auto w1l = [&](int i) {
      if ((i & 3) == 0) asm volatile("s_add_u32 m0, %1, %2\n\ts_add_u32 %0, %3, %4\n\tbuffer_load_dwordx4 %5, %6, %0 offen sc1 lds"
                                     : "=&s"(w1so) : "s"(lds0 + w * 28672), "i"(i * 1024), "s"(fch * 114688 + w * 28672), "i"(O_W1F + i * 1024), "v"(l16), "s"(rWS) : "memory", "scc");
      else asm volatile("buffer_load_dwordx4 %0, %1, %2 offen offset:%3 sc1 lds" ::"v"(l16), "s"(rWS), "s"(w1so), "i"((i & 3) * 1024) : "memory");
    };
    const float dsc = ch ? hi2f(dpair) : lo2f(dpair);
    f32x4 cacc[8], mn[2], ot[2];
    v4u B[32], g1[8], w2f[2]; v2u kr[4];
    float xf[16], ln[16], kd[16], tm[16], hh[8];
    auto fix = [&](int h) {
      u32 df = 0;
#pragma unroll
      for (int i = 4 * h; i < 4 * h + 4; ++i) df |= (g1[i].y ^ vtag) | (g1[i].w ^ vtag);
      if (__builtin_amdgcn_ballot_w64(df != 0)) {
        bool ok;
        for (u32 sp = 0; sp < SPIN_CAP; ++sp) {
          asm volatile("" ::: "memory");
          ok = true;
#pragma unroll
          for (int i = 4 * h; i < 4 * h + 4; ++i) {
            g1[i] = __builtin_amdgcn_raw_buffer_load_b128(WS, O_ST1 + (tB + 256 * ((i >> 1) & 1) + 1024 * h) * 8 + (i & 1) * 16, 0, SC1);
            ok &= (g1[i].y == tag) & (g1[i].w == tag);
          }
          if (__all(ok)) break;
        }
#pragma unroll
        for (int j = 8 * h; j < 8 * h + 8; ++j) { const u32 pay = g1[j >> 1][(j & 1) * 2]; ln[j] = (xf[j] - lo2f(pay)) * __builtin_amdgcn_rcpf(hi2f(pay)); }
      }
    };
    auto give = [&](int h) {
      if (par2) { *(LAS f32x4*)(ep + 2048 * h) = cacc[4 * h]; *(LAS f32x4*)(ep + 2048 * h + 1024) = cacc[4 * h + 1]; mn[0] = cacc[4 * h + 2]; mn[1] = cacc[4 * h + 3]; asm volatile("" : "+v"(mn[0]), "+v"(mn[1])); }
      else { *(LAS f32x4*)(ep + 2048 * h) = cacc[4 * h + 2]; *(LAS f32x4*)(ep + 2048 * h + 1024) = cacc[4 * h + 3]; mn[0] = cacc[4 * h]; mn[1] = cacc[4 * h + 1]; asm volatile("" : "+v"(mn[0]), "+v"(mn[1])); }
    };
    auto pre = [&](auto J_, auto P_) {
      constexpr int j = decltype(J_)::value, ph = decltype(P_)::value;
      const u32 pay = g1[j >> 1][(j & 1) * 2];
      if constexpr (ph == 0) { xf[j] = (j & 1) ? hi2f(xq[j >> 2][(j & 3) >> 1]) : lo2f(xq[j >> 2][(j & 3) >> 1]); asm volatile("" : "+v"(xf[j])); }
      if constexpr (ph == 1) { tm[j] = __builtin_amdgcn_rcpf(hi2f(pay)); asm volatile("" : "+v"(tm[j])); }
      if constexpr (ph == 2) { ln[j] = xf[j] - lo2f(pay); asm volatile("" : "+v"(ln[j])); }
      if constexpr (ph == 3) { ln[j] *= tm[j]; asm volatile("" : "+v"(ln[j])); }
      if constexpr (ph == 4) { const u32 kk = kr[j >> 2][1 - ((j & 3) >> 1)]; kd[j] = (j & 1) ? lo2f(kk) : hi2f(kk); asm volatile("" : "+v"(kd[j])); }
    };
    sfor<0, 32>([&](auto U_) { constexpr int u = decltype(U_)::value; if constexpr (cv_slot(u) < 0) B[u] = *(LAS v4u*)(xp + 128 * (31 - u)); });
    SB();
    sfor<0, 144>([&](auto N_) {
      constexpr int n = decltype(N_)::value, u = cv_u(n), i = u < 16 ? u : u - 16, a = cv_a0(u) + n - cv_first(u), eps = 4 * a - i + 3;
      mfb<i == 0>(cacc[a], T[eps], B[i]);
      SB();
      constexpr int ub = cv_at(n);
      if constexpr (ub >= 0) B[ub] = *(LAS v4u*)(xp + 128 * (31 - ub));
      if constexpr (w1_upto(n) > w1_upto(n - 1)) w1l(w1_upto(n) - 1);
      if constexpr (n < 4) {
        constexpr int pp = n / 2, d = 2 * (n & 1);
        w2f[pp][d] = __builtin_amdgcn_perm(w2raw[2 * d + 1], w2raw[2 * d], pp ? 0x07060302u : 0x05040100u);
        w2f[pp][d + 1] = __builtin_amdgcn_perm(w2raw[2 * d + 3], w2raw[2 * d + 2], pp ? 0x07060302u : 0x05040100u);
        if constexpr (n & 1) __builtin_amdgcn_raw_buffer_store_b128(w2f[pp], WS, w2o + pp * 1024, 0, SC1); else asm volatile("" : "+v"(w2f[pp]));
      }
      if constexpr (n == 4 || n == 6) kr[(n - 4) / 2] = *(LAS v2u*)(kp + 2560 - 512 * ((n - 4) / 2));
      if constexpr (n >= 23 && n < 39 && n % 2 == 1) pre(IC<(n - 23) / 2>{}, IC<4>{});
      if constexpr (n == 44) give(0);
      if constexpr (n == N0) {
        constexpr int nw = 2 + w1_upto(N0);
        asm volatile("s_waitcnt vmcnt(%4) lgkmcnt(0)" : "+v"(xq[0]), "+v"(xq[1]), "+v"(xq[2]), "+v"(xq[3]) : "n"(nw) : "memory");
        __builtin_amdgcn_s_barrier();
        asm volatile("" ::: "memory");
      }
      if constexpr (n == N0 + 1 || n == N0 + 2) { constexpr int k = n - N0 - 1; g1[2 * k] = *(LAS v4u*)(gp + k * 2048); g1[2 * k + 1] = *(LAS v4u*)(gp + k * 2048 + 16); ot[k] = *(LAS f32x4*)(er + k * 1024); }
      if constexpr (n == N0 + 3 || n == N0 + 4) { constexpr int k = 4 * (n - N0 - 3); pre(IC<k>{}, IC<0>{}); pre(IC<k + 1>{}, IC<0>{}); pre(IC<k + 2>{}, IC<0>{}); pre(IC<k + 3>{}, IC<0>{}); }
      if constexpr (n >= N0 + 5 && n < N0 + 13) {
        constexpr int j = n - N0 - 5;
        pre(IC<j>{}, IC<1>{}); pre(IC<j>{}, IC<2>{});
        if constexpr (j > 0) pre(IC<j - 1>{}, IC<3>{});
      }
      if constexpr (n == N0 + 13) { pre(IC<7>{}, IC<3>{}); fix(0); }
      if constexpr (n >= N0 + 14 && n < N0 + 18) {
#pragma unroll
        for (int j = 2 * (n - N0 - 14); j < 2 * (n - N0 - 14) + 2; ++j) { hh[j] = xf[j] + ln[j] * ((kd[j] * dsc + mn[j >> 2][j & 3]) + ot[j >> 2][j & 3]); asm volatile("" : "+v"(hh[j])); }
      }
      if constexpr (n == N0 + 18 || n == N0 + 19) { constexpr int k = n - N0 - 18; hpk[2 * k] = pk(hh[4 * k], hh[4 * k + 1]); hpk[2 * k + 1] = pk(hh[4 * k + 2], hh[4 * k + 3]); asm volatile("" : "+v"(hpk[2 * k]), "+v"(hpk[2 * k + 1])); }
      if constexpr (n == N0 + 20) put_h(0);
      if constexpr (n == 70 || n == 74) kr[2 + (n - 70) / 4] = *(LAS v2u*)(kp + 512 - 512 * ((n - 70) / 4));
      if constexpr (n >= 78 && n < 94 && n % 4 == 2) { constexpr int j = 4 + (n - 78) / 4; g1[j] = *(LAS v4u*)(gp + 8192 + ((j >> 1) & 1) * 2048 + (j & 1) * 16); }
      if constexpr (n == SIGA) {
        constexpr int nw = w1_upto(SIGA) - w1_upto(N0 + 20);
        WAITV(nw);
        __builtin_amdgcn_s_barrier();
        __builtin_amdgcn_raw_buffer_store_b32(vtag, WS, of2w, 0, SC1);
      }
      if constexpr (n >= 96 && n < 136) pre(IC<8 + (n - 96) / 5>{}, IC<(n - 96) % 5>{});
      SB();
    });
    asm volatile("s_nop 7" ::: "memory");
    SB();
    STAMP(5);
    sfor<w1_upto(143), 28>([&](auto I_) { w1l(decltype(I_)::value); });
    give(1);
    fix(1);
    BAR();
#pragma unroll
    for (int aa = 0; aa < 2; ++aa) {
      const f32x4 o = *(LAS f32x4*)(er + 2048 + aa * 1024);
#pragma unroll
      for (int i = 0; i < 4; ++i) hh[4 * aa + i] = xf[8 + 4 * aa + i] + ln[8 + 4 * aa + i] * ((kd[8 + 4 * aa + i] * dsc + mn[aa][i]) + o[i]);
      hpk[2 * aa] = pk(hh[4 * aa], hh[4 * aa + 1]); hpk[2 * aa + 1] = pk(hh[4 * aa + 2], hh[4 * aa + 3]);
    }
    asm volatile("" : "+v"(hpk[0]), "+v"(hpk[1]), "+v"(hpk[2]), "+v"(hpk[3]));
    STAMP(6);
  }

  v4u ones = {0x3f803f80u, 0x3f803f80u, 0x3f803f80u, 0x3f803f80u};
  LAS char* wp0 = lds + l * 16;
  asm volatile("" : "+v"(ones), "+v"(wp0));
  u32 sg[4];
#pragma unroll
  for (int g = 0; g < 4; ++g) sg[g] = (u32)((g + rot) & 3);
  LAS char* wp = wp0 + sg[0] * 28672;
  v4u wf[WD + 1], hf[2][2], g2[2];
  WAITV(0);
  STAMP(7);
  if (w < 2) spin(of2, tag);
  BAR();
  STAMP(8);
  f32x4 cs[7];
  u32 fl3 = 0, fl2 = 0;
  v4u AC[28][2];
  const u32 ao[2] = {(u32)((w & 1) * (224 * 256)), (u32)(((w & 1) ^ 1) * (224 * 256))};
  auto ald = [&](int u) {
#pragma unroll
    for (int c = 0; c < 2; ++c) { AC[u][c] = __builtin_amdgcn_raw_buffer_load_b128(WS, av + (u % 14 & 3) * 1024, ao[c] + (u % 14 >> 2) * 4096 + (u / 14) * (64 * 224 * 256), SC1); SB(); }
  };
  {
    u32 hso;
    auto hdma = [&](int g, int n) {
      if (n == 0) asm volatile("s_add_u32 m0, %1, %2\n\ts_add_u32 %0, %3, %4\n\tbuffer_load_dwordx4 %5, %6, %0 offen sc1 lds"
                               : "=&s"(hso) : "s"(lds0 + ring), "i"((g % 5) * 2048), "s"((4 * hp + w) * 32768 + sg[(g >> 2) & 3] * 8192), "i"(O_HT + (g >> 4) * (32 * 32768) + (g & 3) * 2048), "v"(l16), "s"(rWS) : "memory", "scc");
      else asm volatile("buffer_load_dwordx4 %0, %1, %2 offen offset:1024 sc1 lds" ::"v"(l16), "s"(rWS), "s"(hso) : "memory");
    };
    auto hfrag = [&](int g, int pr, int t) { return trd(hx[pr], (g % 5) * 2048 + t * 256); };
    f32x4 acc[7][2], sS, sQ;
    auto epilogue = [&](auto P_) {
      constexpr int ps = decltype(P_)::value;
      if (!__all((g2[0].y == tag) & (g2[0].w == tag) & (g2[1].y == tag) & (g2[1].w == tag))) {
        for (u32 sp = 0; sp < SPIN_CAP; ++sp) {
          asm volatile("" ::: "memory");
#pragma unroll
          for (int k = 0; k < 2; ++k) g2[k] = __builtin_amdgcn_raw_buffer_load_b128(WS, og2, k * 32768 + ps * 16384, SC1);
          if (__all((g2[0].y == tag) & (g2[0].w == tag) & (g2[1].y == tag) & (g2[1].w == tag))) break;
        }
      }
      const u32 s1 = f2u(u2f(g2[0].x) + u2f(g2[1].x)), s2 = f2u(u2f(g2[0].z) + u2f(g2[1].z));
      const auto h1 = __builtin_amdgcn_permlane32_swap(s1, s1, false, false), h2 = __builtin_amdgcn_permlane32_swap(s2, s2, false, false);
      const float mean = (u2f(h1[0]) + u2f(h1[1])) * (1.f / D);
      const float v2 = __builtin_fmaxf((u2f(h2[0]) + u2f(h2[1])) * (1.f / D) - mean * mean, 0.f) + 1e-5f;
      const u32 e0 = f2u(-mean), e1 = f2u(-1.44269504089f * __builtin_amdgcn_rsqf(v2)), e2 = f2u(v2);
      const auto b0 = __builtin_amdgcn_permlane16_swap(e0, e0, false, false), b1 = __builtin_amdgcn_permlane16_swap(e1, e1, false, false),
                 b2 = __builtin_amdgcn_permlane16_swap(e2, e2, false, false);
      v4u sa[2], sb[2];
#pragma unroll
      for (int pr = 0; pr < 2; ++pr) {
        const bool up = (pr ^ spar) != 0;
        const float nm = u2f(up ? b0[1] : b0[0]), k1 = u2f(up ? b1[1] : b1[0]), vv = u2f(up ? b2[1] : b2[0]);
        u32 d[8];
        f32x2 a[7], bb[7], e[7];
#pragma unroll
        for (int t = 0; t < 7; ++t) a[t] = (f32x2){acc[t][pr][0], acc[t][pr][1]} + nm * (f32x2){cs[t][0], cs[t][1]};
        SB();
#pragma unroll
        for (int t = 0; t < 7; ++t) e[t] = a[t] * k1;
        SB();
#pragma unroll
        for (int t = 0; t < 7; ++t) {
          e[t] = (f32x2){__builtin_amdgcn_exp2f(e[t][0]), __builtin_amdgcn_exp2f(e[t][1])};
          bb[t] = (f32x2){acc[t][pr][2], acc[t][pr][3]} + nm * (f32x2){cs[t][2], cs[t][3]};
          if (ps == 0) w2l(w2_first(16) - W2_EPI + 7 * pr + t);
          if (ps == 1 && (t == 1 || t == 4)) ald(8 + 2 * pr + t / 3);
          SB();
        }
#pragma unroll
        for (int t = 0; t < 7; ++t) e[t] = e[t] * vv + vv;
        SB();
#pragma unroll
        for (int t = 0; t < 7; ++t) { e[t] = (f32x2){__builtin_amdgcn_rcpf(e[t][0]), __builtin_amdgcn_rcpf(e[t][1])}; a[t] *= bb[t]; SB(); }
#pragma unroll
        for (int t = 0; t < 7; ++t) a[t] *= e[t];
        SB();
#pragma unroll
        for (int t = 0; t < 7; ++t) d[t] = pk(a[t][0], a[t][1]);
        sa[pr] = (v4u){d[0], d[1], d[2], d[3]};
        const auto x0 = __builtin_amdgcn_permlane32_swap(d[4], d[6], false, false), x1 = __builtin_amdgcn_permlane32_swap(d[5], d[5], false, false);
        const auto y0 = __builtin_amdgcn_permlane16_swap(x0[0], x1[0], false, false), y1 = __builtin_amdgcn_permlane16_swap(x0[1], x1[1], false, false);
        sb[pr] = (v4u){y0[0], y0[1], y1[0], y1[1]};
        asm volatile("" : "+v"(sa[pr]), "+v"(sb[pr]));
        SB();
      }
#pragma unroll
      for (int pr = 0; pr < 2; ++pr) {
        const u32 ro = (u32)((pr ^ spar) * (224 * 256) + ps * (64 * 224 * 256));
        __builtin_amdgcn_raw_buffer_store_b128(sa[pr], WS, oact + ro, 0, SC1);
        __builtin_amdgcn_raw_buffer_store_b128(sb[pr], WS, oact2 + ro, 0, SC1);
      }
      SB();
    };
    hdma(0, 0); hdma(0, 1); hdma(1, 0); hdma(1, 1);
    {
      LAS char* c0 = wp0 + w * 1024;
      LAS char* c1 = wp0 + (w == 3 ? 3 : w + 4) * 1024;
      LAS char* c2 = c0 + 57344;
      LAS char* c3 = c1 + 57344;
      asm volatile("" : "+v"(c0), "+v"(c1), "+v"(c2), "+v"(c3));
      auto crd = [&](int k, int hi) { return *(LAS v4u*)((k < 8 ? (hi ? c1 : c0) : (hi ? c3 : c2)) + (k & 7) * 7168); };
      v4u fa[16], fb[16]; f32x4 ca, cb;
#pragma unroll
      for (int k = 0; k < 4; ++k) { fa[k] = crd(k, 0); fb[k] = crd(k, 1); }
      SB();
      sfor<0, 16>([&](auto K_) {
        constexpr int k = decltype(K_)::value;
        mfv<k == 0>(ca, fa[k], ones); SB();
        if constexpr (k + 4 < 16) fa[k + 4] = crd(k + 4, 0);
        SB();
        mfv<k == 0>(cb, fb[k], ones); SB();
        if constexpr (k + 4 < 16) fb[k + 4] = crd(k + 4, 1);
        if constexpr (k >= 2 && k < 8) hdma(2 + (k - 2) / 2, k & 1);
        if constexpr (k == 8) asm volatile("buffer_load_dword %0, %1, %2, 0 offen sc1" : "=&v"(fl2) : "v"(w < 2 ? 0xffffff00u : of2), "s"(rWS) : "memory");
        if constexpr (k == 9) put_h(1);
        if constexpr (k >= 9) wf[k - 9] = *(LAS v4u*)(wp + (k - 9) * 1024);
        SB();
      });
      WAITV(10);
#pragma unroll
      for (int pr = 0; pr < 2; ++pr) { const v2u a = hfrag(0, pr, 0), c = hfrag(0, pr, 1); hf[0][pr] = (v4u){a.x, a.y, c.x, c.y}; }
      asm volatile("s_nop 7" ::: "memory");
      *(LAS f32x4*)pcs = ca; *(LAS f32x4*)(pcs + 64) = cb;
    }
    SB();
    STAMP(20);
    sfor<0, 32>([&](auto G_) {
      constexpr int g = decltype(G_)::value, s = g & 15;
      v4u(&hc)[2] = hf[g & 1]; v4u(&hn)[2] = hf[(g + 1) & 1];
      LAS char* wn = wp;
      if constexpr ((g & 3) == 3 && g < 31) wn = wp0 + sg[((g + 1) >> 2) & 3] * 28672;
      v2u ha;
      sfor<0, 7>([&](auto T_) {
        constexpr int t = decltype(T_)::value, it = g * 7 + t, nx = it + WD, ng = nx / 7, nt = nx % 7;
        if constexpr (t == 0) asm volatile("" : "+v"(wf[it % 8]), "+v"(wf[(it + 1) % 8]), "+v"(wf[(it + 2) % 8]), "+v"(wf[(it + 3) % 8]), "+v"(hc[0]), "+v"(hc[1]));
        if constexpr (t == 4) asm volatile("" : "+v"(wf[it % 8]), "+v"(wf[(it + 1) % 8]), "+v"(wf[(it + 2) % 8]));
        mfv<s == 0>(acc[t][0], wf[it % (WD + 1)], hc[0]);
        SB();
        if constexpr (nx < 224) wf[nx % (WD + 1)] = *(LAS v4u*)((ng > g ? wn : wp) + (ng & 3) * 7168 + nt * 1024);
        SB();
        mfv<s == 0>(acc[t][1], wf[it % (WD + 1)], hc[1]);
        SB();
        if constexpr (g < 31 && g != 15 && t < 4) {
          if constexpr (t == 0) { constexpr int hw = h_wait(g); WAITV(hw); }
          if constexpr (t & 1) { const v2u hb = hfrag(g + 1, t >> 1, 1); hn[t >> 1] = (v4u){ha.x, ha.y, hb.x, hb.y}; } else ha = hfrag(g + 1, t >> 1, 0);
        }
        if constexpr (t >= 4 && t - 4 < w2_n(g)) w2l(w2_first(g) + t - 4);
        if constexpr (g == GC && t == 5) {
          constexpr int nw = vm_sum(GS + 1, GC) + w2_n(GC);
          asm volatile("s_waitcnt vmcnt(%1)" : "+v"(fl2) : "n"(nw) : "memory");
          if (!__all(fl2 == c2want)) spin(oc2, c2want);
          sfor<16, GC + 5>([&](auto K_) { hdma(decltype(K_)::value, 0); hdma(decltype(K_)::value, 1); });
        }
        if constexpr (d_n(g) > 0 && t >= 5) hdma(g + 5, t - 5);
        if constexpr (g == 28 && t == 4) {
          constexpr int nw = vm_sum(26, 28);
          asm volatile("s_waitcnt vmcnt(%1)" : "+v"(fl3) : "n"(nw) : "memory");
          if (!__all(fl3 == tag)) spin(of3, tag);
        }
        if constexpr (g >= 28 && (t == 4 || t == 6)) ald(2 * (g - 28) + (t - 4) / 2);
        if constexpr (g == 5 || g == 6) { if constexpr (t >= 3 && 4 * (g - 5) + t - 3 < 7) { constexpr int k = 4 * (g - 5) + t - 3; cs[k] = *(LAS f32x4*)(pcr + (k & 3) * 12288 + (k >> 2) * 64); } }
        SB();
      });
      if constexpr (s < 4) { mfv<s == 0>(sS, ones, hc[0]); mfv<s == 0>(sQ, hc[0], hc[0]); }
      if constexpr (g == 2) {
        constexpr int nw = 1 + vm_sum(0, 3);
        asm volatile("s_waitcnt vmcnt(%1)" : "+v"(fl2) : "n"(nw) : "memory");
        if (w >= 2 && !__all(fl2 == tag)) spin(of2, tag);
        __builtin_amdgcn_s_barrier();
        asm volatile("" ::: "memory");
      }
      if constexpr (g == 4) {
        constexpr int nw = vm_sum(0, 4) + w2_n(4) + 2;
        WAITV(nw);
        __builtin_amdgcn_s_barrier();
        asm volatile("" ::: "memory");
        if (w == 0) { if (l < 8) __hip_atomic_fetch_add(pc2, 1u, RLX); }
        else __builtin_amdgcn_raw_buffer_store_b32(vtag, WS, 0xffffff00u, 0, SC1);
      }
      if constexpr (s == 5) {
        float q01, q23, qq;
        asm volatile("v_cndmask_b32 %0, %3, %4, %7\n\tv_cndmask_b32 %1, %5, %6, %7\n\tv_cndmask_b32 %2, %0, %1, %8"
                     : "=&v"(q01), "=&v"(q23), "=&v"(qq) : "v"(sQ[0]), "v"(sQ[1]), "v"(sQ[2]), "v"(sQ[3]), "s"(0xaaaaaaaaaaaaaaaaull), "s"(0xccccccccccccccccull));
        __builtin_amdgcn_raw_buffer_store_b128((v4u){f2u(sS[0]), vtag, f2u(qq), vtag}, WS, ost + (g >> 4) * 16384, 0, SC1);
      }
      if constexpr (s == 12) {
        asm volatile("buffer_load_dwordx4 %0, %2, %3, %4 offen sc1\n\tbuffer_load_dwordx4 %1, %2, %3, %5 offen sc1" : "=&v"(g2[0]), "=&v"(g2[1])
                     : "v"(og2), "s"(rWS), "s"((g >> 4) * 16384), "s"(32768 + (g >> 4) * 16384) : "memory");
      }
      if constexpr (g == GS) asm volatile("buffer_load_dword %0, %1, %2, 0 offen sc1" : "=&v"(fl2) : "v"(oc2), "s"(rWS) : "memory");
      if constexpr (g == 19) {
        constexpr int nw = vm_sum(16, 19) + w2_n(19) + 2;
        WAITV(nw);
        __builtin_amdgcn_raw_buffer_store_b32(vtag, WS, of3w, 0, SC1);
      }
      if constexpr (g == 27) {
        dma<true>(lds0 + ring, 4096, l16, rWS, (32 * (w >> 1) + 4 * x + rbl) * 32768 + (2 * ct + (w & 1)) * 2048, O_HT);
        dma<true>(lds0 + ring, 5120, l16, rWS, (32 * (w >> 1) + 4 * x + rbl) * 32768 + (2 * ct + (w & 1)) * 2048, O_HT + 1024);
      }
      if constexpr (g == 25) asm volatile("buffer_load_dword %0, %1, %2, 0 offen sc1" : "=&v"(fl3) : "v"(of3), "s"(rWS) : "memory");
      SB();
      wp = wn;
      if constexpr ((g & 7) == 7) STAMP(16 + (g >> 3));
      if constexpr (s == 15) {
        constexpr int nw = (GS == 12) + (g == 15 ? vm_sum(13, 15) + w2_n(15) + 2 * d_n(15) : vm_sum(29, 32) - (GS == 12));
        asm volatile("s_waitcnt vmcnt(%2)\n\ts_nop 7" : "+v"(g2[0]), "+v"(g2[1]) : "n"(nw) : "memory");
        SB();
        epilogue(IC<(g >> 4)>{});
        if constexpr (g == 15) {
          constexpr int hw = h_wait(15);
          WAITV(hw);
#pragma unroll
          for (int pr = 0; pr < 2; ++pr) { const v2u a = hfrag(16, pr, 0), c = hfrag(16, pr, 1); hf[0][pr] = (v4u){a.x, a.y, c.x, c.y}; }
          SB();
        }
        if constexpr (g == 15) STAMP(9);
      }
    });
  }
  STAMP(10);

  {
    v2u hr[4];
    LAS char* pw = lds + w * 4096 + l * 16;
    u32 f3b;
    f32x4 o[4][2], mine[4];
    SB();
    sfor<0, 28>([&](auto U_) {
      constexpr int u = decltype(U_)::value;
#pragma unroll
      for (int c = 0; c < 2; ++c)
#pragma unroll
        for (int pp = 0; pp < 4; ++pp) mfa<u % 14 == 0>(o[pp][c], W2[4 * (u % 14) + pp], AC[u][c]);
      SB();
      if constexpr (u < 4) {
#pragma unroll
        for (int i = 32 + 3 * u; i < 35 + 3 * u; ++i) { w2l(i); SB(); }
      }
      if constexpr (u == UFL) {
        STAMP(26);
        WAITV(UFL < 4 ? 3 * UFL + 3 : 12);
        STAMP(27);
        __builtin_amdgcn_raw_buffer_store_b32(vtag, WS, of3w + 16384, 0, SC1);
        SB();
      }
      if constexpr (u == 5) {
#pragma unroll
        for (int i = 44; i < 48; ++i) { w2l(i); SB(); }
      }
      if constexpr (u == 6) {
        ald(12);
#pragma unroll
        for (int i = 48; i < 52; ++i) { w2l(i); SB(); }
      }
      if constexpr (u == 7) {
        __builtin_amdgcn_s_barrier();
        asm volatile("" ::: "memory");
#pragma unroll
        for (int i = 0; i < 4; ++i)
          hr[i] = trd(lds, L_R + (u32)(((w & 2) + (i >> 1)) * 12288 + 4096 + (8 * q + 4 * (i & 1) + rr) * 64 + (((2 * (w & 1) + (p4 >> 1)) ^ (2 * (q & 1))) << 4) + (p4 & 1) * 8));
        SB();
      }
      if constexpr (u == UPOLL) STAMP(28);
      if constexpr (u == UCHK) STAMP(22);
      if constexpr (u == UPOLL) {
        f3b = __builtin_amdgcn_raw_buffer_load_b32(WS, of3 + 16384, 0, SC1);
        SB();
        ald(13);
#pragma unroll
        for (int i = 52; i < 56; ++i) { w2l(i); SB(); }
      }
      if constexpr (u == UCHK) {
        if (!__all(f3b == tag)) spin(of3 + 16384, tag);
        STAMP(12);
#pragma unroll
        for (int v = 14; v < 20; ++v) ald(v);
      }
      if constexpr (u > UCHK && u <= UCHK + 8) ald(u - UCHK + 19);
      if constexpr (u % 14 == 13) {
        asm volatile("s_nop 7" ::: "memory");
        SB();
        constexpr int hfb = 2 * (u / 14);
#pragma unroll
        for (int pp = 0; pp < 4; ++pp) *(LAS f32x4*)(pw + ((hfb + ((w & 1) ^ 1)) * 16384) + pp * 1024) = o[pp][1];
        if ((w & 2) == hfb) {
#pragma unroll
          for (int pp = 0; pp < 4; ++pp) { mine[pp] = o[pp][0]; asm volatile("" : "+v"(mine[pp])); }
        } else {
#pragma unroll
          for (int pp = 0; pp < 4; ++pp) *(LAS f32x4*)(pw + ((hfb + (w & 1)) * 16384) + pp * 1024) = o[pp][0];
        }
        if constexpr (u == 13) STAMP(11);
        SB();
      }
    });
    STAMP(13);
    BAR();
#pragma unroll
    for (int j = 1; j < 4; ++j)
#pragma unroll
      for (int pp = 0; pp < 4; ++pp) mine[pp] += *(LAS f32x4*)(lds + w * 16384 + ((w + j) & 3) * 4096 + pp * 1024 + l * 16);
    u32 r[8];
#pragma unroll
    for (int i = 0; i < 4; ++i) {
      r[2 * i] = pk(lo2f(hr[i].x) + mine[0][i], hi2f(hr[i].x) + mine[1][i]);
      r[2 * i + 1] = pk(lo2f(hr[i].y) + mine[2][i], hi2f(hr[i].y) + mine[3][i]);
    }
    u16* op = A.out + (1024 * (w >> 1) + 128 * x + 32 * rbl + 16 * (w & 1) + n16) * D + 64 * ct + 8 * q;
    *(v4u*)op = (v4u){r[0], r[1], r[2], r[3]};
    *(v4u*)(op + 32) = (v4u){r[4], r[5], r[6], r[7]};
    if (b == 0 && w == 0 && l == 0) __builtin_amdgcn_raw_buffer_store_b32(tag, WS, O_EP, 0, SC1);
  }
}
}

extern "C" void kernel_launch(void* const* d_in, const int* in_sizes, int n_in, void* d_out, int out_size, void* d_ws, size_t ws_size, hipStream_t stream) {
  fk::Args a;
  a.q = (const fk::u16*)d_in[0]; a.k = (const fk::u16*)d_in[1]; a.v = (const fk::u16*)d_in[2]; a.shk = (const fk::u16*)d_in[3];
  a.ssk = (const fk::u16*)d_in[4]; a.ssd = (const fk::u16*)d_in[5]; a.wg = (const fk::u16*)d_in[7]; a.wu = (const fk::u16*)d_in[8];
  a.wd = (const fk::u16*)d_in[9]; a.out = (fk::u16*)d_out; a.ws = (char*)d_ws;
  fk::fused<<<256, 256, 0, stream>>>(a);
}
```

```cpp
#define OAUX 16
#include <hip/hip_runtime.h>
#include <stdint.h>
#define SIGA 112
#define W1P 5
#define W1A 6
#define N0 45
#define GC 11
#define GS 9
#define PFM 9
#define UFL 3
#define R2A 0
#define DM 0xffffffffu
#define UPOLL 9
#define UCHK 12

namespace fk {
constexpr int WD = 7;
typedef unsigned u32;
typedef unsigned short u16;
typedef unsigned long long u64;
typedef u32 v4u __attribute__((ext_vector_type(4)));
typedef u32 v2u __attribute__((ext_vector_type(2)));
typedef float f32x4 __attribute__((ext_vector_type(4)));
typedef float f32x2 __attribute__((ext_vector_type(2)));
typedef short s16x4 __attribute__((ext_vector_type(4)));
typedef short s16x8 __attribute__((ext_vector_type(8)));
typedef __bf16 bf2 __attribute__((ext_vector_type(2)));
#define LAS __attribute__((address_space(3)))
#define GAS __attribute__((address_space(1)))
#define RLX __ATOMIC_RELAXED, __HIP_MEMORY_SCOPE_AGENT
#define SB() __builtin_amdgcn_sched_barrier(0)
#define WAITV(n) asm volatile("s_waitcnt vmcnt(%0)" ::"n"(n) : "memory")
#define BAR() do { asm volatile("s_waitcnt lgkmcnt(0)" ::: "memory"); __builtin_amdgcn_s_barrier(); asm volatile("" ::: "memory"); } while (0)

constexpr int S = 2048, D = 512, F = 1792;
constexpr u32 O_EP = 0, O_C2 = 16384, O_F2 = 294912, O_ST1 = 65536, O_F1 = 98304, O_ST2 = 131072, O_F3 = 262144;
constexpr u32 O_KVT = 1u << 20, O_QT = 3u << 20, O_HT = 5u << 20, O_W2T = 7u << 20, O_ACT = 9u << 20, O_W1F = 17u << 20, WS_BYTES = 21u << 20;
constexpr u32 O_DIAG = 24u << 20;
constexpr u32 L_R = 114688, L_K = L_R, L_V = L_R + 9216, L_Q = L_R + 17408, L_WR = 0, WR_S = 1056, L_TZ = 36864, TZ_S = 4160, TZ_C = 4 * TZ_S, L_XR = L_R + 33280, XR_S = 4608,
              L_S1 = L_R, L_EX = L_R + 16384, LDS_BYTES = 163840;
constexpr u32 SPIN_CAP = 1u << 18;
constexpr int SC1 = 16;

struct Args { const u16 *q, *k, *v, *shk, *ssk, *ssd, *wg, *wu, *wd; u16* out; char* ws; };

__device__ __forceinline__ u32 f2u(float x) { return __builtin_bit_cast(u32, x); }
__device__ __forceinline__ float u2f(u32 x) { return __builtin_bit_cast(float, x); }
__device__ __forceinline__ float lo2f(u32 x) { return u2f(x << 16); }
__device__ __forceinline__ float hi2f(u32 x) { return u2f(x & 0xffff0000u); }
__device__ __forceinline__ u32 pk(float lo, float hi) { bf2 r = {(__bf16)lo, (__bf16)hi}; return __builtin_bit_cast(u32, r); }
__device__ __forceinline__ float rbf(float x) { return lo2f(pk(x, x)); }
__device__ __forceinline__ f32x4 mfma(v4u a, v4u b, f32x4 c) {
  return __builtin_amdgcn_mfma_f32_16x16x32_bf16(__builtin_bit_cast(s16x8, a), __builtin_bit_cast(s16x8, b), c, 0, 0, 0);
}
template <bool FIRST> __device__ __forceinline__ void mfv(f32x4& c, v4u a, v4u b) {
  if (FIRST) asm volatile("v_mfma_f32_16x16x32_bf16 %0, %1, %2, 0" : "=&v"(c) : "v"(a), "v"(b));
  else asm volatile("v_mfma_f32_16x16x32_bf16 %0, %1, %2, %0" : "+v"(c) : "v"(a), "v"(b));
}
template <bool FIRST> __device__ __forceinline__ void mfa(f32x4& c, v4u a, v4u b) {
  if (FIRST) asm volatile("v_mfma_f32_16x16x32_bf16 %0, %1, %2, 0" : "=&v"(c) : "a"(a), "v"(b));
  else asm volatile("v_mfma_f32_16x16x32_bf16 %0, %1, %2, %0" : "+v"(c) : "a"(a), "v"(b));
}
template <bool FIRST> __device__ __forceinline__ void mfb(f32x4& c, v4u a, v4u b) {
  if (FIRST) asm volatile("v_mfma_f32_16x16x32_bf16 %0, %1, %2, 0" : "=&v"(c) : "v"(a), "a"(b));
  else asm volatile("v_mfma_f32_16x16x32_bf16 %0, %1, %2, %0" : "+v"(c) : "v"(a), "a"(b));
}
__device__ __forceinline__ v2u trd(LAS char* lds, u32 off) {
  return __builtin_bit_cast(v2u, __builtin_amdgcn_ds_read_tr16_b64_v4i16((LAS s16x4*)(lds + off)));
}
__device__ __forceinline__ v4u mkrs(const void* p, u32 bytes) { u64 a = (u64)p; return (v4u){(u32)a, (u32)(a >> 32) & 0xffffu, bytes, 0x00020000u}; }
template <bool COH> __device__ __forceinline__ void dma(u32 lb, u32 lo, u32 voff, v4u rs, u32 sb, u32 so) {
  u32 t;
  if (COH) asm volatile("s_add_u32 m0, %1, %6\n\ts_add_u32 %0, %4, %5\n\tbuffer_load_dwordx4 %2, %3, %0 offen sc1 lds" : "=&s"(t) : "s"(lb), "v"(voff), "s"(rs), "s"(sb), "i"(so), "i"(lo) : "memory", "scc");
  else asm volatile("s_add_u32 m0, %1, %6\n\ts_add_u32 %0, %4, %5\n\tbuffer_load_dwordx4 %2, %3, %0 offen lds" : "=&s"(t) : "s"(lb), "v"(voff), "s"(rs), "s"(sb), "i"(so), "i"(lo) : "memory", "scc");
}
template <int CTRL, int RM> __device__ __forceinline__ float dppadd(float v) {
  return v + __builtin_bit_cast(float, __builtin_amdgcn_update_dpp(0, __builtin_bit_cast(int, v), CTRL, RM, 0xf, true));
}
__device__ __forceinline__ float hsum(float v) {
  v = dppadd<0x111, 0xf>(v); v = dppadd<0x112, 0xf>(v); v = dppadd<0x114, 0xf>(v); v = dppadd<0x118, 0xf>(v);
  return dppadd<0x142, 0xa>(v);
}
#define STAMP(i)
template <int V> struct IC { static constexpr int value = V; };
template <int B, int E, class Fn> __device__ __forceinline__ void sfor(Fn&& f) { if constexpr (B < E) { f(IC<B>{}); sfor<B + 1, E>(f); } }
constexpr int cv_a0(int u) { return u < 16 ? u >> 2 : ((u - 16) >> 2) > 4 ? (u - 16) >> 2 : 4; }
constexpr int cv_first(int u) { int n = 0; for (int j = 0; j < u; ++j) n += (j < 16 ? 4 : 8) - cv_a0(j); return n; }
constexpr int cv_u(int n) { int u = 0; while (cv_first(u + 1) <= n) ++u; return u; }
constexpr int cv_slot(int i) { int s_ = cv_first(i < 16 ? i : i + 16) - 12; if (i < 31) { const int v = cv_slot(i + 1) - 2; if (v < s_) s_ = v; } return s_ & ~1; }
constexpr int cv_at(int n) { for (int i = 0; i < 32; ++i) if (cv_slot(i) == n) return i; return -1; }
constexpr int w1_upto(int n) {
  int c = n < 5 ? 0 : (n - 5) / 3 + 1;
  if (c > W1A) c = W1A;
  if (n >= N0 + 22) c += (n - N0 - 22) / W1P + 1;
  return c < 28 ? c : 28;
}
constexpr int w2_n(int g) { return g < 27 && g % 3 != 2; }
constexpr int W2_EPI = 14, W2_EPB = 24;
constexpr int a_n(int g) { return g >= 28 ? 4 : 0; }
constexpr int w2_first(int g) { int n = g > 15 ? W2_EPI : 0; for (int j = 0; j < g; ++j) n += w2_n(j); return n; }
constexpr int vm_tail(int g) { return (g == GS) + ((g & 15) == 5) + 2 * ((g & 15) == 11) + (g == 15 ? W2_EPI + 4 : 0) + (g == 19) + (g == 25) + 2 * (g == 27); }
constexpr int d_n(int g) { return g >= 27 || (g >= 11 && g < GC) ? 0 : g == GC ? GC - 10 : 1; }
constexpr int vm_ops(int g) { return w2_n(g) + a_n(g) + 2 * d_n(g) + vm_tail(g); }
constexpr int vm_sum(int a, int e) { int n = 0; for (int j = a; j < e; ++j) n += vm_ops(j); return n; }
constexpr int h_wait(int g) {
  if (g < 4) return 2 * (3 - g) + 1 + vm_sum(0, g);
  if (g >= 15 && g - 4 <= GC) return 2 * (GC - (g - 4)) + vm_tail(GC) + vm_sum(GC + 1, g + (g == 15));
  return vm_tail(g - 4) + vm_sum(g - 3, g);
}
static_assert(w2_first(32) + W2_EPB == 56, "all w_down fragments are requested");

__global__ void __launch_bounds__(256, 1) fused(Args A) {
  __shared__ __attribute__((aligned(16))) char smem_[LDS_BYTES];
  LAS char* lds = (LAS char*)smem_;
  const u32 lds0 = (u32)(uintptr_t)lds;
  STAMP(0);
  const int b = blockIdx.x, x = b & 7, y = b >> 3;
  const int l = threadIdx.x & 63, w = __builtin_amdgcn_readfirstlane(threadIdx.x >> 6);
  const int n16 = l & 15, q = l >> 4, rr = (l >> 2) & 3, p4 = l & 3;
  u32 pfd;
  {
    u64 pc;
    asm volatile("s_getpc_b64 %0" : "=s"(pc));
    constexpr u32 CODE = 36864;
    const u32 co = (u32)((((y & (PFM == 9 ? 7 : 3)) * 4 + w) * PFM + (l < PFM ? l : 0)) * 128);
    asm volatile("global_load_dword %0, %1, %2" : "=&v"(pfd) : "v"(co < CODE ? co : 0u), "s"(pc & ~3ull) : "memory");
  }
  const __amdgpu_buffer_rsrc_t WS = __builtin_amdgcn_make_buffer_rsrc(A.ws, 0, WS_BYTES, 0x00020000);
  const v4u rWS = mkrs(A.ws, WS_BYTES);
  auto spin = [&](u32 off, u32 want) {
    for (u32 sp = 0; sp < SPIN_CAP; ++sp) {
      asm volatile("" ::: "memory");
      if (__builtin_amdgcn_raw_buffer_load_b32(WS, off, 0, SC1) == want) off = 0xffffff00u;
      if (__all(off == 0xffffff00u)) break;
    }
  };

  const int ti = 4 * x + (y >> 3), cj = y & 7;
  const int cA = 64 * cj + 16 * w + n16;
  const int srow = 256 * x + 8 * y + 2 * w;
  const int ch = w >> 1, par2 = w & 1, cB = 64 * x + 2 * y + ch;
  v2u shk; v4u sx0, sx1, kz[2];
  v2u kedge; u32 vtag, vdp;
  u32 l16 = l * 16;
  {
    const v4u rK = mkrs(A.k, S * D * 2), rV = mkrs(A.v, S * D * 2), rQ = mkrs(A.q, S * D * 2);
    const u32 c0 = ((l & 7) ^ ((l >> 3) & 2)) * 16, tv = (u32)((64 * ti + (l >> 3)) * 1024 + 128 * cj);
    const u32 va = tv + c0, vb = tv + (c0 ^ 64);
    const u32 vk = ((w & 1) ? vb : va) + (u32)(w * 8192 - 8192);
    dma<false>(lds0 + w * 1024, L_K, vk, rK, 0, 0);
    dma<false>(lds0 + w * 1024, L_K + 4096, vk + 32768, rK, 0, 0);
    if (w == 0) dma<false>(lds0, L_K + 8192, vk + 65536, rK, 0, 0);
    dma<false>(lds0 + w * 2048, L_V, va, rV, w * 16384, 0);
    dma<false>(lds0 + w * 2048, L_V + 1024, vb, rV, w * 16384, 8192);
    asm volatile("global_load_dwordx2 %0, %1, %2" : "=&v"(shk) : "v"(cA * 8), "s"(A.shk) : "memory");
    asm volatile("global_load_dwordx4 %0, %2, %3\n\tglobal_load_dwordx4 %1, %2, %3 offset:1024" : "=&v"(kz[0]), "=&v"(kz[1]) : "v"(l16), "s"(A.ssk + cB * S + 1024 * par2) : "memory");
    asm volatile("global_load_dwordx2 %0, %3, %4 offset:2048\n\tglobal_load_dword %1, %3, %5 sc1\n\tglobal_load_dword %2, %3, %6"
                 : "=&v"(kedge), "=&v"(vtag), "=&v"(vdp) : "v"(0), "s"(A.ssk + cB * S), "s"(A.ws + O_EP), "s"(A.ssd + 64 * x + 2 * y) : "memory");
    dma<false>(lds0 + w * 2048, L_Q, va, rQ, w * 16384, 0);
    dma<false>(lds0 + w * 2048, L_Q + 1024, vb, rQ, w * 16384, 8192);
    asm volatile("global_load_dwordx4 %0, %2, %3\n\tglobal_load_dwordx4 %1, %2, %3 offset:512" : "=&v"(sx0), "=&v"(sx1) : "v"((l >> 5) * 1024 + (l & 31) * 16), "s"(A.q + srow * D) : "memory");
  }
  const int rb = 8 * x + 2 * (y >> 3) + ((y >> 2) & 1), fg = y & 3;
  const v4u rW = mkrs(ch ? A.wu : A.wg, D * F * 2);
  u32 wrv = l < 56 ? l16 : 0xffffff00u, w1d[4];
  LAS char* pW = lds + L_WR + ((p4 >> 1) * 8 + rr) * WR_S + (4 * w + q) * 16 + (p4 & 1) * 8;
#pragma unroll
  for (int j = 0; j < 4; ++j) {
    int Z = 16 * j + 4 * w + q;
    asm volatile("" : "+v"(Z));
    const int c = (Z * 37) >> 8;
    w1d[j] = (j < 3 || Z < 56) ? O_W1F + (u32)(((8 * fg + c) * 16 + (rb >> 2)) * 7168 + (Z - 7 * c) * 1024 + (rb & 3) * 256 + (4 * ((n16 & 7) >> 1) + 2 * (n16 >> 3) + (n16 & 1)) * 16) : 0xffffff00u;
  }
  const u32 aAo = (u32)((8 * q + rr) * 128 + (((2 * w + (p4 >> 1)) ^ (rr & 2) ^ ((q & 1) << 2)) << 4) + (p4 & 1) * 8);
  LAS char* pA = lds + L_K + aAo;
  LAS char* pB = lds + L_K + (aAo ^ 64);
  u32 okv = O_KVT + cA * 4096 + (2040 - 32 - 64 * ti - 8 * q) * 2, oqt = O_QT + cA * 4096 + (64 * ti + 8 * q) * 2;
  LAS char* tw = lds + L_TZ + ch * TZ_C + (128 * par2 + l) * 16 + 32;
  u32 og = (l & 31) == 31 ? O_ST1 + (srow + (l >> 5)) * 8 : 0xffffff00u;
  u32 oci = (x == 0 && y < 8 && w == 0 && l < 8) ? O_C2 + l * 2048 + y * 256 : 0xffffff00u;
  asm volatile("" : "+v"(oci), "+v"(w1d[0]), "+v"(w1d[1]), "+v"(w1d[2]), "+v"(w1d[3]), "+v"(pA), "+v"(pB), "+v"(okv), "+v"(oqt), "+v"(tw), "+v"(og), "+v"(wrv), "+v"(pW));
  const int wU = b < 224 ? 2 * b + ch : 0;
  u32 ow[4], w2o = b < 224 ? O_W2T + (u32)(((wU & 7) * 56 + (wU >> 3)) * 4096 + par2 * 2048) + l16 : 0xffff0000u;
  {
    const int UU = 4 * (wU >> 3) + q, f = UU / 7, u = UU - 7 * f;
    const int col = 64 * (wU & 7) + 8 * rr + 4 * (p4 & 1) + 32 * (p4 >> 1) + 2 * par2;
#pragma unroll
    for (int d = 0; d < 4; ++d) ow[d] = (u32)(((56 * f + (u < 4 ? 2 * u + 8 * d : 8 * u + 2 * d)) * D + col) * 2);
  }
  asm volatile("" : "+v"(w2o), "+v"(ow[0]), "+v"(ow[1]), "+v"(ow[2]), "+v"(ow[3]));
  if (w == 0) *(LAS v4u*)(lds + L_XR + (l >> 5) * XR_S + 4096 + (l & 31) * 16) = (v4u){0, 0, 0, 0};
  SB();
  STAMP(23);
  asm volatile("s_waitcnt vmcnt(9)" : "+v"(shk), "+v"(pfd)::"memory");
  BAR();
  STAMP(1);
  u32 tag, dpair;
  {
    v2u ka[2], kb[2], kc[2], va[2], vb[2];
#pragma unroll
    for (int it = 0; it < 2; ++it) { ka[it] = trd(pA, it * 4096 + 512); kb[it] = trd(pB, it * 4096 + 1024); kc[it] = trd(pB, it * 4096 + 1536); }
#pragma unroll
    for (int it = 0; it < 2; ++it) { va[it] = trd(pA, L_V - L_K + it * 4096); vb[it] = trd(pA, L_V - L_K + it * 4096 + 512); }
    SB();
#pragma unroll
    for (int j = 0; j < 4; ++j) dma<false>(lds0 + (8 * ch + 4 * par2) * WR_S, L_WR + j * WR_S, wrv, rW, (8 * rb + 4 * par2) * (F * 2) + fg * 896, j * (F * 2));
    SB();
    auto LO = [](u32 a, u32 c) { return (f32x2){lo2f(a), lo2f(c)}; };
    auto HI = [](u32 a, u32 c) { return (f32x2){hi2f(a), hi2f(c)}; };
    const f32x2 w0 = LO(shk.x, shk.x), w1 = HI(shk.x, shk.x), w2 = LO(shk.y, shk.y), w3 = HI(shk.y, shk.y);
    const f32x2 e[12] = {w0, HI(ka[0].x, ka[1].x), LO(ka[0].y, ka[1].y), HI(ka[0].y, ka[1].y), LO(kb[0].x, kb[1].x), HI(kb[0].x, kb[1].x), LO(kb[0].y, kb[1].y),
                         HI(kb[0].y, kb[1].y), LO(kc[0].x, kc[1].x), HI(kc[0].x, kc[1].x), LO(kc[0].y, kc[1].y), HI(kc[0].y, kc[1].y)};
    const f32x2 vv[8] = {LO(va[0].x, va[1].x), HI(va[0].x, va[1].x), LO(va[0].y, va[1].y), HI(va[0].y, va[1].y), LO(vb[0].x, vb[1].x), HI(vb[0].x, vb[1].x), LO(vb[0].y, vb[1].y), HI(vb[0].y, vb[1].y)};
    f32x2 kv[8];
#pragma unroll
    for (int s = 0; s < 8; ++s) kv[s] = (w3 * e[s + 1] + w2 * e[s + 2] + w1 * e[s + 3] + w0 * e[s + 4]) * vv[s];
#pragma unroll
    for (int it = 0; it < 2; ++it)
      __builtin_amdgcn_raw_buffer_store_b128((v4u){pk(kv[7][it], kv[6][it]), pk(kv[5][it], kv[4][it]), pk(kv[3][it], kv[2][it]), pk(kv[1][it], kv[0][it])}, WS, okv + (1 - it) * 64, 0, SC1);
    SB();
    asm volatile("s_waitcnt vmcnt(8)" : "+v"(kz[0]), "+v"(kz[1]), "+v"(kedge), "+v"(vtag), "+v"(vdp)::"memory");
    BAR();
    vtag += 1u;
    tag = __builtin_amdgcn_readfirstlane(vtag); dpair = __builtin_amdgcn_readfirstlane(vdp);
    v2u qa[2], qb[2];
#pragma unroll
    for (int it = 0; it < 2; ++it) { qa[it] = trd(pA, L_Q - L_K + it * 4096); qb[it] = trd(pA, L_Q - L_K + it * 4096 + 512); }
#pragma unroll
    for (int it = 0; it < 2; ++it) __builtin_amdgcn_raw_buffer_store_b128((v4u){qa[it].x, qa[it].y, qb[it].x, qb[it].y}, WS, oqt + it * 64, 0, SC1);
    __builtin_amdgcn_raw_buffer_store_b32(tag * 64u, WS, oci, 0, SC1);
  }
  SB();
  STAMP(24);
  {
    auto put = [&](LAS char* p, v4u d, u32 d4, u32 d5) {
      const u32 e0 = __builtin_amdgcn_alignbit(d.y, d.x, 16), e1 = __builtin_amdgcn_alignbit(d.z, d.y, 16), e2 = __builtin_amdgcn_alignbit(d.w, d.z, 16),
                e3 = __builtin_amdgcn_alignbit(d4, d.w, 16), e4 = __builtin_amdgcn_alignbit(d5, d4, 16);
      *(LAS v4u*)p = d; *(LAS v4u*)(p + TZ_S) = (v4u){e0, e1, e2, e3}; *(LAS v4u*)(p + 2 * TZ_S) = (v4u){d.y, d.z, d.w, d4}; *(LAS v4u*)(p + 3 * TZ_S) = (v4u){e1, e2, e3, e4};
    };
    auto nxt = [](u32 last, u32 v) { return (u32)__builtin_amdgcn_update_dpp((int)last, (int)v, 0x130, 0xf, 0xf, false); };
    put(tw, kz[0], nxt(__builtin_amdgcn_readlane(kz[1].x, 0), kz[0].x), nxt(__builtin_amdgcn_readlane(kz[1].y, 0), kz[0].y));
    put(tw + 1024, kz[1], nxt(par2 ? 0u : kedge.x, kz[1].x), nxt(par2 ? 0u : kedge.y, kz[1].y));
    const u32 f0 = __builtin_amdgcn_readlane(kz[0].x, 0), f1 = __builtin_amdgcn_readlane(kz[0].y, 0);
    if (l < 2 && !par2) put(tw - 32, (v4u){0, 0, 0, 0}, l ? f0 : 0u, l ? f1 : 0u);
  }
  SB();
  v2u st1;
  asm volatile("s_waitcnt vmcnt(9)" : "+v"(sx0), "+v"(sx1)::"memory");
  {
    const f32x2 v[8] = {{lo2f(sx0.x), hi2f(sx0.x)}, {lo2f(sx0.y), hi2f(sx0.y)}, {lo2f(sx0.z), hi2f(sx0.z)}, {lo2f(sx0.w), hi2f(sx0.w)},
                        {lo2f(sx1.x), hi2f(sx1.x)}, {lo2f(sx1.y), hi2f(sx1.y)}, {lo2f(sx1.z), hi2f(sx1.z)}, {lo2f(sx1.w), hi2f(sx1.w)}};
    const f32x2 s2 = ((v[0] + v[1]) + (v[2] + v[3])) + ((v[4] + v[5]) + (v[6] + v[7]));
    const float sm = hsum(s2[0] + s2[1]);
    const float m0 = u2f(__builtin_amdgcn_readlane(f2u(sm), 31)), m1 = u2f(__builtin_amdgcn_readlane(f2u(sm), 63));
    const float mean = (l < 32 ? m0 : m1) * (1.f / D);
    f32x2 ss = {0.f, 0.f};
#pragma unroll
    for (int j = 0; j < 8; ++j) { const f32x2 d = v[j] - mean; ss += d * d; }
    const float var = hsum(ss[0] + ss[1]) * (1.f / D);
    st1 = (v2u){pk(mean, __builtin_amdgcn_sqrtf(rbf(rbf(var) + 1e-5f))), vtag};
    __builtin_amdgcn_raw_buffer_store_b64(st1, WS, og, 0, SC1);
  }
  SB();
  WAITV(6);
  BAR();
  {
    v2u ta[4], tc[4];
#pragma unroll
    for (int j = 0; j < 4; ++j) { ta[j] = trd(pW, j * 256); tc[j] = trd(pW, j * 256 + 4 * WR_S); }
#pragma unroll
    for (int j = 0; j < 4; ++j) __builtin_amdgcn_raw_buffer_store_b128((v4u){ta[j].x, ta[j].y, tc[j].x, tc[j].y}, WS, w1d[j], 0, SC1);
  }
  SB();
  WAITV(0);
  BAR();
  STAMP(2);
  if (w == 0 && l == 0) __builtin_amdgcn_raw_buffer_store_b32(tag, WS, O_F1 + b * 128, 0, SC1);
  u32 w2raw[8];
  asm volatile("global_load_dword %0, %8, %12\n\tglobal_load_dword %1, %8, %12 offset:1024\n\tglobal_load_dword %2, %9, %12\n\tglobal_load_dword %3, %9, %12 offset:1024\n\t"
               "global_load_dword %4, %10, %12\n\tglobal_load_dword %5, %10, %12 offset:1024\n\tglobal_load_dword %6, %11, %12\n\tglobal_load_dword %7, %11, %12 offset:1024"
               : "=&v"(w2raw[0]), "=&v"(w2raw[1]), "=&v"(w2raw[2]), "=&v"(w2raw[3]), "=&v"(w2raw[4]), "=&v"(w2raw[5]), "=&v"(w2raw[6]), "=&v"(w2raw[7])
               : "v"(ow[0]), "v"(ow[1]), "v"(ow[2]), "v"(ow[3]), "s"(A.wd) : "memory");
  v4u T[32];
  {
    const u32 tb = lds0 + L_TZ + ch * TZ_C + ((n16 + 1) & 3) * TZ_S + (u32)((((n16 + 1) >> 2) + 2 * q + 8 * par2) * 8);
#pragma unroll
    for (int i = 0; i < 32; ++i) {
      v2u a, c;
      asm volatile("ds_read_b64 %0, %2 offset:%3\n\tds_read_b64 %1, %2 offset:%4" : "=&v"(a), "=&v"(c) : "v"(tb), "i"(128 * i), "i"(128 * i + 8) : "memory");
      T[i] = (v4u){a.x, a.y, c.x, c.y};
    }
    asm volatile("s_waitcnt lgkmcnt(0)" : "+v"(T[0]), "+v"(T[1]), "+v"(T[2]), "+v"(T[3]), "+v"(T[4]), "+v"(T[5]), "+v"(T[6]), "+v"(T[7]), "+v"(T[8]), "+v"(T[9]), "+v"(T[10]),
                 "+v"(T[11]), "+v"(T[12]), "+v"(T[13]), "+v"(T[14]), "+v"(T[15])::"memory");
    asm volatile("" : "+v"(T[16]), "+v"(T[17]), "+v"(T[18]), "+v"(T[19]), "+v"(T[20]), "+v"(T[21]), "+v"(T[22]), "+v"(T[23]), "+v"(T[24]), "+v"(T[25]), "+v"(T[26]),
                 "+v"(T[27]), "+v"(T[28]), "+v"(T[29]), "+v"(T[30]), "+v"(T[31])::"memory");
  }
  const int tB = 512 * par2 + 16 * n16 + 4 * q;
  LAS char* xp = lds + L_XR + ch * XR_S + (2032 - 16 * n16 + 32 * par2 + 8 * q) * 2 - 3584;
  LAS char* kp = lds + L_XR + ch * XR_S + (2044 - 1280 - tB) * 2;
  LAS char* gp = lds + L_S1 + tB * 8;
  LAS char* ep = lds + L_EX + w * 4096 + l * 16;
  LAS char* er = lds + L_EX + (w ^ 1) * 4096 + l * 16;
  u32 qo = O_QT + cB * 4096 + tB * 2;
  u32 oh = O_HT + (u32)((8 * (2 * par2 + (q & 1)) + (n16 >> 1)) * 32768 + (cB >> 5) * 2048 + (cB & 31) * 64 + (((2 * (n16 & 1) + (q >> 1)) ^ (2 * ((cB >> 3) & 1))) << 4));
  asm volatile("" : "+v"(xp), "+v"(kp), "+v"(gp), "+v"(ep), "+v"(er), "+v"(qo), "+v"(oh), "+v"(l16));
  const int hp = 4 * (x >> 2) + (y >> 3), fch = 8 * (x & 3) + (y & 7);
  const int ct = y & 7, rbl = y >> 3;
  const u32 ring = L_R + w * 12288;
  const int rot = fch & 3, spar = (fch >> 2) & 1;
  u32 w2v = O_W2T + (u32)((ct * 56 + 14 * w) * 4096) + l16;
  u32 ost = (fch < 8 && rr == q) ? O_ST2 + (u32)(rot * 32768 + (128 * hp + 32 * w + 16 * spar + n16) * 16) : 0xffff0000u;
  u32 og2 = O_ST2 + (u32)((l >> 5) * 65536 + (128 * hp + 32 * w + (l & 31)) * 16);
  u32 oact = O_ACT + (u32)(((8 * hp + 2 * w) * 224 + 7 * fch + q) * 256 + n16 * 16), oact2 = q < 3 ? oact + 1024 : 0xff000000u;
  u32 of3 = O_F3 + (u32)(((x * 4 + rbl) * 4 + w) * 128 + (l & 7) * 4), of3w = l == 0 ? O_F3 + (u32)(((hp * 4 + w) * 4 + (fch >> 3)) * 128 + (fch & 7) * 4) : 0xffff0000u;
  u32 av = O_ACT + (u32)(((8 * x + 2 * rbl) * 224 + 56 * w) * 256) + l16;
  LAS char* hx[2];
#pragma unroll
  for (int pr = 0; pr < 2; ++pr) hx[pr] = lds + ring + (8 * q + rr) * 64 + (((2 * (pr ^ spar) + (p4 >> 1)) ^ (2 * (q & 1))) << 4) + (p4 & 1) * 8;
  LAS char* pcs = lds + ring + 10240 + (n16 == 0 ? q * 16 : 128 + l * 16);
  LAS char* pcr = lds + L_R + 10240 + q * 16;
  asm volatile("" : "+v"(av), "+v"(pcs), "+v"(pcr), "+v"(w2v), "+v"(ost), "+v"(og2), "+v"(oact), "+v"(oact2), "+v"(of3), "+v"(of3w), "+v"(hx[0]), "+v"(hx[1]));
  u32 of1 = O_F1 + (u32)((l & 7) + 8 * ((x & 3) + 4 * (l >> 3))) * 128, of2 = O_F2 + (u32)(64 * w + l) * 128, of2w = (w == 0 && l == 0) ? O_F2 + (u32)b * 128 : 0xffff0000u;
  asm volatile("" : "+v"(of1), "+v"(of2), "+v"(of2w));
  STAMP(21);
  if (w == 0) spin(of1, tag);
  BAR();
  STAMP(3);

  dma<true>(lds0 + ch * XR_S + par2 * 2048, L_XR, l16, rWS, cB * 4096 + par2 * 2048, O_KVT);
  dma<true>(lds0 + ch * XR_S + par2 * 2048, L_XR + 1024, l16, rWS, cB * 4096 + par2 * 2048, O_KVT + 1024);
#pragma unroll
  for (int i = 0; i < 4; ++i) dma<true>(lds0 + w * 4096, L_S1 + i * 1024, l16, rWS, w * 4096, O_ST1 + i * 1024);
  v2u xq[4];
  asm volatile("buffer_load_dwordx2 %0, %4, %5, 0 offen sc1\n\tbuffer_load_dwordx2 %1, %4, %5, 0 offen offset:512 sc1\n\t"
               "buffer_load_dwordx2 %2, %4, %5, 0 offen offset:2048 sc1\n\tbuffer_load_dwordx2 %3, %4, %5, 0 offen offset:2560 sc1"
               : "=&v"(xq[0]), "=&v"(xq[1]), "=&v"(xq[2]), "=&v"(xq[3]) : "v"(qo), "s"(rWS) : "memory");
  SB();
  asm volatile("s_waitcnt vmcnt(8)" : "+v"(w2raw[0]), "+v"(w2raw[1]), "+v"(w2raw[2]), "+v"(w2raw[3]), "+v"(w2raw[4]), "+v"(w2raw[5]), "+v"(w2raw[6]), "+v"(w2raw[7])::"memory");
  BAR();
  STAMP(4);
  GAS u32* pc2 = (GAS u32*)(A.ws + O_C2 + (l & 7) * 2048 + x * 256);
  const u32 oc2 = O_C2 + x * 2048 + (l & 7) * 256, c2want = tag * 64u + 32u;
  v4u W2[56];
  const int r2 = R2A * rbl;
  auto kpos = [&](int k) { return (u32)(k + r2 >= 14 ? k + r2 - 14 : k + r2); };
  auto w2l = [&](int i) { W2[i] = __builtin_amdgcn_raw_buffer_load_b128(WS, w2v + (i & 3) * 1024, kpos(i >> 2) * 4096, SC1); };
  u32 hpk[4];
  auto put_h = [&](int h) {
    const auto s0 = __builtin_amdgcn_permlane16_swap(hpk[0], hpk[2], false, false);
    const auto s1 = __builtin_amdgcn_permlane16_swap(hpk[1], hpk[3], false, false);
    __builtin_amdgcn_raw_buffer_store_b128((v4u){s0[0], s1[0], s0[1], s1[1]}, WS, oh + h * (32 * 32768), 0, SC1);
  };
  {
    u32 w1so;
    auto w1l = [&](int i) {
      if ((i & 3) == 0) asm volatile("s_add_u32 m0, %1, %2\n\ts_add_u32 %0, %3, %4\n\tbuffer_load_dwordx4 %5, %6, %0 offen sc1 lds"
                                     : "=&s"(w1so) : "s"(lds0 + w * 28672), "i"(i * 1024), "s"(fch * 114688 + w * 28672), "i"(O_W1F + i * 1024), "v"(l16), "s"(rWS) : "memory", "scc");
      else asm volatile("buffer_load_dwordx4 %0, %1, %2 offen offset:%3 sc1 lds" ::"v"(l16), "s"(rWS), "s"(w1so), "i"((i & 3) * 1024) : "memory");
    };
    const float dsc = ch ? hi2f(dpair) : lo2f(dpair);
    f32x4 cacc[8], mn[2], ot[2];
    v4u B[32], g1[8], w2f[2]; v2u kr[4];
    float xf[16], ln[16], kd[16], tm[16], hh[8];
    auto fix = [&](int h) {
      u32 df = 0;
#pragma unroll
      for (int i = 4 * h; i < 4 * h + 4; ++i) df |= (g1[i].y ^ vtag) | (g1[i].w ^ vtag);
      if (__builtin_amdgcn_ballot_w64(df != 0)) {
        bool ok;
        for (u32 sp = 0; sp < SPIN_CAP; ++sp) {
          asm volatile("" ::: "memory");
          ok = true;
#pragma unroll
          for (int i = 4 * h; i < 4 * h + 4; ++i) {
            g1[i] = __builtin_amdgcn_raw_buffer_load_b128(WS, O_ST1 + (tB + 256 * ((i >> 1) & 1) + 1024 * h) * 8 + (i & 1) * 16, 0, SC1);
            ok &= (g1[i].y == tag) & (g1[i].w == tag);
          }
          if (__all(ok)) break;
        }
#pragma unroll
        for (int j = 8 * h; j < 8 * h + 8; ++j) { const u32 pay = g1[j >> 1][(j & 1) * 2]; ln[j] = (xf[j] - lo2f(pay)) * __builtin_amdgcn_rcpf(hi2f(pay)); }
      }
    };
    auto give = [&](int h) {
      if (par2) { *(LAS f32x4*)(ep + 2048 * h) = cacc[4 * h]; *(LAS f32x4*)(ep + 2048 * h + 1024) = cacc[4 * h + 1]; mn[0] = cacc[4 * h + 2]; mn[1] = cacc[4 * h + 3]; asm volatile("" : "+v"(mn[0]), "+v"(mn[1])); }
      else { *(LAS f32x4*)(ep + 2048 * h) = cacc[4 * h + 2]; *(LAS f32x4*)(ep + 2048 * h + 1024) = cacc[4 * h + 3]; mn[0] = cacc[4 * h]; mn[1] = cacc[4 * h + 1]; asm volatile("" : "+v"(mn[0]), "+v"(mn[1])); }
    };
    auto pre = [&](auto J_, auto P_) {
      constexpr int j = decltype(J_)::value, ph = decltype(P_)::value;
      const u32 pay = g1[j >> 1][(j & 1) * 2];
      if constexpr (ph == 0) { xf[j] = (j & 1) ? hi2f(xq[j >> 2][(j & 3) >> 1]) : lo2f(xq[j >> 2][(j & 3) >> 1]); asm volatile("" : "+v"(xf[j])); }
      if constexpr (ph == 1) { tm[j] = __builtin_amdgcn_rcpf(hi2f(pay)); asm volatile("" : "+v"(tm[j])); }
      if constexpr (ph == 2) { ln[j] = xf[j] - lo2f(pay); asm volatile("" : "+v"(ln[j])); }
      if constexpr (ph == 3) { ln[j] *= tm[j]; asm volatile("" : "+v"(ln[j])); }
      if constexpr (ph == 4) { const u32 kk = kr[j >> 2][1 - ((j & 3) >> 1)]; kd[j] = (j & 1) ? lo2f(kk) : hi2f(kk); asm volatile("" : "+v"(kd[j])); }
    };
    sfor<0, 32>([&](auto U_) { constexpr int u = decltype(U_)::value; if constexpr (cv_slot(u) < 0) B[u] = *(LAS v4u*)(xp + 128 * (31 - u)); });
    SB();
    sfor<0, 144>([&](auto N_) {
      constexpr int n = decltype(N_)::value, u = cv_u(n), i = u < 16 ? u : u - 16, a = cv_a0(u) + n - cv_first(u), eps = 4 * a - i + 3;
      mfb<i == 0>(cacc[a], T[eps], B[i]);
      SB();
      constexpr int ub = cv_at(n);
      if constexpr (ub >= 0) B[ub] = *(LAS v4u*)(xp + 128 * (31 - ub));
      if constexpr (w1_upto(n) > w1_upto(n - 1)) w1l(w1_upto(n) - 1);
      if constexpr (n < 4) {
        constexpr int pp = n / 2, d = 2 * (n & 1);
        w2f[pp][d] = __builtin_amdgcn_perm(w2raw[2 * d + 1], w2raw[2 * d], pp ? 0x07060302u : 0x05040100u);
        w2f[pp][d + 1] = __builtin_amdgcn_perm(w2raw[2 * d + 3], w2raw[2 * d + 2], pp ? 0x07060302u : 0x05040100u);
        if constexpr (n & 1) __builtin_amdgcn_raw_buffer_store_b128(w2f[pp], WS, w2o + pp * 1024, 0, SC1); else asm volatile("" : "+v"(w2f[pp]));
      }
      if constexpr (n == 4 || n == 6) kr[(n - 4) / 2] = *(LAS v2u*)(kp + 2560 - 512 * ((n - 4) / 2));
      if constexpr (n >= 23 && n < 39 && n % 2 == 1) pre(IC<(n - 23) / 2>{}, IC<4>{});
      if constexpr (n == 44) give(0);
      if constexpr (n == N0) {
        constexpr int nw = 2 + w1_upto(N0);
        asm volatile("s_waitcnt vmcnt(%4) lgkmcnt(0)" : "+v"(xq[0]), "+v"(xq[1]), "+v"(xq[2]), "+v"(xq[3]) : "n"(nw) : "memory");
        __builtin_amdgcn_s_barrier();
        asm volatile("" ::: "memory");
      }
      if constexpr (n == N0 + 1 || n == N0 + 2) { constexpr int k = n - N0 - 1; g1[2 * k] = *(LAS v4u*)(gp + k * 2048); g1[2 * k + 1] = *(LAS v4u*)(gp + k * 2048 + 16); ot[k] = *(LAS f32x4*)(er + k * 1024); }
      if constexpr (n == N0 + 3 || n == N0 + 4) { constexpr int k = 4 * (n - N0 - 3); pre(IC<k>{}, IC<0>{}); pre(IC<k + 1>{}, IC<0>{}); pre(IC<k + 2>{}, IC<0>{}); pre(IC<k + 3>{}, IC<0>{}); }
      if constexpr (n >= N0 + 5 && n < N0 + 13) {
        constexpr int j = n - N0 - 5;
        pre(IC<j>{}, IC<1>{}); pre(IC<j>{}, IC<2>{});
        if constexpr (j > 0) pre(IC<j - 1>{}, IC<3>{});
      }
      if constexpr (n == N0 + 13) { pre(IC<7>{}, IC<3>{}); fix(0); }
      if constexpr (n >= N0 + 14 && n < N0 + 18) {
#pragma unroll
        for (int j = 2 * (n - N0 - 14); j < 2 * (n - N0 - 14) + 2; ++j) { hh[j] = xf[j] + ln[j] * ((kd[j] * dsc + mn[j >> 2][j & 3]) + ot[j >> 2][j & 3]); asm volatile("" : "+v"(hh[j])); }
      }
      if constexpr (n == N0 + 18 || n == N0 + 19) { constexpr int k = n - N0 - 18; hpk[2 * k] = pk(hh[4 * k], hh[4 * k + 1]); hpk[2 * k + 1] = pk(hh[4 * k + 2], hh[4 * k + 3]); asm volatile("" : "+v"(hpk[2 * k]), "+v"(hpk[2 * k + 1])); }
      if constexpr (n == N0 + 20) put_h(0);
      if constexpr (n == 70 || n == 74) kr[2 + (n - 70) / 4] = *(LAS v2u*)(kp + 512 - 512 * ((n - 70) / 4));
      if constexpr (n >= 78 && n < 94 && n % 4 == 2) { constexpr int j = 4 + (n - 78) / 4; g1[j] = *(LAS v4u*)(gp + 8192 + ((j >> 1) & 1) * 2048 + (j & 1) * 16); }
      if constexpr (n == SIGA) {
        constexpr int nw = w1_upto(SIGA) - w1_upto(N0 + 20);
        WAITV(nw);
        __builtin_amdgcn_s_barrier();
        __builtin_amdgcn_raw_buffer_store_b32(vtag, WS, of2w, 0, SC1);
      }
      if constexpr (n >= 96 && n < 136) pre(IC<8 + (n - 96) / 5>{}, IC<(n - 96) % 5>{});
      SB();
    });
    asm volatile("s_nop 7" ::: "memory");
    SB();
    STAMP(5);
    sfor<w1_upto(143), 28>([&](auto I_) { w1l(decltype(I_)::value); });
    give(1);
    fix(1);
    BAR();
#pragma unroll
    for (int aa = 0; aa < 2; ++aa) {
      const f32x4 o = *(LAS f32x4*)(er + 2048 + aa * 1024);
#pragma unroll
      for (int i = 0; i < 4; ++i) hh[4 * aa + i] = xf[8 + 4 * aa + i] + ln[8 + 4 * aa + i] * ((kd[8 + 4 * aa + i] * dsc + mn[aa][i]) + o[i]);
      hpk[2 * aa] = pk(hh[4 * aa], hh[4 * aa + 1]); hpk[2 * aa + 1] = pk(hh[4 * aa + 2], hh[4 * aa + 3]);
    }
    put_h(1);
    SB();
    STAMP(6);
  }

  v4u ones = {0x3f803f80u, 0x3f803f80u, 0x3f803f80u, 0x3f803f80u};
  LAS char* wp0 = lds + l * 16;
  asm volatile("" : "+v"(ones), "+v"(wp0));
  u32 sg[4];
#pragma unroll
  for (int g = 0; g < 4; ++g) sg[g] = (u32)((g + rot) & 3);
  LAS char* wp = wp0 + sg[0] * 28672;
  v4u wf[WD + 1], hf[2][2], g2[2];
  WAITV(1);
  BAR();
  STAMP(7);
  u32 fl2;
  asm volatile("buffer_load_dword %0, %1, %2, 0 offen sc1" : "=&v"(fl2) : "v"(of2), "s"(rWS) : "memory");
  {
    LAS char* c0 = wp0 + w * 1024;
    LAS char* c1 = wp0 + (w == 3 ? 3 : w + 4) * 1024;
    LAS char* c2 = c0 + 57344;
    LAS char* c3 = c1 + 57344;
    asm volatile("" : "+v"(c0), "+v"(c1), "+v"(c2), "+v"(c3));
    auto crd = [&](int k, int hi) { return *(LAS v4u*)((k < 8 ? (hi ? c1 : c0) : (hi ? c3 : c2)) + (k & 7) * 7168); };
    v4u fa[16], fb[16]; f32x4 ca, cb;
#pragma unroll
    for (int k = 0; k < 4; ++k) { fa[k] = crd(k, 0); fb[k] = crd(k, 1); }
    SB();
    sfor<0, 16>([&](auto K_) {
      constexpr int k = decltype(K_)::value;
      mfv<k == 0>(ca, fa[k], ones); SB();
      if constexpr (k + 4 < 16) fa[k + 4] = crd(k + 4, 0);
      SB();
      mfv<k == 0>(cb, fb[k], ones); SB();
      if constexpr (k + 4 < 16) fb[k + 4] = crd(k + 4, 1);
      if constexpr (k >= 9) wf[k - 9] = *(LAS v4u*)(wp + (k - 9) * 1024);
      SB();
    });
    asm volatile("s_nop 7" ::: "memory");
    *(LAS f32x4*)pcs = ca; *(LAS f32x4*)(pcs + 64) = cb;
  }
  asm volatile("s_waitcnt vmcnt(0)" : "+v"(fl2)::"memory");
  if (!__all(fl2 == tag)) spin(of2, tag);
  BAR();
  STAMP(8);
  f32x4 cs[7];
  u32 fl3 = 0;
  v4u AC[28][2];
  const u32 ao[2] = {(u32)((w & 1) * (224 * 256)), (u32)(((w & 1) ^ 1) * (224 * 256))};
  auto ald = [&](int u) {
#pragma unroll
    for (int c = 0; c < 2; ++c) { AC[u][c] = __builtin_amdgcn_raw_buffer_load_b128(WS, R2A ? av : av + (u % 14 & 3) * 1024, ao[c] + (R2A ? kpos(u % 14) * 1024 : (u % 14 >> 2) * 4096) + (u / 14) * (64 * 224 * 256), SC1); SB(); }
  };
  {
    u32 hso;
    auto hdma = [&](int g, int n) {
      if (n == 0) asm volatile("s_add_u32 m0, %1, %2\n\ts_add_u32 %0, %3, %4\n\tbuffer_load_dwordx4 %5, %6, %0 offen sc1 lds"
                               : "=&s"(hso) : "s"(lds0 + ring), "i"((g % 5) * 2048), "s"((4 * hp + w) * 32768 + sg[(g >> 2) & 3] * 8192), "i"(O_HT + (g >> 4) * (32 * 32768) + (g & 3) * 2048), "v"(l16), "s"(rWS) : "memory", "scc");
      else asm volatile("buffer_load_dwordx4 %0, %1, %2 offen offset:1024 sc1 lds" ::"v"(l16), "s"(rWS), "s"(hso) : "memory");
    };
    auto hfrag = [&](int g, int pr, int t) { return trd(hx[pr], (g % 5) * 2048 + t * 256); };
    f32x4 acc[7][2], sS, sQ;
    float z0, z1, z2, sv[3][2];
    auto stat = [&](auto T_) {
      constexpr int t = decltype(T_)::value;
      auto both = [&](float v, float(&o)[2]) {
        const auto r = __builtin_amdgcn_permlane16_swap(f2u(v), f2u(v), false, false);
        o[0] = u2f(spar ? r[1] : r[0]); o[1] = u2f(spar ? r[0] : r[1]);
        asm volatile("" : "+v"(o[0]), "+v"(o[1]));
      };
      if constexpr (t == 0) { z0 = u2f(g2[0].x) + u2f(g2[1].x); asm volatile("" : "+v"(z0)); z1 = u2f(g2[0].z) + u2f(g2[1].z); asm volatile("" : "+v"(z1)); }
      if constexpr (t == 1) { const auto h = __builtin_amdgcn_permlane32_swap(f2u(z0), f2u(z0), false, false); z0 = (u2f(h[0]) + u2f(h[1])) * (-1.f / D); asm volatile("" : "+v"(z0)); }
      if constexpr (t == 2) { const auto h = __builtin_amdgcn_permlane32_swap(f2u(z1), f2u(z1), false, false); z1 = (u2f(h[0]) + u2f(h[1])) * (1.f / D); asm volatile("" : "+v"(z1)); }
      if constexpr (t == 3) { z1 = __builtin_fmaxf(z1 - z0 * z0, 0.f) + 1e-5f; z2 = -1.44269504089f * __builtin_amdgcn_rsqf(z1); asm volatile("" : "+v"(z1), "+v"(z2)); }
      if constexpr (t == 4) both(z0, sv[0]);
      if constexpr (t == 5) both(z2, sv[1]);
      if constexpr (t == 6) both(z1, sv[2]);
    };
    auto epilogue = [&](auto P_) {
      constexpr int ps = decltype(P_)::value;
      v4u sa[2], sb[2];
#pragma unroll
      for (int pr = 0; pr < 2; ++pr) {
        const float nm = sv[0][pr], k1 = sv[1][pr], vv = sv[2][pr];
        u32 d[8];
        f32x2 a[7], bb[7], e[7];
#pragma unroll
        for (int t = 0; t < 7; ++t) a[t] = (f32x2){acc[t][pr][0], acc[t][pr][1]} + nm * (f32x2){cs[t][0], cs[t][1]};
        SB();
#pragma unroll
        for (int t = 0; t < 7; ++t) e[t] = a[t] * k1;
        SB();
#pragma unroll
        for (int t = 0; t < 7; ++t) {
          e[t] = (f32x2){__builtin_amdgcn_exp2f(e[t][0]), __builtin_amdgcn_exp2f(e[t][1])};
          bb[t] = (f32x2){acc[t][pr][2], acc[t][pr][3]} + nm * (f32x2){cs[t][2], cs[t][3]};
          if (ps == 0) w2l(w2_first(16) - W2_EPI + 7 * pr + t);
          if (ps == 1 && (t == 1 || t == 4)) ald(8 + 2 * pr + t / 3);
          SB();
        }
#pragma unroll
        for (int t = 0; t < 7; ++t) e[t] = e[t] * vv + vv;
        SB();
#pragma unroll
        for (int t = 0; t < 7; ++t) { e[t] = (f32x2){__builtin_amdgcn_rcpf(e[t][0]), __builtin_amdgcn_rcpf(e[t][1])}; a[t] *= bb[t]; SB(); }
#pragma unroll
        for (int t = 0; t < 7; ++t) a[t] *= e[t];
        SB();
#pragma unroll
        for (int t = 0; t < 7; ++t) d[t] = pk(a[t][0], a[t][1]);
        sa[pr] = (v4u){d[0], d[1], d[2], d[3]};
        const auto x0 = __builtin_amdgcn_permlane32_swap(d[4], d[6], false, false), x1 = __builtin_amdgcn_permlane32_swap(d[5], d[5], false, false);
        const auto y0 = __builtin_amdgcn_permlane16_swap(x0[0], x1[0], false, false), y1 = __builtin_amdgcn_permlane16_swap(x0[1], x1[1], false, false);
        sb[pr] = (v4u){y0[0], y0[1], y1[0], y1[1]};
        asm volatile("" : "+v"(sa[pr]), "+v"(sb[pr]));
        SB();
      }
#pragma unroll
      for (int pr = 0; pr < 2; ++pr) {
        const u32 ro = (u32)((pr ^ spar) * (224 * 256) + ps * (64 * 224 * 256));
        __builtin_amdgcn_raw_buffer_store_b128(sa[pr], WS, oact + ro, 0, SC1);
        __builtin_amdgcn_raw_buffer_store_b128(sb[pr], WS, oact2 + ro, 0, SC1);
      }
      SB();
    };
    hdma(0, 0); hdma(0, 1); hdma(1, 0); hdma(1, 1);
#pragma unroll
    for (int g = 2; g < 5; ++g) { hdma(g, 0); hdma(g, 1); }
    if (w == 0) { if (l < 8) __hip_atomic_fetch_add(pc2, 1u, RLX); }
    else __builtin_amdgcn_raw_buffer_store_b32(vtag, WS, 0xffffff00u, 0, SC1);
#pragma unroll
    for (int k = 0; k < 7; ++k) cs[k] = *(LAS f32x4*)(pcr + (k & 3) * 12288 + (k >> 2) * 64);
    WAITV(9);
#pragma unroll
    for (int pr = 0; pr < 2; ++pr) { const v2u a = hfrag(0, pr, 0), c = hfrag(0, pr, 1); hf[0][pr] = (v4u){a.x, a.y, c.x, c.y}; }
    SB();
    STAMP(20);
    sfor<0, 32>([&](auto G_) {
      constexpr int g = decltype(G_)::value, s = g & 15;
      v4u(&hc)[2] = hf[g & 1]; v4u(&hn)[2] = hf[(g + 1) & 1];
      LAS char* wn = wp;
      if constexpr ((g & 3) == 3 && g < 31) wn = wp0 + sg[((g + 1) >> 2) & 3] * 28672;
      v2u ha;
      sfor<0, 7>([&](auto T_) {
        constexpr int t = decltype(T_)::value, it = g * 7 + t, nx = it + WD, ng = nx / 7, nt = nx % 7;
        if constexpr (t == 0) asm volatile("" : "+v"(wf[it % 8]), "+v"(wf[(it + 1) % 8]), "+v"(wf[(it + 2) % 8]), "+v"(wf[(it + 3) % 8]), "+v"(hc[0]), "+v"(hc[1]));
        if constexpr (t == 4) asm volatile("" : "+v"(wf[it % 8]), "+v"(wf[(it + 1) % 8]), "+v"(wf[(it + 2) % 8]));
        mfv<s == 0>(acc[t][0], wf[it % (WD + 1)], hc[0]);
        SB();
        if constexpr (nx < 224) wf[nx % (WD + 1)] = *(LAS v4u*)((ng > g ? wn : wp) + (ng & 3) * 7168 + nt * 1024);
        SB();
        mfv<s == 0>(acc[t][1], wf[it % (WD + 1)], hc[1]);
        SB();
        if constexpr (g < 31 && g != 15 && t < 4) {
          if constexpr (t == 0) { constexpr int hw = h_wait(g); WAITV(hw); }
          if constexpr (t & 1) { const v2u hb = hfrag(g + 1, t >> 1, 1); hn[t >> 1] = (v4u){ha.x, ha.y, hb.x, hb.y}; } else ha = hfrag(g + 1, t >> 1, 0);
        }
        if constexpr (t >= 4 && t - 4 < w2_n(g)) w2l(w2_first(g) + t - 4);
        if constexpr (g == GC && t == 5) {
          constexpr int nw = vm_sum(GS + 1, GC) + w2_n(GC);
          asm volatile("s_waitcnt vmcnt(%1)" : "+v"(fl2) : "n"(nw) : "memory");
          if (!__all(fl2 == c2want)) spin(oc2, c2want);
          sfor<16, GC + 5>([&](auto K_) { hdma(decltype(K_)::value, 0); hdma(decltype(K_)::value, 1); });
        }
        if constexpr (d_n(g) > 0 && t >= 5) hdma(g + 5, t - 5);
        if constexpr (g == 28 && t == 4) {
          constexpr int nw = vm_sum(26, 28);
          asm volatile("s_waitcnt vmcnt(%1)" : "+v"(fl3) : "n"(nw) : "memory");
          if (!__all(fl3 == tag)) spin(of3, tag);
        }
        if constexpr (g >= 28 && (t == 4 || t == 6)) ald(2 * (g - 28) + (t - 4) / 2);
        if constexpr (s == 15) stat(T_);
        SB();
      });
      if constexpr (s < 4) { mfv<s == 0>(sS, ones, hc[0]); mfv<s == 0>(sQ, hc[0], hc[0]); }
      if constexpr (s == 5) {
        float q01, q23, qq;
        asm volatile("v_cndmask_b32 %0, %3, %4, %7\n\tv_cndmask_b32 %1, %5, %6, %7\n\tv_cndmask_b32 %2, %0, %1, %8"
                     : "=&v"(q01), "=&v"(q23), "=&v"(qq) : "v"(sQ[0]), "v"(sQ[1]), "v"(sQ[2]), "v"(sQ[3]), "s"(0xaaaaaaaaaaaaaaaaull), "s"(0xccccccccccccccccull));
        __builtin_amdgcn_raw_buffer_store_b128((v4u){f2u(sS[0]), vtag, f2u(qq), vtag}, WS, ost + (g >> 4) * 16384, 0, SC1);
      }
      if constexpr (s == 11) {
        asm volatile("buffer_load_dwordx4 %0, %2, %3, %4 offen sc1\n\tbuffer_load_dwordx4 %1, %2, %3, %5 offen sc1" : "=&v"(g2[0]), "=&v"(g2[1])
                     : "v"(og2), "s"(rWS), "s"((g >> 4) * 16384), "s"(32768 + (g >> 4) * 16384) : "memory");
      }
      if constexpr (g == GS) asm volatile("buffer_load_dword %0, %1, %2, 0 offen sc1" : "=&v"(fl2) : "v"(oc2), "s"(rWS) : "memory");
      if constexpr (g == 19) {
        constexpr int nw = vm_sum(16, 19) + w2_n(19) + 2;
        WAITV(nw);
        __builtin_amdgcn_raw_buffer_store_b32(vtag, WS, of3w, 0, SC1);
      }
      if constexpr (g == 27) {
        dma<true>(lds0 + ring, 4096, l16, rWS, (32 * (w >> 1) + 4 * x + rbl) * 32768 + (2 * ct + (w & 1)) * 2048, O_HT);
        dma<true>(lds0 + ring, 5120, l16, rWS, (32 * (w >> 1) + 4 * x + rbl) * 32768 + (2 * ct + (w & 1)) * 2048, O_HT + 1024);
      }
      if constexpr (g == 25) asm volatile("buffer_load_dword %0, %1, %2, 0 offen sc1" : "=&v"(fl3) : "v"(of3), "s"(rWS) : "memory");
      if constexpr (s == 14) {
        constexpr int ps = g >> 4, nw = vm_sum(g - 2, g + 1);
        asm volatile("s_waitcnt vmcnt(%2)" : "+v"(g2[0]), "+v"(g2[1]) : "n"(nw) : "memory");
        if (!__all((g2[0].y == tag) & (g2[0].w == tag) & (g2[1].y == tag) & (g2[1].w == tag))) {
          for (u32 sp = 0; sp < SPIN_CAP; ++sp) {
            asm volatile("" ::: "memory");
#pragma unroll
            for (int k = 0; k < 2; ++k) g2[k] = __builtin_amdgcn_raw_buffer_load_b128(WS, og2, k * 32768 + ps * 16384, SC1);
            if (__all((g2[0].y == tag) & (g2[0].w == tag) & (g2[1].y == tag) & (g2[1].w == tag))) break;
          }
        }
      }
      SB();
      wp = wn;
      if constexpr ((g & 7) == 7) STAMP(16 + (g >> 3));
      if constexpr (s == 15) {
        asm volatile("s_nop 7" ::: "memory");
        SB();
        epilogue(IC<(g >> 4)>{});
        if constexpr (g == 15) {
          constexpr int hw = h_wait(15);
          WAITV(hw);
#pragma unroll
          for (int pr = 0; pr < 2; ++pr) { const v2u a = hfrag(16, pr, 0), c = hfrag(16, pr, 1); hf[0][pr] = (v4u){a.x, a.y, c.x, c.y}; }
          SB();
        }
        if constexpr (g == 15) STAMP(9);
      }
    });
  }
  STAMP(10);

  {
    v2u hr[4];
    LAS char* pw = lds + w * 4096 + l * 16;
    u32 f3b;
    f32x4 o[4][2], mine[4];
    SB();
    sfor<0, 28>([&](auto U_) {
      constexpr int u = decltype(U_)::value;
#pragma unroll
      for (int c = 0; c < 2; ++c)
#pragma unroll
        for (int pp = 0; pp < 4; ++pp) mfa<u % 14 == 0>(o[pp][c], W2[4 * (u % 14) + pp], AC[u][c]);
      SB();
      if constexpr (u < 4) {
#pragma unroll
        for (int i = 32 + 3 * u; i < 35 + 3 * u; ++i) { w2l(i); SB(); }
      }
      if constexpr (u == UFL) {
        STAMP(26);
        WAITV(UFL < 4 ? 3 * UFL + 3 : 12);
        STAMP(27);
        __builtin_amdgcn_raw_buffer_store_b32(vtag, WS, of3w + 16384, 0, SC1);
        SB();
      }
      if constexpr (u == 5) {
#pragma unroll
        for (int i = 44; i < 48; ++i) { w2l(i); SB(); }
      }
      if constexpr (u == 6) {
        ald(12);
#pragma unroll
        for (int i = 48; i < 52; ++i) { w2l(i); SB(); }
      }
      if constexpr (u == 7) {
        __builtin_amdgcn_s_barrier();
        asm volatile("" ::: "memory");
#pragma unroll
        for (int i = 0; i < 4; ++i)
          hr[i] = trd(lds, L_R + (u32)(((w & 2) + (i >> 1)) * 12288 + 4096 + (8 * q + 4 * (i & 1) + rr) * 64 + (((2 * (w & 1) + (p4 >> 1)) ^ (2 * (q & 1))) << 4) + (p4 & 1) * 8));
        SB();
      }
      if constexpr (u == UPOLL) STAMP(28);
      if constexpr (u == UCHK) STAMP(22);
      if constexpr (u == UPOLL) {
        f3b = __builtin_amdgcn_raw_buffer_load_b32(WS, of3 + 16384, 0, SC1);
        SB();
        ald(13);
#pragma unroll
        for (int i = 52; i < 56; ++i) { w2l(i); SB(); }
      }
      if constexpr (u == UCHK) {
        if (!__all(f3b == tag)) spin(of3 + 16384, tag);
        STAMP(12);
#pragma unroll
        for (int v = 14; v < 20; ++v) ald(v);
      }
      if constexpr (u > UCHK && u <= UCHK + 8) ald(u - UCHK + 19);
      if constexpr (u % 14 == 13) {
        asm volatile("s_nop 7" ::: "memory");
        SB();
        constexpr int hfb = 2 * (u / 14);
#pragma unroll
        for (int pp = 0; pp < 4; ++pp) *(LAS f32x4*)(pw + ((hfb + ((w & 1) ^ 1)) * 16384) + pp * 1024) = o[pp][1];
        if ((w & 2) == hfb) {
#pragma unroll
          for (int pp = 0; pp < 4; ++pp) { mine[pp] = o[pp][0]; asm volatile("" : "+v"(mine[pp])); }
        } else {
#pragma unroll
          for (int pp = 0; pp < 4; ++pp) *(LAS f32x4*)(pw + ((hfb + (w & 1)) * 16384) + pp * 1024) = o[pp][0];
        }
        if constexpr (u == 13) STAMP(11);
        SB();
      }
    });
    STAMP(13);
    BAR();
#pragma unroll
    for (int j = 1; j < 4; ++j)
#pragma unroll
      for (int pp = 0; pp < 4; ++pp) mine[pp] += *(LAS f32x4*)(lds + w * 16384 + ((w + j) & 3) * 4096 + pp * 1024 + l * 16);
    u32 r[8];
#pragma unroll
    for (int i = 0; i < 4; ++i) {
      r[2 * i] = pk(lo2f(hr[i].x) + mine[0][i], hi2f(hr[i].x) + mine[1][i]);
      r[2 * i + 1] = pk(lo2f(hr[i].y) + mine[2][i], hi2f(hr[i].y) + mine[3][i]);
    }
    const __amdgpu_buffer_rsrc_t OUT = __builtin_amdgcn_make_buffer_rsrc(A.out, 0, S * D * 2, 0x00020000);
    const u32 oo = (u32)(((1024 * (w >> 1) + 128 * x + 32 * rbl + 16 * (w & 1) + n16) * D + 64 * ct + 8 * q) * 2);
    __builtin_amdgcn_raw_buffer_store_b128((v4u){r[0], r[1], r[2], r[3]}, OUT, oo, 0, OAUX);
    __builtin_amdgcn_raw_buffer_store_b128((v4u){r[4], r[5], r[6], r[7]}, OUT, oo + 64, 0, OAUX);
    if (b == 0 && w == 0 && l == 0) __builtin_amdgcn_raw_buffer_store_b32(tag, WS, O_EP, 0, SC1);
  }
}
}

extern "C" void kernel_launch(void* const* d_in, const int* in_sizes, int n_in, void* d_out, int out_size, void* d_ws, size_t ws_size, hipStream_t stream) {
  fk::Args a;
  a.q = (const fk::u16*)d_in[0]; a.k = (const fk::u16*)d_in[1]; a.v = (const fk::u16*)d_in[2]; a.shk = (const fk::u16*)d_in[3];
  a.ssk = (const fk::u16*)d_in[4]; a.ssd = (const fk::u16*)d_in[5]; a.wg = (const fk::u16*)d_in[7]; a.wu = (const fk::u16*)d_in[8];
  a.wd = (const fk::u16*)d_in[9]; a.out = (fk::u16*)d_out; a.ws = (char*)d_ws;
  fk::fused<<<256, 256, 0, stream>>>(a);
}
```

```cpp
#include <hip/hip_runtime.h>
#include <stdint.h>
#define SIGA 112
#define W1P 5
#define W1A 6
#define N0 45
#define GC 11
#define GS 9
#define PFH 0
#define UFL 3
#define R2A 0
#define DM 0xffffffffu
#define OAUX 16
#define UPOLL 9
#define UCHK 12

namespace fk {
constexpr int WD = 7;
typedef unsigned u32;
typedef unsigned short u16;
typedef unsigned long long u64;
typedef u32 v4u __attribute__((ext_vector_type(4)));
typedef u32 v2u __attribute__((ext_vector_type(2)));
typedef float f32x4 __attribute__((ext_vector_type(4)));
typedef float f32x2 __attribute__((ext_vector_type(2)));
typedef short s16x4 __attribute__((ext_vector_type(4)));
typedef short s16x8 __attribute__((ext_vector_type(8)));
typedef __bf16 bf2 __attribute__((ext_vector_type(2)));
#define LAS __attribute__((address_space(3)))
#define GAS __attribute__((address_space(1)))
#define RLX __ATOMIC_RELAXED, __HIP_MEMORY_SCOPE_AGENT
#define SB() __builtin_amdgcn_sched_barrier(0)
#define WAITV(n) asm volatile("s_waitcnt vmcnt(%0)" ::"n"(n) : "memory")
#define BAR() do { asm volatile("s_waitcnt lgkmcnt(0)" ::: "memory"); __builtin_amdgcn_s_barrier(); asm volatile("" ::: "memory"); } while (0)

constexpr int S = 2048, D = 512, F = 1792;
constexpr u32 O_EP = 0, O_C2 = 16384, O_F2 = 294912, O_ST1 = 65536, O_F1 = 98304, O_ST2 = 131072, O_F3 = 262144;
constexpr u32 O_KVT = 1u << 20, O_QT = 3u << 20, O_HT = 5u << 20, O_W2T = 7u << 20, O_ACT = 9u << 20, O_W1F = 17u << 20, WS_BYTES = 21u << 20;
constexpr u32 O_DIAG = 24u << 20;
constexpr u32 L_R = 114688, L_K = L_R, L_V = L_R + 9216, L_Q = L_R + 17408, L_WR = 0, WR_S = 1056, L_TZ = 36864, TZ_S = 4160, TZ_C = 4 * TZ_S, L_XR = L_R + 33280, XR_S = 4608,
              L_S1 = L_R, L_EX = L_R + 16384, LDS_BYTES = 163840;
constexpr u32 SPIN_CAP = 1u << 18;
constexpr int SC1 = 16;

struct Args { const u16 *q, *k, *v, *shk, *ssk, *ssd, *wg, *wu, *wd; u16* out; char* ws; };

__device__ __forceinline__ u32 f2u(float x) { return __builtin_bit_cast(u32, x); }
__device__ __forceinline__ float u2f(u32 x) { return __builtin_bit_cast(float, x); }
__device__ __forceinline__ float lo2f(u32 x) { return u2f(x << 16); }
__device__ __forceinline__ float hi2f(u32 x) { return u2f(x & 0xffff0000u); }
__device__ __forceinline__ u32 pk(float lo, float hi) { bf2 r = {(__bf16)lo, (__bf16)hi}; return __builtin_bit_cast(u32, r); }
__device__ __forceinline__ float rbf(float x) { return lo2f(pk(x, x)); }
__device__ __forceinline__ f32x4 mfma(v4u a, v4u b, f32x4 c) {
  return __builtin_amdgcn_mfma_f32_16x16x32_bf16(__builtin_bit_cast(s16x8, a), __builtin_bit_cast(s16x8, b), c, 0, 0, 0);
}
template <bool FIRST> __device__ __forceinline__ void mfv(f32x4& c, v4u a, v4u b) {
  if (FIRST) asm volatile("v_mfma_f32_16x16x32_bf16 %0, %1, %2, 0" : "=&v"(c) : "v"(a), "v"(b));
  else asm volatile("v_mfma_f32_16x16x32_bf16 %0, %1, %2, %0" : "+v"(c) : "v"(a), "v"(b));
}
template <bool FIRST> __device__ __forceinline__ void mfa(f32x4& c, v4u a, v4u b) {
  if (FIRST) asm volatile("v_mfma_f32_16x16x32_bf16 %0, %1, %2, 0" : "=&v"(c) : "a"(a), "v"(b));
  else asm volatile("v_mfma_f32_16x16x32_bf16 %0, %1, %2, %0" : "+v"(c) : "a"(a), "v"(b));
}
template <bool FIRST> __device__ __forceinline__ void mfb(f32x4& c, v4u a, v4u b) {
  if (FIRST) asm volatile("v_mfma_f32_16x16x32_bf16 %0, %1, %2, 0" : "=&v"(c) : "v"(a), "a"(b));
  else asm volatile("v_mfma_f32_16x16x32_bf16 %0, %1, %2, %0" : "+v"(c) : "v"(a), "a"(b));
}
__device__ __forceinline__ v2u trd(LAS char* lds, u32 off) {
  return __builtin_bit_cast(v2u, __builtin_amdgcn_ds_read_tr16_b64_v4i16((LAS s16x4*)(lds + off)));
}
__device__ __forceinline__ v4u mkrs(const void* p, u32 bytes) { u64 a = (u64)p; return (v4u){(u32)a, (u32)(a >> 32) & 0xffffu, bytes, 0x00020000u}; }
template <bool COH> __device__ __forceinline__ void dma(u32 lb, u32 lo, u32 voff, v4u rs, u32 sb, u32 so) {
  u32 t;
  if (COH) asm volatile("s_add_u32 m0, %1, %6\n\ts_add_u32 %0, %4, %5\n\tbuffer_load_dwordx4 %2, %3, %0 offen sc1 lds" : "=&s"(t) : "s"(lb), "v"(voff), "s"(rs), "s"(sb), "i"(so), "i"(lo) : "memory", "scc");
  else asm volatile("s_add_u32 m0, %1, %6\n\ts_add_u32 %0, %4, %5\n\tbuffer_load_dwordx4 %2, %3, %0 offen lds" : "=&s"(t) : "s"(lb), "v"(voff), "s"(rs), "s"(sb), "i"(so), "i"(lo) : "memory", "scc");
}
template <int CTRL, int RM> __device__ __forceinline__ float dppadd(float v) {
  return v + __builtin_bit_cast(float, __builtin_amdgcn_update_dpp(0, __builtin_bit_cast(int, v), CTRL, RM, 0xf, true));
}
__device__ __forceinline__ float hsum(float v) {
  v = dppadd<0x111, 0xf>(v); v = dppadd<0x112, 0xf>(v); v = dppadd<0x114, 0xf>(v); v = dppadd<0x118, 0xf>(v);
  return dppadd<0x142, 0xa>(v);
}
#define STAMP(i)
template <int V> struct IC { static constexpr int value = V; };
template <int B, int E, class Fn> __device__ __forceinline__ void sfor(Fn&& f) { if constexpr (B < E) { f(IC<B>{}); sfor<B + 1, E>(f); } }
constexpr int cv_a0(int u) { return u < 16 ? u >> 2 : ((u - 16) >> 2) > 4 ? (u - 16) >> 2 : 4; }
constexpr int cv_first(int u) { int n = 0; for (int j = 0; j < u; ++j) n += (j < 16 ? 4 : 8) - cv_a0(j); return n; }
constexpr int cv_u(int n) { int u = 0; while (cv_first(u + 1) <= n) ++u; return u; }
constexpr int cv_slot(int i) { int s_ = cv_first(i < 16 ? i : i + 16) - 12; if (i < 31) { const int v = cv_slot(i + 1) - 2; if (v < s_) s_ = v; } return s_ & ~1; }
constexpr int cv_at(int n) { for (int i = 0; i < 32; ++i) if (cv_slot(i) == n) return i; return -1; }
constexpr int w1_upto(int n) {
  int c = n < 5 ? 0 : (n - 5) / 3 + 1;
  if (c > W1A) c = W1A;
  if (n >= N0 + 22) c += (n - N0 - 22) / W1P + 1;
  return c < 28 ? c : 28;
}
constexpr int w2_n(int g) { return g < 27 && g % 3 != 2; }
constexpr int W2_EPI = 14, W2_EPB = 24;
constexpr int a_n(int g) { return g >= 28 ? 4 : 0; }
constexpr int w2_first(int g) { int n = g > 15 ? W2_EPI : 0; for (int j = 0; j < g; ++j) n += w2_n(j); return n; }
constexpr int vm_tail(int g) { return (g == GS) + ((g & 15) == 5) + 2 * ((g & 15) == 11) + (g == 15 ? W2_EPI + 4 : 0) + (g == 19) + (g == 25) + 2 * (g == 27); }
constexpr int d_n(int g) { return g >= 27 || (g >= 11 && g < GC) ? 0 : g == GC ? GC - 10 : 1; }
constexpr int vm_ops(int g) { return w2_n(g) + a_n(g) + 2 * d_n(g) + vm_tail(g); }
constexpr int vm_sum(int a, int e) { int n = 0; for (int j = a; j < e; ++j) n += vm_ops(j); return n; }
constexpr int h_wait(int g) {
  if (g < 4) return 2 * (3 - g) + 1 + vm_sum(0, g);
  if (g >= 15 && g - 4 <= GC) return 2 * (GC - (g - 4)) + vm_tail(GC) + vm_sum(GC + 1, g + (g == 15));
  return vm_tail(g - 4) + vm_sum(g - 3, g);
}
static_assert(w2_first(32) + W2_EPB == 56, "all w_down fragments are requested");

__global__ void __launch_bounds__(256, 1) fused(Args A) {
  __shared__ __attribute__((aligned(16))) char smem_[LDS_BYTES];
  LAS char* lds = (LAS char*)smem_;
  const u32 lds0 = (u32)(uintptr_t)lds;
  STAMP(0);
  const int b = blockIdx.x, x = b & 7, y = b >> 3;
  const int l = threadIdx.x & 63, w = __builtin_amdgcn_readfirstlane(threadIdx.x >> 6);
  const int n16 = l & 15, q = l >> 4, rr = (l >> 2) & 3, p4 = l & 3;
  u32 pfd;
  {
    u64 pc;
    asm volatile("s_getpc_b64 %0" : "=s"(pc));
    constexpr u32 CODE = 36864;
    const int tid = threadIdx.x;
    const u32 co = l < 9 ? (u32)((((y & 7) * 4 + (tid >> 6)) * 9 + l) * 128) : l < 9 + PFH ? (u32)(((tid >> 6) * PFH + l - 8) * 128) : 0u;
    asm volatile("global_load_dword %0, %1, %2" : "=&v"(pfd) : "v"(co), "s"(pc & ~3ull) : "memory");
  }
  const __amdgpu_buffer_rsrc_t WS = __builtin_amdgcn_make_buffer_rsrc(A.ws, 0, WS_BYTES, 0x00020000);
  const v4u rWS = mkrs(A.ws, WS_BYTES);
  auto spin = [&](u32 off, u32 want) {
    for (u32 sp = 0; sp < SPIN_CAP; ++sp) {
      asm volatile("" ::: "memory");
      if (__builtin_amdgcn_raw_buffer_load_b32(WS, off, 0, SC1) == want) off = 0xffffff00u;
      if (__all(off == 0xffffff00u)) break;
    }
  };

  const int ti = 4 * x + (y >> 3), cj = y & 7;
  const int cA = 64 * cj + 16 * w + n16;
  const int srow = 256 * x + 8 * y + 2 * w;
  const int ch = w >> 1, par2 = w & 1, cB = 64 * x + 2 * y + ch;
  v2u shk; v4u sx0, sx1, kz[2];
  v2u kedge; u32 vtag, vdp;
  u32 l16 = l * 16;
  {
    const v4u rK = mkrs(A.k, S * D * 2), rV = mkrs(A.v, S * D * 2), rQ = mkrs(A.q, S * D * 2);
    const u32 c0 = ((l & 7) ^ ((l >> 3) & 2)) * 16, tv = (u32)((64 * ti + (l >> 3)) * 1024 + 128 * cj);
    const u32 va = tv + c0, vb = tv + (c0 ^ 64);
    const u32 vk = ((w & 1) ? vb : va) + (u32)(w * 8192 - 8192);
    dma<false>(lds0 + w * 1024, L_K, vk, rK, 0, 0);
    dma<false>(lds0 + w * 1024, L_K + 4096, vk + 32768, rK, 0, 0);
    if (w == 0) dma<false>(lds0, L_K + 8192, vk + 65536, rK, 0, 0);
    dma<false>(lds0 + w * 2048, L_V, va, rV, w * 16384, 0);
    dma<false>(lds0 + w * 2048, L_V + 1024, vb, rV, w * 16384, 8192);
    asm volatile("global_load_dwordx2 %0, %1, %2" : "=&v"(shk) : "v"(cA * 8), "s"(A.shk) : "memory");
    asm volatile("global_load_dwordx4 %0, %2, %3\n\tglobal_load_dwordx4 %1, %2, %3 offset:1024" : "=&v"(kz[0]), "=&v"(kz[1]) : "v"(l16), "s"(A.ssk + cB * S + 1024 * par2) : "memory");
    asm volatile("global_load_dwordx2 %0, %3, %4 offset:2048\n\tglobal_load_dword %1, %3, %5 sc1\n\tglobal_load_dword %2, %3, %6"
                 : "=&v"(kedge), "=&v"(vtag), "=&v"(vdp) : "v"(0), "s"(A.ssk + cB * S), "s"(A.ws + O_EP), "s"(A.ssd + 64 * x + 2 * y) : "memory");
    dma<false>(lds0 + w * 2048, L_Q, va, rQ, w * 16384, 0);
    dma<false>(lds0 + w * 2048, L_Q + 1024, vb, rQ, w * 16384, 8192);
    asm volatile("global_load_dwordx4 %0, %2, %3\n\tglobal_load_dwordx4 %1, %2, %3 offset:512" : "=&v"(sx0), "=&v"(sx1) : "v"((l >> 5) * 1024 + (l & 31) * 16), "s"(A.q + srow * D) : "memory");
  }
  const int rb = 8 * x + 2 * (y >> 3) + ((y >> 2) & 1), fg = y & 3;
  const v4u rW = mkrs(ch ? A.wu : A.wg, D * F * 2);
  u32 wrv = l < 56 ? l16 : 0xffffff00u, w1d[4];
  LAS char* pW = lds + L_WR + ((p4 >> 1) * 8 + rr) * WR_S + (4 * w + q) * 16 + (p4 & 1) * 8;
#pragma unroll
  for (int j = 0; j < 4; ++j) {
    int Z = 16 * j + 4 * w + q;
    asm volatile("" : "+v"(Z));
    const int c = (Z * 37) >> 8;
    w1d[j] = (j < 3 || Z < 56) ? O_W1F + (u32)(((8 * fg + c) * 16 + (rb >> 2)) * 7168 + (Z - 7 * c) * 1024 + (rb & 3) * 256 + (4 * ((n16 & 7) >> 1) + 2 * (n16 >> 3) + (n16 & 1)) * 16) : 0xffffff00u;
  }
  const u32 aAo = (u32)((8 * q + rr) * 128 + (((2 * w + (p4 >> 1)) ^ (rr & 2) ^ ((q & 1) << 2)) << 4) + (p4 & 1) * 8);
  LAS char* pA = lds + L_K + aAo;
  LAS char* pB = lds + L_K + (aAo ^ 64);
  u32 okv = O_KVT + cA * 4096 + (2040 - 32 - 64 * ti - 8 * q) * 2, oqt = O_QT + cA * 4096 + (64 * ti + 8 * q) * 2;
  LAS char* tw = lds + L_TZ + ch * TZ_C + (128 * par2 + l) * 16 + 32;
  u32 og = (l & 31) == 31 ? O_ST1 + (srow + (l >> 5)) * 8 : 0xffffff00u;
  u32 oci = (x == 0 && y < 8 && w == 0 && l < 8) ? O_C2 + l * 2048 + y * 256 : 0xffffff00u;
  asm volatile("" : "+v"(oci), "+v"(w1d[0]), "+v"(w1d[1]), "+v"(w1d[2]), "+v"(w1d[3]), "+v"(pA), "+v"(pB), "+v"(okv), "+v"(oqt), "+v"(tw), "+v"(og), "+v"(wrv), "+v"(pW));
  const int wU = b < 224 ? 2 * b + ch : 0;
  u32 ow[4], w2o = b < 224 ? O_W2T + (u32)(((wU & 7) * 56 + (wU >> 3)) * 4096 + par2 * 2048) + l16 : 0xffff0000u;
  {
    const int UU = 4 * (wU >> 3) + q, f = UU / 7, u = UU - 7 * f;
    const int col = 64 * (wU & 7) + 8 * rr + 4 * (p4 & 1) + 32 * (p4 >> 1) + 2 * par2;
#pragma unroll
    for (int d = 0; d < 4; ++d) ow[d] = (u32)(((56 * f + (u < 4 ? 2 * u + 8 * d : 8 * u + 2 * d)) * D + col) * 2);
  }
  asm volatile("" : "+v"(w2o), "+v"(ow[0]), "+v"(ow[1]), "+v"(ow[2]), "+v"(ow[3]));
  if (w == 0) *(LAS v4u*)(lds + L_XR + (l >> 5) * XR_S + 4096 + (l & 31) * 16) = (v4u){0, 0, 0, 0};
  SB();
  STAMP(23);
  asm volatile("s_waitcnt vmcnt(9)" : "+v"(shk), "+v"(pfd)::"memory");
  BAR();
  STAMP(1);
  u32 tag, dpair;
  {
    v2u ka[2], kb[2], kc[2], va[2], vb[2];
#pragma unroll
    for (int it = 0; it < 2; ++it) { ka[it] = trd(pA, it * 4096 + 512); kb[it] = trd(pB, it * 4096 + 1024); kc[it] = trd(pB, it * 4096 + 1536); }
#pragma unroll
    for (int it = 0; it < 2; ++it) { va[it] = trd(pA, L_V - L_K + it * 4096); vb[it] = trd(pA, L_V - L_K + it * 4096 + 512); }
    SB();
#pragma unroll
    for (int j = 0; j < 4; ++j) dma<false>(lds0 + (8 * ch + 4 * par2) * WR_S, L_WR + j * WR_S, wrv, rW, (8 * rb + 4 * par2) * (F * 2) + fg * 896, j * (F * 2));
    SB();
    auto LO = [](u32 a, u32 c) { return (f32x2){lo2f(a), lo2f(c)}; };
    auto HI = [](u32 a, u32 c) { return (f32x2){hi2f(a), hi2f(c)}; };
    const f32x2 w0 = LO(shk.x, shk.x), w1 = HI(shk.x, shk.x), w2 = LO(shk.y, shk.y), w3 = HI(shk.y, shk.y);
    const f32x2 e[12] = {w0, HI(ka[0].x, ka[1].x), LO(ka[0].y, ka[1].y), HI(ka[0].y, ka[1].y), LO(kb[0].x, kb[1].x), HI(kb[0].x, kb[1].x), LO(kb[0].y, kb[1].y),
                         HI(kb[0].y, kb[1].y), LO(kc[0].x, kc[1].x), HI(kc[0].x, kc[1].x), LO(kc[0].y, kc[1].y), HI(kc[0].y, kc[1].y)};
    const f32x2 vv[8] = {LO(va[0].x, va[1].x), HI(va[0].x, va[1].x), LO(va[0].y, va[1].y), HI(va[0].y, va[1].y), LO(vb[0].x, vb[1].x), HI(vb[0].x, vb[1].x), LO(vb[0].y, vb[1].y), HI(vb[0].y, vb[1].y)};
    f32x2 kv[8];
#pragma unroll
    for (int s = 0; s < 8; ++s) kv[s] = (w3 * e[s + 1] + w2 * e[s + 2] + w1 * e[s + 3] + w0 * e[s + 4]) * vv[s];
#pragma unroll
    for (int it = 0; it < 2; ++it)
      __builtin_amdgcn_raw_buffer_store_b128((v4u){pk(kv[7][it], kv[6][it]), pk(kv[5][it], kv[4][it]), pk(kv[3][it], kv[2][it]), pk(kv[1][it], kv[0][it])}, WS, okv + (1 - it) * 64, 0, SC1);
    SB();
    asm volatile("s_waitcnt vmcnt(8)" : "+v"(kz[0]), "+v"(kz[1]), "+v"(kedge), "+v"(vtag), "+v"(vdp)::"memory");
    BAR();
    vtag += 1u;
    tag = __builtin_amdgcn_readfirstlane(vtag); dpair = __builtin_amdgcn_readfirstlane(vdp);
    v2u qa[2], qb[2];
#pragma unroll
    for (int it = 0; it < 2; ++it) { qa[it] = trd(pA, L_Q - L_K + it * 4096); qb[it] = trd(pA, L_Q - L_K + it * 4096 + 512); }
#pragma unroll
    for (int it = 0; it < 2; ++it) __builtin_amdgcn_raw_buffer_store_b128((v4u){qa[it].x, qa[it].y, qb[it].x, qb[it].y}, WS, oqt + it * 64, 0, SC1);
    __builtin_amdgcn_raw_buffer_store_b32(tag * 64u, WS, oci, 0, SC1);
  }
  SB();
  STAMP(24);
  {
    auto put = [&](LAS char* p, v4u d, u32 d4, u32 d5) {
      const u32 e0 = __builtin_amdgcn_alignbit(d.y, d.x, 16), e1 = __builtin_amdgcn_alignbit(d.z, d.y, 16), e2 = __builtin_amdgcn_alignbit(d.w, d.z, 16),
                e3 = __builtin_amdgcn_alignbit(d4, d.w, 16), e4 = __builtin_amdgcn_alignbit(d5, d4, 16);
      *(LAS v4u*)p = d; *(LAS v4u*)(p + TZ_S) = (v4u){e0, e1, e2, e3}; *(LAS v4u*)(p + 2 * TZ_S) = (v4u){d.y, d.z, d.w, d4}; *(LAS v4u*)(p + 3 * TZ_S) = (v4u){e1, e2, e3, e4};
    };
    auto nxt = [](u32 last, u32 v) { return (u32)__builtin_amdgcn_update_dpp((int)last, (int)v, 0x130, 0xf, 0xf, false); };
    put(tw, kz[0], nxt(__builtin_amdgcn_readlane(kz[1].x, 0), kz[0].x), nxt(__builtin_amdgcn_readlane(kz[1].y, 0), kz[0].y));
    put(tw + 1024, kz[1], nxt(par2 ? 0u : kedge.x, kz[1].x), nxt(par2 ? 0u : kedge.y, kz[1].y));
    const u32 f0 = __builtin_amdgcn_readlane(kz[0].x, 0), f1 = __builtin_amdgcn_readlane(kz[0].y, 0);
    if (l < 2 && !par2) put(tw - 32, (v4u){0, 0, 0, 0}, l ? f0 : 0u, l ? f1 : 0u);
  }
  SB();
  v2u st1;
  asm volatile("s_waitcnt vmcnt(9)" : "+v"(sx0), "+v"(sx1)::"memory");
  {
    const f32x2 v[8] = {{lo2f(sx0.x), hi2f(sx0.x)}, {lo2f(sx0.y), hi2f(sx0.y)}, {lo2f(sx0.z), hi2f(sx0.z)}, {lo2f(sx0.w), hi2f(sx0.w)},
                        {lo2f(sx1.x), hi2f(sx1.x)}, {lo2f(sx1.y), hi2f(sx1.y)}, {lo2f(sx1.z), hi2f(sx1.z)}, {lo2f(sx1.w), hi2f(sx1.w)}};
    const f32x2 s2 = ((v[0] + v[1]) + (v[2] + v[3])) + ((v[4] + v[5]) + (v[6] + v[7]));
    const float sm = hsum(s2[0] + s2[1]);
    const float m0 = u2f(__builtin_amdgcn_readlane(f2u(sm), 31)), m1 = u2f(__builtin_amdgcn_readlane(f2u(sm), 63));
    const float mean = (l < 32 ? m0 : m1) * (1.f / D);
    f32x2 ss = {0.f, 0.f};
#pragma unroll
    for (int j = 0; j < 8; ++j) { const f32x2 d = v[j] - mean; ss += d * d; }
    const float var = hsum(ss[0] + ss[1]) * (1.f / D);
    st1 = (v2u){pk(mean, __builtin_amdgcn_sqrtf(rbf(rbf(var) + 1e-5f))), vtag};
    __builtin_amdgcn_raw_buffer_store_b64(st1, WS, og, 0, SC1);
  }
  SB();
  WAITV(6);
  BAR();
  {
    v2u ta[4], tc[4];
#pragma unroll
    for (int j = 0; j < 4; ++j) { ta[j] = trd(pW, j * 256); tc[j] = trd(pW, j * 256 + 4 * WR_S); }
#pragma unroll
    for (int j = 0; j < 4; ++j) __builtin_amdgcn_raw_buffer_store_b128((v4u){ta[j].x, ta[j].y, tc[j].x, tc[j].y}, WS, w1d[j], 0, SC1);
  }
  SB();
  WAITV(0);
  BAR();
  STAMP(2);
  if (w == 0 && l == 0) __builtin_amdgcn_raw_buffer_store_b32(tag, WS, O_F1 + b * 128, 0, SC1);
  u32 w2raw[8];
  asm volatile("global_load_dword %0, %8, %12\n\tglobal_load_dword %1, %8, %12 offset:1024\n\tglobal_load_dword %2, %9, %12\n\tglobal_load_dword %3, %9, %12 offset:1024\n\t"
               "global_load_dword %4, %10, %12\n\tglobal_load_dword %5, %10, %12 offset:1024\n\tglobal_load_dword %6, %11, %12\n\tglobal_load_dword %7, %11, %12 offset:1024"
               : "=&v"(w2raw[0]), "=&v"(w2raw[1]), "=&v"(w2raw[2]), "=&v"(w2raw[3]), "=&v"(w2raw[4]), "=&v"(w2raw[5]), "=&v"(w2raw[6]), "=&v"(w2raw[7])
               : "v"(ow[0]), "v"(ow[1]), "v"(ow[2]), "v"(ow[3]), "s"(A.wd) : "memory");
  v4u T[32];
  {
    const u32 tb = lds0 + L_TZ + ch * TZ_C + ((n16 + 1) & 3) * TZ_S + (u32)((((n16 + 1) >> 2) + 2 * q + 8 * par2) * 8);
#pragma unroll
    for (int i = 0; i < 32; ++i) {
      v2u a, c;
      asm volatile("ds_read_b64 %0, %2 offset:%3\n\tds_read_b64 %1, %2 offset:%4" : "=&v"(a), "=&v"(c) : "v"(tb), "i"(128 * i), "i"(128 * i + 8) : "memory");
      T[i] = (v4u){a.x, a.y, c.x, c.y};
    }
    asm volatile("s_waitcnt lgkmcnt(0)" : "+v"(T[0]), "+v"(T[1]), "+v"(T[2]), "+v"(T[3]), "+v"(T[4]), "+v"(T[5]), "+v"(T[6]), "+v"(T[7]), "+v"(T[8]), "+v"(T[9]), "+v"(T[10]),
                 "+v"(T[11]), "+v"(T[12]), "+v"(T[13]), "+v"(T[14]), "+v"(T[15])::"memory");
    asm volatile("" : "+v"(T[16]), "+v"(T[17]), "+v"(T[18]), "+v"(T[19]), "+v"(T[20]), "+v"(T[21]), "+v"(T[22]), "+v"(T[23]), "+v"(T[24]), "+v"(T[25]), "+v"(T[26]),
                 "+v"(T[27]), "+v"(T[28]), "+v"(T[29]), "+v"(T[30]), "+v"(T[31])::"memory");
  }
  const int tB = 512 * par2 + 16 * n16 + 4 * q;
  LAS char* xp = lds + L_XR + ch * XR_S + (2032 - 16 * n16 + 32 * par2 + 8 * q) * 2 - 3584;
  LAS char* kp = lds + L_XR + ch * XR_S + (2044 - 1280 - tB) * 2;
  LAS char* gp = lds + L_S1 + tB * 8;
  LAS char* ep = lds + L_EX + w * 4096 + l * 16;
  LAS char* er = lds + L_EX + (w ^ 1) * 4096 + l * 16;
  u32 qo = O_QT + cB * 4096 + tB * 2;
  u32 oh = O_HT + (u32)((8 * (2 * par2 + (q & 1)) + (n16 >> 1)) * 32768 + (cB >> 5) * 2048 + (cB & 31) * 64 + (((2 * (n16 & 1) + (q >> 1)) ^ (2 * ((cB >> 3) & 1))) << 4));
  asm volatile("" : "+v"(xp), "+v"(kp), "+v"(gp), "+v"(ep), "+v"(er), "+v"(qo), "+v"(oh), "+v"(l16));
  const int hp = 4 * (x >> 2) + (y >> 3), fch = 8 * (x & 3) + (y & 7);
  const int ct = y & 7, rbl = y >> 3;
  const u32 ring = L_R + w * 12288;
  const int rot = fch & 3, spar = (fch >> 2) & 1;
  u32 w2v = O_W2T + (u32)((ct * 56 + 14 * w) * 4096) + l16;
  u32 ost = (fch < 8 && rr == q) ? O_ST2 + (u32)(rot * 32768 + (128 * hp + 32 * w + 16 * spar + n16) * 16) : 0xffff0000u;
  u32 og2 = O_ST2 + (u32)((l >> 5) * 65536 + (128 * hp + 32 * w + (l & 31)) * 16);
  u32 oact = O_ACT + (u32)(((8 * hp + 2 * w) * 224 + 7 * fch + q) * 256 + n16 * 16), oact2 = q < 3 ? oact + 1024 : 0xff000000u;
  u32 of3 = O_F3 + (u32)(((x * 4 + rbl) * 4 + w) * 128 + (l & 7) * 4), of3w = l == 0 ? O_F3 + (u32)(((hp * 4 + w) * 4 + (fch >> 3)) * 128 + (fch & 7) * 4) : 0xffff0000u;
  u32 av = O_ACT + (u32)(((8 * x + 2 * rbl) * 224 + 56 * w) * 256) + l16;
  LAS char* hx[2];
#pragma unroll
  for (int pr = 0; pr < 2; ++pr) hx[pr] = lds + ring + (8 * q + rr) * 64 + (((2 * (pr ^ spar) + (p4 >> 1)) ^ (2 * (q & 1))) << 4) + (p4 & 1) * 8;
  LAS char* pcs = lds + ring + 10240 + (n16 == 0 ? q * 16 : 128 + l * 16);
  LAS char* pcr = lds + L_R + 10240 + q * 16;
  asm volatile("" : "+v"(av), "+v"(pcs), "+v"(pcr), "+v"(w2v), "+v"(ost), "+v"(og2), "+v"(oact), "+v"(oact2), "+v"(of3), "+v"(of3w), "+v"(hx[0]), "+v"(hx[1]));
  u32 of1 = O_F1 + (u32)((l & 7) + 8 * ((x & 3) + 4 * (l >> 3))) * 128, of2 = O_F2 + (u32)(64 * w + l) * 128, of2w = (w == 0 && l == 0) ? O_F2 + (u32)b * 128 : 0xffff0000u;
  asm volatile("" : "+v"(of1), "+v"(of2), "+v"(of2w));
  STAMP(21);
  if (w == 0) spin(of1, tag);
  BAR();
  STAMP(3);

  dma<true>(lds0 + ch * XR_S + par2 * 2048, L_XR, l16, rWS, cB * 4096 + par2 * 2048, O_KVT);
  dma<true>(lds0 + ch * XR_S + par2 * 2048, L_XR + 1024, l16, rWS, cB * 4096 + par2 * 2048, O_KVT + 1024);
#pragma unroll
  for (int i = 0; i < 4; ++i) dma<true>(lds0 + w * 4096, L_S1 + i * 1024, l16, rWS, w * 4096, O_ST1 + i * 1024);
  v2u xq[4];
  asm volatile("buffer_load_dwordx2 %0, %4, %5, 0 offen sc1\n\tbuffer_load_dwordx2 %1, %4, %5, 0 offen offset:512 sc1\n\t"
               "buffer_load_dwordx2 %2, %4, %5, 0 offen offset:2048 sc1\n\tbuffer_load_dwordx2 %3, %4, %5, 0 offen offset:2560 sc1"
               : "=&v"(xq[0]), "=&v"(xq[1]), "=&v"(xq[2]), "=&v"(xq[3]) : "v"(qo), "s"(rWS) : "memory");
  SB();
  asm volatile("s_waitcnt vmcnt(8)" : "+v"(w2raw[0]), "+v"(w2raw[1]), "+v"(w2raw[2]), "+v"(w2raw[3]), "+v"(w2raw[4]), "+v"(w2raw[5]), "+v"(w2raw[6]), "+v"(w2raw[7])::"memory");
  BAR();
  STAMP(4);
  GAS u32* pc2 = (GAS u32*)(A.ws + O_C2 + (l & 7) * 2048 + x * 256);
  const u32 oc2 = O_C2 + x * 2048 + (l & 7) * 256, c2want = tag * 64u + 32u;
  v4u W2[56];
  const int r2 = R2A * rbl;
  auto kpos = [&](int k) { return (u32)(k + r2 >= 14 ? k + r2 - 14 : k + r2); };
  auto w2l = [&](int i) { W2[i] = __builtin_amdgcn_raw_buffer_load_b128(WS, w2v + (i & 3) * 1024, kpos(i >> 2) * 4096, SC1); };
  u32 hpk[4];
  auto put_h = [&](int h) {
    const auto s0 = __builtin_amdgcn_permlane16_swap(hpk[0], hpk[2], false, false);
    const auto s1 = __builtin_amdgcn_permlane16_swap(hpk[1], hpk[3], false, false);
    __builtin_amdgcn_raw_buffer_store_b128((v4u){s0[0], s1[0], s0[1], s1[1]}, WS, oh + h * (32 * 32768), 0, SC1);
  };
  {
    u32 w1so;
    auto w1l = [&](int i) {
      if ((i & 3) == 0) asm volatile("s_add_u32 m0, %1, %2\n\ts_add_u32 %0, %3, %4\n\tbuffer_load_dwordx4 %5, %6, %0 offen sc1 lds"
                                     : "=&s"(w1so) : "s"(lds0 + w * 28672), "i"(i * 1024), "s"(fch * 114688 + w * 28672), "i"(O_W1F + i * 1024), "v"(l16), "s"(rWS) : "memory", "scc");
      else asm volatile("buffer_load_dwordx4 %0, %1, %2 offen offset:%3 sc1 lds" ::"v"(l16), "s"(rWS), "s"(w1so), "i"((i & 3) * 1024) : "memory");
    };
    const float dsc = ch ? hi2f(dpair) : lo2f(dpair);
    f32x4 cacc[8], mn[2], ot[2];
    v4u B[32], g1[8], w2f[2]; v2u kr[4];
    float xf[16], ln[16], kd[16], tm[16], hh[8];
    auto fix = [&](int h) {
      u32 df = 0;
#pragma unroll
      for (int i = 4 * h; i < 4 * h + 4; ++i) df |= (g1[i].y ^ vtag) | (g1[i].w ^ vtag);
      if (__builtin_amdgcn_ballot_w64(df != 0)) {
        bool ok;
        for (u32 sp = 0; sp < SPIN_CAP; ++sp) {
          asm volatile("" ::: "memory");
          ok = true;
#pragma unroll
          for (int i = 4 * h; i < 4 * h + 4; ++i) {
            g1[i] = __builtin_amdgcn_raw_buffer_load_b128(WS, O_ST1 + (tB + 256 * ((i >> 1) & 1) + 1024 * h) * 8 + (i & 1) * 16, 0, SC1);
            ok &= (g1[i].y == tag) & (g1[i].w == tag);
          }
          if (__all(ok)) break;
        }
#pragma unroll
        for (int j = 8 * h; j < 8 * h + 8; ++j) { const u32 pay = g1[j >> 1][(j & 1) * 2]; ln[j] = (xf[j] - lo2f(pay)) * __builtin_amdgcn_rcpf(hi2f(pay)); }
      }
    };
    auto give = [&](int h) {
      if (par2) { *(LAS f32x4*)(ep + 2048 * h) = cacc[4 * h]; *(LAS f32x4*)(ep + 2048 * h + 1024) = cacc[4 * h + 1]; mn[0] = cacc[4 * h + 2]; mn[1] = cacc[4 * h + 3]; asm volatile("" : "+v"(mn[0]), "+v"(mn[1])); }
      else { *(LAS f32x4*)(ep + 2048 * h) = cacc[4 * h + 2]; *(LAS f32x4*)(ep + 2048 * h + 1024) = cacc[4 * h + 3]; mn[0] = cacc[4 * h]; mn[1] = cacc[4 * h + 1]; asm volatile("" : "+v"(mn[0]), "+v"(mn[1])); }
    };
    auto pre = [&](auto J_, auto P_) {
      constexpr int j = decltype(J_)::value, ph = decltype(P_)::value;
      const u32 pay = g1[j >> 1][(j & 1) * 2];
      if constexpr (ph == 0) { xf[j] = (j & 1) ? hi2f(xq[j >> 2][(j & 3) >> 1]) : lo2f(xq[j >> 2][(j & 3) >> 1]); asm volatile("" : "+v"(xf[j])); }
      if constexpr (ph == 1) { tm[j] = __builtin_amdgcn_rcpf(hi2f(pay)); asm volatile("" : "+v"(tm[j])); }
      if constexpr (ph == 2) { ln[j] = xf[j] - lo2f(pay); asm volatile("" : "+v"(ln[j])); }
      if constexpr (ph == 3) { ln[j] *= tm[j]; asm volatile("" : "+v"(ln[j])); }
      if constexpr (ph == 4) { const u32 kk = kr[j >> 2][1 - ((j & 3) >> 1)]; kd[j] = (j & 1) ? lo2f(kk) : hi2f(kk); asm volatile("" : "+v"(kd[j])); }
    };
    sfor<0, 32>([&](auto U_) { constexpr int u = decltype(U_)::value; if constexpr (cv_slot(u) < 0) B[u] = *(LAS v4u*)(xp + 128 * (31 - u)); });
    SB();
    sfor<0, 144>([&](auto N_) {
      constexpr int n = decltype(N_)::value, u = cv_u(n), i = u < 16 ? u : u - 16, a = cv_a0(u) + n - cv_first(u), eps = 4 * a - i + 3;
      mfb<i == 0>(cacc[a], T[eps], B[i]);
      SB();
      constexpr int ub = cv_at(n);
      if constexpr (ub >= 0) B[ub] = *(LAS v4u*)(xp + 128 * (31 - ub));
      if constexpr (w1_upto(n) > w1_upto(n - 1)) w1l(w1_upto(n) - 1);
      if constexpr (n < 4) {
        constexpr int pp = n / 2, d = 2 * (n & 1);
        w2f[pp][d] = __builtin_amdgcn_perm(w2raw[2 * d + 1], w2raw[2 * d], pp ? 0x07060302u : 0x05040100u);
        w2f[pp][d + 1] = __builtin_amdgcn_perm(w2raw[2 * d + 3], w2raw[2 * d + 2], pp ? 0x07060302u : 0x05040100u);
        if constexpr (n & 1) __builtin_amdgcn_raw_buffer_store_b128(w2f[pp], WS, w2o + pp * 1024, 0, SC1); else asm volatile("" : "+v"(w2f[pp]));
      }
      if constexpr (n == 4 || n == 6) kr[(n - 4) / 2] = *(LAS v2u*)(kp + 2560 - 512 * ((n - 4) / 2));
      if constexpr (n >= 23 && n < 39 && n % 2 == 1) pre(IC<(n - 23) / 2>{}, IC<4>{});
      if constexpr (n == 44) give(0);
      if constexpr (n == N0) {
        constexpr int nw = 2 + w1_upto(N0);
        asm volatile("s_waitcnt vmcnt(%4) lgkmcnt(0)" : "+v"(xq[0]), "+v"(xq[1]), "+v"(xq[2]), "+v"(xq[3]) : "n"(nw) : "memory");
        __builtin_amdgcn_s_barrier();
        asm volatile("" ::: "memory");
      }
      if constexpr (n == N0 + 1 || n == N0 + 2) { constexpr int k = n - N0 - 1; g1[2 * k] = *(LAS v4u*)(gp + k * 2048); g1[2 * k + 1] = *(LAS v4u*)(gp + k * 2048 + 16); ot[k] = *(LAS f32x4*)(er + k * 1024); }
      if constexpr (n == N0 + 3 || n == N0 + 4) { constexpr int k = 4 * (n - N0 - 3); pre(IC<k>{}, IC<0>{}); pre(IC<k + 1>{}, IC<0>{}); pre(IC<k + 2>{}, IC<0>{}); pre(IC<k + 3>{}, IC<0>{}); }
      if constexpr (n >= N0 + 5 && n < N0 + 13) {
        constexpr int j = n - N0 - 5;
        pre(IC<j>{}, IC<1>{}); pre(IC<j>{}, IC<2>{});
        if constexpr (j > 0) pre(IC<j - 1>{}, IC<3>{});
      }
      if constexpr (n == N0 + 13) { pre(IC<7>{}, IC<3>{}); fix(0); }
      if constexpr (n >= N0 + 14 && n < N0 + 18) {
#pragma unroll
        for (int j = 2 * (n - N0 - 14); j < 2 * (n - N0 - 14) + 2; ++j) { hh[j] = xf[j] + ln[j] * ((kd[j] * dsc + mn[j >> 2][j & 3]) + ot[j >> 2][j & 3]); asm volatile("" : "+v"(hh[j])); }
      }
      if constexpr (n == N0 + 18 || n == N0 + 19) { constexpr int k = n - N0 - 18; hpk[2 * k] = pk(hh[4 * k], hh[4 * k + 1]); hpk[2 * k + 1] = pk(hh[4 * k + 2], hh[4 * k + 3]); asm volatile("" : "+v"(hpk[2 * k]), "+v"(hpk[2 * k + 1])); }
      if constexpr (n == N0 + 20) put_h(0);
      if constexpr (n == 70 || n == 74) kr[2 + (n - 70) / 4] = *(LAS v2u*)(kp + 512 - 512 * ((n - 70) / 4));
      if constexpr (n >= 78 && n < 94 && n % 4 == 2) { constexpr int j = 4 + (n - 78) / 4; g1[j] = *(LAS v4u*)(gp + 8192 + ((j >> 1) & 1) * 2048 + (j & 1) * 16); }
      if constexpr (n == SIGA) {
        constexpr int nw = w1_upto(SIGA) - w1_upto(N0 + 20);
        WAITV(nw);
        __builtin_amdgcn_s_barrier();
        __builtin_amdgcn_raw_buffer_store_b32(vtag, WS, of2w, 0, SC1);
      }
      if constexpr (n >= 96 && n < 136) pre(IC<8 + (n - 96) / 5>{}, IC<(n - 96) % 5>{});
      SB();
    });
    asm volatile("s_nop 7" ::: "memory");
    SB();
    STAMP(5);
    sfor<w1_upto(143), 28>([&](auto I_) { w1l(decltype(I_)::value); });
    give(1);
    fix(1);
    BAR();
#pragma unroll
    for (int aa = 0; aa < 2; ++aa) {
      const f32x4 o = *(LAS f32x4*)(er + 2048 + aa * 1024);
#pragma unroll
      for (int i = 0; i < 4; ++i) hh[4 * aa + i] = xf[8 + 4 * aa + i] + ln[8 + 4 * aa + i] * ((kd[8 + 4 * aa + i] * dsc + mn[aa][i]) + o[i]);
      hpk[2 * aa] = pk(hh[4 * aa], hh[4 * aa + 1]); hpk[2 * aa + 1] = pk(hh[4 * aa + 2], hh[4 * aa + 3]);
    }
    put_h(1);
    SB();
    STAMP(6);
  }

  v4u ones = {0x3f803f80u, 0x3f803f80u, 0x3f803f80u, 0x3f803f80u};
  LAS char* wp0 = lds + l * 16;
  asm volatile("" : "+v"(ones), "+v"(wp0));
  u32 sg[4];
#pragma unroll
  for (int g = 0; g < 4; ++g) sg[g] = (u32)((g + rot) & 3);
  LAS char* wp = wp0 + sg[0] * 28672;
  v4u wf[WD + 1], hf[2][2], g2[2];
  WAITV(1);
  BAR();
  STAMP(7);
  u32 fl2;
  asm volatile("buffer_load_dword %0, %1, %2, 0 offen sc1" : "=&v"(fl2) : "v"(of2), "s"(rWS) : "memory");
  {
    LAS char* c0 = wp0 + w * 1024;
    LAS char* c1 = wp0 + (w == 3 ? 3 : w + 4) * 1024;
    LAS char* c2 = c0 + 57344;
    LAS char* c3 = c1 + 57344;
    asm volatile("" : "+v"(c0), "+v"(c1), "+v"(c2), "+v"(c3));
    auto crd = [&](int k, int hi) { return *(LAS v4u*)((k < 8 ? (hi ? c1 : c0) : (hi ? c3 : c2)) + (k & 7) * 7168); };
    v4u fa[16], fb[16]; f32x4 ca, cb;
#pragma unroll
    for (int k = 0; k < 4; ++k) { fa[k] = crd(k, 0); fb[k] = crd(k, 1); }
    SB();
    sfor<0, 16>([&](auto K_) {
      constexpr int k = decltype(K_)::value;
      mfv<k == 0>(ca, fa[k], ones); SB();
      if constexpr (k + 4 < 16) fa[k + 4] = crd(k + 4, 0);
      SB();
      mfv<k == 0>(cb, fb[k], ones); SB();
      if constexpr (k + 4 < 16) fb[k + 4] = crd(k + 4, 1);
      if constexpr (k >= 9) wf[k - 9] = *(LAS v4u*)(wp + (k - 9) * 1024);
      SB();
    });
    asm volatile("s_nop 7" ::: "memory");
    *(LAS f32x4*)pcs = ca; *(LAS f32x4*)(pcs + 64) = cb;
  }
  asm volatile("s_waitcnt vmcnt(0)" : "+v"(fl2)::"memory");
  if (!__all(fl2 == tag)) spin(of2, tag);
  BAR();
  STAMP(8);
  f32x4 cs[7];
  u32 fl3 = 0;
  v4u AC[28][2];
  const u32 ao[2] = {(u32)((w & 1) * (224 * 256)), (u32)(((w & 1) ^ 1) * (224 * 256))};
  auto ald = [&](int u) {
#pragma unroll
    for (int c = 0; c < 2; ++c) { AC[u][c] = __builtin_amdgcn_raw_buffer_load_b128(WS, R2A ? av : av + (u % 14 & 3) * 1024, ao[c] + (R2A ? kpos(u % 14) * 1024 : (u % 14 >> 2) * 4096) + (u / 14) * (64 * 224 * 256), SC1); SB(); }
  };
  {
    u32 hso;
    auto hdma = [&](int g, int n) {
      if (n == 0) asm volatile("s_add_u32 m0, %1, %2\n\ts_add_u32 %0, %3, %4\n\tbuffer_load_dwordx4 %5, %6, %0 offen sc1 lds"
                               : "=&s"(hso) : "s"(lds0 + ring), "i"((g % 5) * 2048), "s"((4 * hp + w) * 32768 + sg[(g >> 2) & 3] * 8192), "i"(O_HT + (g >> 4) * (32 * 32768) + (g & 3) * 2048), "v"(l16), "s"(rWS) : "memory", "scc");
      else asm volatile("buffer_load_dwordx4 %0, %1, %2 offen offset:1024 sc1 lds" ::"v"(l16), "s"(rWS), "s"(hso) : "memory");
    };
    auto hfrag = [&](int g, int pr, int t) { return trd(hx[pr], (g % 5) * 2048 + t * 256); };
    f32x4 acc[7][2], sS, sQ;
    float z0, z1, z2, sv[3][2];
    auto stat = [&](auto T_) {
      constexpr int t = decltype(T_)::value;
      auto both = [&](float v, float(&o)[2]) {
        const auto r = __builtin_amdgcn_permlane16_swap(f2u(v), f2u(v), false, false);
        o[0] = u2f(spar ? r[1] : r[0]); o[1] = u2f(spar ? r[0] : r[1]);
        asm volatile("" : "+v"(o[0]), "+v"(o[1]));
      };
      if constexpr (t == 0) { z0 = u2f(g2[0].x) + u2f(g2[1].x); asm volatile("" : "+v"(z0)); z1 = u2f(g2[0].z) + u2f(g2[1].z); asm volatile("" : "+v"(z1)); }
      if constexpr (t == 1) { const auto h = __builtin_amdgcn_permlane32_swap(f2u(z0), f2u(z0), false, false); z0 = (u2f(h[0]) + u2f(h[1])) * (-1.f / D); asm volatile("" : "+v"(z0)); }
      if constexpr (t == 2) { const auto h = __builtin_amdgcn_permlane32_swap(f2u(z1), f2u(z1), false, false); z1 = (u2f(h[0]) + u2f(h[1])) * (1.f / D); asm volatile("" : "+v"(z1)); }
      if constexpr (t == 3) { z1 = __builtin_fmaxf(z1 - z0 * z0, 0.f) + 1e-5f; z2 = -1.44269504089f * __builtin_amdgcn_rsqf(z1); asm volatile("" : "+v"(z1), "+v"(z2)); }
      if constexpr (t == 4) both(z0, sv[0]);
      if constexpr (t == 5) both(z2, sv[1]);
      if constexpr (t == 6) both(z1, sv[2]);
    };
    auto epilogue = [&](auto P_) {
      constexpr int ps = decltype(P_)::value;
      v4u sa[2], sb[2];
#pragma unroll
      for (int pr = 0; pr < 2; ++pr) {
        const float nm = sv[0][pr], k1 = sv[1][pr], vv = sv[2][pr];
        u32 d[8];
        f32x2 a[7], bb[7], e[7];
#pragma unroll
        for (int t = 0; t < 7; ++t) a[t] = (f32x2){acc[t][pr][0], acc[t][pr][1]} + nm * (f32x2){cs[t][0], cs[t][1]};
        SB();
#pragma unroll
        for (int t = 0; t < 7; ++t) e[t] = a[t] * k1;
        SB();
#pragma unroll
        for (int t = 0; t < 7; ++t) {
          e[t] = (f32x2){__builtin_amdgcn_exp2f(e[t][0]), __builtin_amdgcn_exp2f(e[t][1])};
          bb[t] = (f32x2){acc[t][pr][2], acc[t][pr][3]} + nm * (f32x2){cs[t][2], cs[t][3]};
          if (ps == 0) w2l(w2_first(16) - W2_EPI + 7 * pr + t);
          if (ps == 1 && (t == 1 || t == 4)) ald(8 + 2 * pr + t / 3);
          SB();
        }
#pragma unroll
        for (int t = 0; t < 7; ++t) e[t] = e[t] * vv + vv;
        SB();
#pragma unroll
        for (int t = 0; t < 7; ++t) { e[t] = (f32x2){__builtin_amdgcn_rcpf(e[t][0]), __builtin_amdgcn_rcpf(e[t][1])}; a[t] *= bb[t]; SB(); }
#pragma unroll
        for (int t = 0; t < 7; ++t) a[t] *= e[t];
        SB();
#pragma unroll
        for (int t = 0; t < 7; ++t) d[t] = pk(a[t][0], a[t][1]);
        sa[pr] = (v4u){d[0], d[1], d[2], d[3]};
        const auto x0 = __builtin_amdgcn_permlane32_swap(d[4], d[6], false, false), x1 = __builtin_amdgcn_permlane32_swap(d[5], d[5], false, false);
        const auto y0 = __builtin_amdgcn_permlane16_swap(x0[0], x1[0], false, false), y1 = __builtin_amdgcn_permlane16_swap(x0[1], x1[1], false, false);
        sb[pr] = (v4u){y0[0], y0[1], y1[0], y1[1]};
        asm volatile("" : "+v"(sa[pr]), "+v"(sb[pr]));
        SB();
      }
#pragma unroll
      for (int pr = 0; pr < 2; ++pr) {
        const u32 ro = (u32)((pr ^ spar) * (224 * 256) + ps * (64 * 224 * 256));
        __builtin_amdgcn_raw_buffer_store_b128(sa[pr], WS, oact + ro, 0, SC1);
        __builtin_amdgcn_raw_buffer_store_b128(sb[pr], WS, oact2 + ro, 0, SC1);
      }
      SB();
    };
    hdma(0, 0); hdma(0, 1); hdma(1, 0); hdma(1, 1);
#pragma unroll
    for (int g = 2; g < 5; ++g) { hdma(g, 0); hdma(g, 1); }
    if (w == 0) { if (l < 8) __hip_atomic_fetch_add(pc2, 1u, RLX); }
    else __builtin_amdgcn_raw_buffer_store_b32(vtag, WS, 0xffffff00u, 0, SC1);
#pragma unroll
    for (int k = 0; k < 7; ++k) cs[k] = *(LAS f32x4*)(pcr + (k & 3) * 12288 + (k >> 2) * 64);
    WAITV(9);
#pragma unroll
    for (int pr = 0; pr < 2; ++pr) { const v2u a = hfrag(0, pr, 0), c = hfrag(0, pr, 1); hf[0][pr] = (v4u){a.x, a.y, c.x, c.y}; }
    SB();
    STAMP(20);
    sfor<0, 32>([&](auto G_) {
      constexpr int g = decltype(G_)::value, s = g & 15;
      v4u(&hc)[2] = hf[g & 1]; v4u(&hn)[2] = hf[(g + 1) & 1];
      LAS char* wn = wp;
      if constexpr ((g & 3) == 3 && g < 31) wn = wp0 + sg[((g + 1) >> 2) & 3] * 28672;
      v2u ha;
      sfor<0, 7>([&](auto T_) {
        constexpr int t = decltype(T_)::value, it = g * 7 + t, nx = it + WD, ng = nx / 7, nt = nx % 7;
        if constexpr (t == 0) asm volatile("" : "+v"(wf[it % 8]), "+v"(wf[(it + 1) % 8]), "+v"(wf[(it + 2) % 8]), "+v"(wf[(it + 3) % 8]), "+v"(hc[0]), "+v"(hc[1]));
        if constexpr (t == 4) asm volatile("" : "+v"(wf[it % 8]), "+v"(wf[(it + 1) % 8]), "+v"(wf[(it + 2) % 8]));
        mfv<s == 0>(acc[t][0], wf[it % (WD + 1)], hc[0]);
        SB();
        if constexpr (nx < 224) wf[nx % (WD + 1)] = *(LAS v4u*)((ng > g ? wn : wp) + (ng & 3) * 7168 + nt * 1024);
        SB();
        mfv<s == 0>(acc[t][1], wf[it % (WD + 1)], hc[1]);
        SB();
        if constexpr (g < 31 && g != 15 && t < 4) {
          if constexpr (t == 0) { constexpr int hw = h_wait(g); WAITV(hw); }
          if constexpr (t & 1) { const v2u hb = hfrag(g + 1, t >> 1, 1); hn[t >> 1] = (v4u){ha.x, ha.y, hb.x, hb.y}; } else ha = hfrag(g + 1, t >> 1, 0);
        }
        if constexpr (t >= 4 && t - 4 < w2_n(g)) w2l(w2_first(g) + t - 4);
        if constexpr (g == GC && t == 5) {
          constexpr int nw = vm_sum(GS + 1, GC) + w2_n(GC);
          asm volatile("s_waitcnt vmcnt(%1)" : "+v"(fl2) : "n"(nw) : "memory");
          if (!__all(fl2 == c2want)) spin(oc2, c2want);
          sfor<16, GC + 5>([&](auto K_) { hdma(decltype(K_)::value, 0); hdma(decltype(K_)::value, 1); });
        }
        if constexpr (d_n(g) > 0 && t >= 5) hdma(g + 5, t - 5);
        if constexpr (g == 28 && t == 4) {
          constexpr int nw = vm_sum(26, 28);
          asm volatile("s_waitcnt vmcnt(%1)" : "+v"(fl3) : "n"(nw) : "memory");
          if (!__all(fl3 == tag)) spin(of3, tag);
        }
        if constexpr (g >= 28 && (t == 4 || t == 6)) ald(2 * (g - 28) + (t - 4) / 2);
        if constexpr (s == 15) stat(T_);
        SB();
      });
      if constexpr (s < 4) { mfv<s == 0>(sS, ones, hc[0]); mfv<s == 0>(sQ, hc[0], hc[0]); }
      if constexpr (s == 5) {
        float q01, q23, qq;
        asm volatile("v_cndmask_b32 %0, %3, %4, %7\n\tv_cndmask_b32 %1, %5, %6, %7\n\tv_cndmask_b32 %2, %0, %1, %8"
                     : "=&v"(q01), "=&v"(q23), "=&v"(qq) : "v"(sQ[0]), "v"(sQ[1]), "v"(sQ[2]), "v"(sQ[3]), "s"(0xaaaaaaaaaaaaaaaaull), "s"(0xccccccccccccccccull));
        __builtin_amdgcn_raw_buffer_store_b128((v4u){f2u(sS[0]), vtag, f2u(qq), vtag}, WS, ost + (g >> 4) * 16384, 0, SC1);
      }
      if constexpr (s == 11) {
        asm volatile("buffer_load_dwordx4 %0, %2, %3, %4 offen sc1\n\tbuffer_load_dwordx4 %1, %2, %3, %5 offen sc1" : "=&v"(g2[0]), "=&v"(g2[1])
                     : "v"(og2), "s"(rWS), "s"((g >> 4) * 16384), "s"(32768 + (g >> 4) * 16384) : "memory");
      }
      if constexpr (g == GS) asm volatile("buffer_load_dword %0, %1, %2, 0 offen sc1" : "=&v"(fl2) : "v"(oc2), "s"(rWS) : "memory");
      if constexpr (g == 19) {
        constexpr int nw = vm_sum(16, 19) + w2_n(19) + 2;
        WAITV(nw);
        __builtin_amdgcn_raw_buffer_store_b32(vtag, WS, of3w, 0, SC1);
      }
      if constexpr (g == 27) {
        dma<true>(lds0 + ring, 4096, l16, rWS, (32 * (w >> 1) + 4 * x + rbl) * 32768 + (2 * ct + (w & 1)) * 2048, O_HT);
        dma<true>(lds0 + ring, 5120, l16, rWS, (32 * (w >> 1) + 4 * x + rbl) * 32768 + (2 * ct + (w & 1)) * 2048, O_HT + 1024);
      }
      if constexpr (g == 25) asm volatile("buffer_load_dword %0, %1, %2, 0 offen sc1" : "=&v"(fl3) : "v"(of3), "s"(rWS) : "memory");
      if constexpr (s == 14) {
        constexpr int ps = g >> 4, nw = vm_sum(g - 2, g + 1);
        asm volatile("s_waitcnt vmcnt(%2)" : "+v"(g2[0]), "+v"(g2[1]) : "n"(nw) : "memory");
        if (!__all((g2[0].y == tag) & (g2[0].w == tag) & (g2[1].y == tag) & (g2[1].w == tag))) {
          for (u32 sp = 0; sp < SPIN_CAP; ++sp) {
            asm volatile("" ::: "memory");
#pragma unroll
            for (int k = 0; k < 2; ++k) g2[k] = __builtin_amdgcn_raw_buffer_load_b128(WS, og2, k * 32768 + ps * 16384, SC1);
            if (__all((g2[0].y == tag) & (g2[0].w == tag) & (g2[1].y == tag) & (g2[1].w == tag))) break;
          }
        }
      }
      SB();
      wp = wn;
      if constexpr ((g & 7) == 7) STAMP(16 + (g >> 3));
      if constexpr (s == 15) {
        asm volatile("s_nop 7" ::: "memory");
        SB();
        epilogue(IC<(g >> 4)>{});
        if constexpr (g == 15) {
          constexpr int hw = h_wait(15);
          WAITV(hw);
#pragma unroll
          for (int pr = 0; pr < 2; ++pr) { const v2u a = hfrag(16, pr, 0), c = hfrag(16, pr, 1); hf[0][pr] = (v4u){a.x, a.y, c.x, c.y}; }
          SB();
        }
        if constexpr (g == 15) STAMP(9);
      }
    });
  }
  STAMP(10);

  {
    v2u hr[4];
    LAS char* pw = lds + w * 4096 + l * 16;
    u32 f3b;
    f32x4 o[4][2], mine[4];
    SB();
    sfor<0, 28>([&](auto U_) {
      constexpr int u = decltype(U_)::value;
#pragma unroll
      for (int c = 0; c < 2; ++c)
#pragma unroll
        for (int pp = 0; pp < 4; ++pp) mfa<u % 14 == 0>(o[pp][c], W2[4 * (u % 14) + pp], AC[u][c]);
      SB();
      if constexpr (u < 4) {
#pragma unroll
        for (int i = 32 + 3 * u; i < 35 + 3 * u; ++i) { w2l(i); SB(); }
      }
      if constexpr (u == UFL) {
        STAMP(26);
        WAITV(UFL < 4 ? 3 * UFL + 3 : 12);
        STAMP(27);
        __builtin_amdgcn_raw_buffer_store_b32(vtag, WS, of3w + 16384, 0, SC1);
        SB();
      }
      if constexpr (u == 5) {
#pragma unroll
        for (int i = 44; i < 48; ++i) { w2l(i); SB(); }
      }
      if constexpr (u == 6) {
        ald(12);
#pragma unroll
        for (int i = 48; i < 52; ++i) { w2l(i); SB(); }
      }
      if constexpr (u == 7) {
        __builtin_amdgcn_s_barrier();
        asm volatile("" ::: "memory");
#pragma unroll
        for (int i = 0; i < 4; ++i)
          hr[i] = trd(lds, L_R + (u32)(((w & 2) + (i >> 1)) * 12288 + 4096 + (8 * q + 4 * (i & 1) + rr) * 64 + (((2 * (w & 1) + (p4 >> 1)) ^ (2 * (q & 1))) << 4) + (p4 & 1) * 8));
        SB();
      }
      if constexpr (u == UPOLL) STAMP(28);
      if constexpr (u == UCHK) STAMP(22);
      if constexpr (u == UPOLL) {
        f3b = __builtin_amdgcn_raw_buffer_load_b32(WS, of3 + 16384, 0, SC1);
        SB();
        ald(13);
#pragma unroll
        for (int i = 52; i < 56; ++i) { w2l(i); SB(); }
      }
      if constexpr (u == UCHK) {
        if (!__all(f3b == tag)) spin(of3 + 16384, tag);
        STAMP(12);
#pragma unroll
        for (int v = 14; v < 20; ++v) ald(v);
      }
      if constexpr (u > UCHK && u <= UCHK + 8) ald(u - UCHK + 19);
      if constexpr (u % 14 == 13) {
        asm volatile("s_nop 7" ::: "memory");
        SB();
        constexpr int hfb = 2 * (u / 14);
#pragma unroll
        for (int pp = 0; pp < 4; ++pp) *(LAS f32x4*)(pw + ((hfb + ((w & 1) ^ 1)) * 16384) + pp * 1024) = o[pp][1];
        if ((w & 2) == hfb) {
#pragma unroll
          for (int pp = 0; pp < 4; ++pp) { mine[pp] = o[pp][0]; asm volatile("" : "+v"(mine[pp])); }
        } else {
#pragma unroll
          for (int pp = 0; pp < 4; ++pp) *(LAS f32x4*)(pw + ((hfb + (w & 1)) * 16384) + pp * 1024) = o[pp][0];
        }
        if constexpr (u == 13) STAMP(11);
        SB();
      }
    });
    STAMP(13);
    BAR();
#pragma unroll
    for (int j = 1; j < 4; ++j)
#pragma unroll
      for (int pp = 0; pp < 4; ++pp) mine[pp] += *(LAS f32x4*)(lds + w * 16384 + ((w + j) & 3) * 4096 + pp * 1024 + l * 16);
    u32 r[8];
#pragma unroll
    for (int i = 0; i < 4; ++i) {
      r[2 * i] = pk(lo2f(hr[i].x) + mine[0][i], hi2f(hr[i].x) + mine[1][i]);
      r[2 * i + 1] = pk(lo2f(hr[i].y) + mine[2][i], hi2f(hr[i].y) + mine[3][i]);
    }
    const __amdgpu_buffer_rsrc_t OUT = __builtin_amdgcn_make_buffer_rsrc(A.out, 0, S * D * 2, 0x00020000);
    const u32 oo = (u32)(((1024 * (w >> 1) + 128 * x + 32 * rbl + 16 * (w & 1) + n16) * D + 64 * ct + 8 * q) * 2);
    __builtin_amdgcn_raw_buffer_store_b128((v4u){r[0], r[1], r[2], r[3]}, OUT, oo, 0, OAUX);
    __builtin_amdgcn_raw_buffer_store_b128((v4u){r[4], r[5], r[6], r[7]}, OUT, oo + 64, 0, OAUX);
    if (b == 0 && w == 0 && l == 0) __builtin_amdgcn_raw_buffer_store_b32(tag, WS, O_EP, 0, SC1);
  }
}
}

extern "C" void kernel_launch(void* const* d_in, const int* in_sizes, int n_in, void* d_out, int out_size, void* d_ws, size_t ws_size, hipStream_t stream) {
  fk::Args a;
  a.q = (const fk::u16*)d_in[0]; a.k = (const fk::u16*)d_in[1]; a.v = (const fk::u16*)d_in[2]; a.shk = (const fk::u16*)d_in[3];
  a.ssk = (const fk::u16*)d_in[4]; a.ssd = (const fk::u16*)d_in[5]; a.wg = (const fk::u16*)d_in[7]; a.wu = (const fk::u16*)d_in[8];
  a.wd = (const fk::u16*)d_in[9]; a.out = (fk::u16*)d_out; a.ws = (char*)d_ws;
  fk::fused<<<256, 256, 0, stream>>>(a);
}
```
